```python
import torch

def _softmax(x, axis=-1):
    return torch.softmax(x, dim=axis)

def _gelu(x):
    return torch.nn.functional.gelu(x, approximate='tanh')

def _silu(x):
    return torch.nn.functional.silu(x)

def _sigmoid(x):
    return torch.sigmoid(x)

def _layernorm(x, axis=-1, eps=1e-05):
    m = x.mean(dim=axis, keepdim=True)
    v = x.var(dim=axis, keepdim=True, unbiased=False)
    return (x - m) / torch.sqrt(v + eps)

def _scatter_add(tgt, idx, src, axis=0):
    return tgt.clone().index_add_(axis, idx, src)

def _segment_sum(src, seg, n):
    out = torch.zeros((n,) + src.shape[1:], dtype=src.dtype)
    return out.index_add_(0, seg, src)

def _pad(x, pw):
    flat = tuple((v for pair in reversed(pw) for v in pair))
    return torch.nn.functional.pad(x, flat)
kernel_name = 'standalone__dit_adaln_block__diffusion'

def setup_inputs():

    _w = lambda a: (lambda t: t.to(torch.bfloat16) if t.is_floating_point() else t)(torch.as_tensor(a))
    B, N, D = (2, 1024, 384)
    return {'x': _w((lambda *s: torch.randn(*s, dtype=torch.bfloat16) * 0.05)(B, N, D)), 'c': _w((lambda *s: torch.randn(*s, dtype=torch.bfloat16) * 0.2)(B, D)), 'wqkv': _w((lambda *s: torch.randn(*s, dtype=torch.bfloat16) * 0.05)(D, 3 * D)), 'wo': _w((lambda *s: torch.randn(*s, dtype=torch.bfloat16) * 0.1)(D, D)), 'w_mod': _w((lambda *s: torch.randn(*s, dtype=torch.bfloat16) * 0.2)(D, 6 * D))}

def reference(x, c, wqkv, wo, w_mod):
    B, N, D = x.shape
    mod = (_silu(c) @ w_mod).reshape(B, 6, 1, D)
    shift_a, scale_a, gate_a, shift_m, scale_m, gate_m = [mod[:, i] for i in range(6)]
    h = _layernorm(x, axis=-1) * (1 + scale_a) + shift_a
    qkv = h @ wqkv
    q, k, v = (qkv[..., :D].clone(), qkv[..., D:2 * D].clone(), qkv[..., 2 * D:].clone())
    p = _softmax(torch.einsum('bqd,bkd->bqk', q, k) * D ** (-0.5), axis=-1)
    a = torch.einsum('bqk,bkd->bqd', p, v) @ wo
    return x + gate_a * a
```

```cpp
#include <hip/hip_runtime.h>
#include <hip/hip_bf16.h>

typedef short bf16x8 __attribute__((ext_vector_type(8)));
typedef short s16x4 __attribute__((ext_vector_type(4)));
typedef float f32x16 __attribute__((ext_vector_type(16)));
typedef float f32x4 __attribute__((ext_vector_type(4)));
typedef unsigned v4u __attribute__((ext_vector_type(4)));
typedef unsigned v2u __attribute__((ext_vector_type(2)));
typedef unsigned short u16;
#define DI __device__ __forceinline__
#define LAS __attribute__((address_space(3)))
#define GAS __attribute__((address_space(1)))

constexpr int NB = 2, NSEQ = 1024, D = 384, NR = NB * NSEQ;
constexpr int NSPLIT = 8;
constexpr size_t WS_MOD = 0;
constexpr size_t WS_MT = 16384;
constexpr size_t WS_W2T = WS_MT + 294912;
constexpr size_t WS_N = WS_W2T + 294912;
constexpr size_t WS_ML = WS_N + (size_t)NR * D * 2;
constexpr size_t WS_OP = WS_ML + (size_t)NSPLIT * NR * 8;

DI float bf2f(u16 v) { return __uint_as_float((unsigned)v << 16); }
DI float bflo(unsigned v) { return __uint_as_float(v << 16); }
DI float bfhi(unsigned v) { return __uint_as_float(v & 0xffff0000u); }
DI unsigned cvtpk(float lo, float hi) {
  typedef __bf16 bf2 __attribute__((ext_vector_type(2)));
  typedef float f2 __attribute__((ext_vector_type(2)));
  f2 x = {lo, hi};
  bf2 r = __builtin_convertvector(x, bf2);
  return __builtin_bit_cast(unsigned, r);
}
DI float bfround(float x) { return bflo(cvtpk(x, 0.f)); }
DI f32x16 mfma32(v4u a, v4u b, f32x16 c) {
  return __builtin_amdgcn_mfma_f32_32x32x16_bf16(__builtin_bit_cast(bf16x8, a), __builtin_bit_cast(bf16x8, b), c, 0, 0, 0);
}

constexpr int K1_MOD = 36, K1_M = 144, K1_W2 = 144, K1_LN = 128;
__global__ __launch_bounds__(256) void k1_prep(const u16* __restrict__ x, const u16* __restrict__ c, const u16* __restrict__ wqkv,
                                               const u16* __restrict__ wo, const u16* __restrict__ wmod, unsigned char* __restrict__ ws) {
  __shared__ __attribute__((aligned(16))) float red[4 * 64 * 16];
  const int tid = threadIdx.x, lane = tid & 63, w = __builtin_amdgcn_readfirstlane(tid >> 6);
  int bid = blockIdx.x;
  if (bid < K1_MOD) {
    const int cg = tid & 3, rg = tid >> 2, col0 = bid * 32;
    v4u wv[6];
#pragma unroll
    for (int i = 0; i < 6; ++i) wv[i] = *(const v4u*)(wmod + (size_t)(rg * 6 + i) * 2304 + col0 + cg * 8);
    float acc[2][8];
#pragma unroll
    for (int b = 0; b < 2; ++b)
#pragma unroll
      for (int e = 0; e < 8; ++e) acc[b][e] = 0.f;
#pragma unroll
    for (int i = 0; i < 6; ++i) {
#pragma unroll
      for (int b = 0; b < 2; ++b) {
        const float cv = bf2f(c[b * D + rg * 6 + i]);
        const float sv = bfround(cv / (1.f + __expf(-cv)));
#pragma unroll
        for (int e = 0; e < 4; ++e) {
          acc[b][2 * e] = fmaf(sv, bflo(wv[i][e]), acc[b][2 * e]);
          acc[b][2 * e + 1] = fmaf(sv, bfhi(wv[i][e]), acc[b][2 * e + 1]);
        }
      }
    }
#pragma unroll
    for (int b = 0; b < 2; ++b) {
      *(f32x4*)&red[rg * 64 + b * 32 + cg * 8] = f32x4{acc[b][0], acc[b][1], acc[b][2], acc[b][3]};
      *(f32x4*)&red[rg * 64 + b * 32 + cg * 8 + 4] = f32x4{acc[b][4], acc[b][5], acc[b][6], acc[b][7]};
    }
    __syncthreads();
    float s = 0.f;
#pragma unroll
    for (int i = 0; i < 16; ++i) s += red[(w * 16 + i) * 64 + lane];
    __syncthreads();
    red[w * 64 + lane] = s;
    __syncthreads();
    if (tid < 64) {
      float t = red[lane] + red[64 + lane] + red[128 + lane] + red[192 + lane];
      t = bfround(t);
      const int b = lane >> 5, col = col0 + (lane & 31);
      if (col >= 384 && col < 768) t = bfround(1.f + t);
      ((float*)(ws + WS_MOD))[b * 1152 + col] = t;
    }
    return;
  }
  bid -= K1_MOD;
  if (bid < K1_M + K1_W2) {
    const bool isM = bid < K1_M;
    if (!isM) bid -= K1_M;
    const int rt = bid / 12, ct = bid % 12;
    const int r = lane & 31, hh = lane >> 5;
    v4u a[6], bq[6];
    const u16* ap = wqkv + (size_t)(32 * rt + r) * 1152 + (isM ? 0 : 768) + 96 * w + 8 * hh;
#pragma unroll
    for (int s = 0; s < 6; ++s) a[s] = *(const v4u*)(ap + 16 * s);
    if (isM) {
      const u16* bp = wqkv + (size_t)(32 * ct + r) * 1152 + 384 + 96 * w + 8 * hh;
#pragma unroll
      for (int s = 0; s < 6; ++s) bq[s] = *(const v4u*)(bp + 16 * s);
    } else {
      const u16* bp = wo + (size_t)(96 * w + 8 * hh) * 384 + 32 * ct + r;
#pragma unroll
      for (int s = 0; s < 6; ++s)
#pragma unroll
        for (int e = 0; e < 4; ++e)
          bq[s][e] = (unsigned)bp[(16 * s + 2 * e) * 384] | ((unsigned)bp[(16 * s + 2 * e + 1) * 384] << 16);
    }
    f32x16 acc = {};
#pragma unroll
    for (int s = 0; s < 6; ++s) acc = mfma32(a[s], bq[s], acc);
#pragma unroll
    for (int g = 0; g < 4; ++g) *(f32x4*)&red[((w * 4 + g) * 64 + lane) * 4] = f32x4{acc[4 * g], acc[4 * g + 1], acc[4 * g + 2], acc[4 * g + 3]};
    __syncthreads();
    f32x4 t = *(f32x4*)&red[((0 * 4 + w) * 64 + lane) * 4];
#pragma unroll
    for (int ww = 1; ww < 4; ++ww) t += *(f32x4*)&red[((ww * 4 + w) * 64 + lane) * 4];
    v2u o = {cvtpk(t[0], t[1]), cvtpk(t[2], t[3])};
    const int g = w;
    if (isM) {
      const size_t off = ((size_t)(ct * 24 + 2 * rt + (g >> 1)) * 64 + (r + 32 * (g & 1))) * 16 + 8 * hh;
      *(v2u*)(ws + WS_MT + off) = o;
    } else {
      const size_t off = ((size_t)(ct * 24 + 2 * rt + (g >> 1)) * 64 + lane) * 16 + 8 * (g & 1);
      *(v2u*)(ws + WS_W2T + off) = o;
    }
    return;
  }
  bid -= K1_M + K1_W2;
  {
    const int row = bid * 16 + (tid >> 4), l16 = tid & 15;
    const u16* xp = x + (size_t)row * D;
    v4u v[3];
#pragma unroll
    for (int i = 0; i < 3; ++i) v[i] = *(const v4u*)(xp + (l16 + 16 * i) * 8);
    float f[24];
#pragma unroll
    for (int i = 0; i < 3; ++i)
#pragma unroll
      for (int e = 0; e < 4; ++e) { f[i * 8 + 2 * e] = bflo(v[i][e]); f[i * 8 + 2 * e + 1] = bfhi(v[i][e]); }
    float s = 0.f;
#pragma unroll
    for (int i = 0; i < 24; ++i) s += f[i];
#pragma unroll
    for (int m = 1; m < 16; m <<= 1) s += __shfl_xor(s, m);
    const float mu = s * (1.f / D);
    float q = 0.f;
#pragma unroll
    for (int i = 0; i < 24; ++i) { f[i] -= mu; q = fmaf(f[i], f[i], q); }
#pragma unroll
    for (int m = 1; m < 16; m <<= 1) q += __shfl_xor(q, m);
    const float rs = rsqrtf(q * (1.f / D) + 1e-5f);
    u16* np = (u16*)(ws + WS_N) + (size_t)row * D;
#pragma unroll
    for (int i = 0; i < 3; ++i) {
      v4u o;
#pragma unroll
      for (int e = 0; e < 4; ++e) o[e] = cvtpk(f[i * 8 + 2 * e] * rs, f[i * 8 + 2 * e + 1] * rs);
      *(v4u*)(np + (l16 + 16 * i) * 8) = o;
    }
  }
}

constexpr int L_NK = 0;
constexpr int L_H = 98304;
constexpr int L_MOD = L_H + 50176;
constexpr int L_MX = L_MOD + 3072;
constexpr int L_K2 = L_MX + 1024;
DI unsigned koff(unsigned row, unsigned ch) { return 6144u * (row >> 3) + 512u * (ch >> 2) + 64u * (row & 7) + 16u * ((ch & 3) ^ ((row >> 2) & 3)); }

__global__ __launch_bounds__(256, 1) void k2_attn(unsigned char* __restrict__ ws) {
  extern __shared__ __attribute__((aligned(16))) unsigned char lds[];
  const int tid = threadIdx.x, lane = tid & 63, w = __builtin_amdgcn_readfirstlane(tid >> 6);
  const int r = lane & 31, hh = lane >> 5;
  const int bid = blockIdx.x, xg = bid & 7, rest = bid >> 3;
  const int qb = xg * 4 + (rest & 3);
  const int ks = rest >> 2;
  const int b = qb >> 4;
  const int q0 = qb * 64, k0 = b * NSEQ + ks * 128;
  const u16* n = (const u16*)(ws + WS_N);
  {
    const int row_in = (lane >> 2) & 7, slot = lane & 3, sub = lane >> 5;
#pragma unroll
    for (int i = 0; i < 24; ++i) {
      const int p = w * 24 + i, rb = p / 6, cp = p % 6;
      const int row = rb * 8 + row_in;
      const int ch = (cp * 2 + sub) * 4 + (slot ^ ((row >> 2) & 3));
      __builtin_amdgcn_global_load_lds((const GAS unsigned*)(n + (size_t)(k0 + row) * D + ch * 8), (LAS unsigned*)(lds + L_NK + p * 1024), 16, 0, 0);
    }
  }
  const float* mod = (const float*)(ws + WS_MOD) + b * 1152;
  v4u nq[12];
#pragma unroll
  for (int i = 0; i < 12; ++i) {
    const int idx = tid + 256 * i, row = idx / 48, ch = idx % 48;
    nq[i] = *(const v4u*)(n + (size_t)(q0 + row) * D + ch * 8);
  }
  if (tid < 192) {
    f32x4 m4 = *(const f32x4*)(mod + tid * 4);
    *(f32x4*)(lds + L_MOD + tid * 16) = m4;
  }
  __syncthreads();
  const float* tl = (const float*)(lds + L_MOD);
  const float* s1l = tl + 384;
#pragma unroll
  for (int i = 0; i < 12; ++i) {
    const int idx = tid + 256 * i, row = idx / 48, ch = idx % 48;
    const f32x4 sa = *(const f32x4*)(s1l + ch * 8), sb = *(const f32x4*)(s1l + ch * 8 + 4);
    const f32x4 ta = *(const f32x4*)(tl + ch * 8), tb = *(const f32x4*)(tl + ch * 8 + 4);
    v4u o;
    o[0] = cvtpk(fmaf(bflo(nq[i][0]), sa[0], ta[0]), fmaf(bfhi(nq[i][0]), sa[1], ta[1]));
    o[1] = cvtpk(fmaf(bflo(nq[i][1]), sa[2], ta[2]), fmaf(bfhi(nq[i][1]), sa[3], ta[3]));
    o[2] = cvtpk(fmaf(bflo(nq[i][2]), sb[0], tb[0]), fmaf(bfhi(nq[i][2]), sb[1], tb[1]));
    o[3] = cvtpk(fmaf(bflo(nq[i][3]), sb[2], tb[2]), fmaf(bfhi(nq[i][3]), sb[3], tb[3]));
    *(v4u*)(lds + L_H + row * 784 + ch * 16) = o;
  }
  __syncthreads();
  f32x16 qa[3][2] = {};
  {
    const v4u* mt = (const v4u*)(ws + WS_MT) + (size_t)(3 * w) * 24 * 64 + lane;
#pragma unroll 4
    for (int s = 0; s < 24; ++s) {
      v4u a0 = mt[s * 64], a1 = mt[(24 + s) * 64], a2 = mt[(48 + s) * 64];
      v4u b0 = *(const v4u*)(lds + L_H + r * 784 + (2 * s + hh) * 16);
      v4u b1 = *(const v4u*)(lds + L_H + (32 + r) * 784 + (2 * s + hh) * 16);
      qa[0][0] = mfma32(a0, b0, qa[0][0]); qa[0][1] = mfma32(a0, b1, qa[0][1]);
      qa[1][0] = mfma32(a1, b0, qa[1][0]); qa[1][1] = mfma32(a1, b1, qa[1][1]);
      qa[2][0] = mfma32(a2, b0, qa[2][0]); qa[2][1] = mfma32(a2, b1, qa[2][1]);
    }
  }
  __syncthreads();
  {
    constexpr float C = 0.05103103630798288f * 1.4426950408889634f;
#pragma unroll
    for (int jt = 0; jt < 3; ++jt)
#pragma unroll
      for (int g = 0; g < 4; ++g) {
        const int j = (3 * w + jt) * 32 + 8 * g + 4 * hh;
        f32x4 sc = *(const f32x4*)(s1l + j);
        sc *= C;
#pragma unroll
        for (int qt = 0; qt < 2; ++qt) {
          v2u o = {cvtpk(qa[jt][qt][4 * g] * sc[0], qa[jt][qt][4 * g + 1] * sc[1]), cvtpk(qa[jt][qt][4 * g + 2] * sc[2], qa[jt][qt][4 * g + 3] * sc[3])};
          const int s = 2 * (3 * w + jt) + (g >> 1);
          *(v2u*)(lds + L_H + ((qt * 24 + s) * 64 + r + 32 * (g & 1)) * 16 + 8 * hh) = o;
        }
      }
  }
  asm volatile("s_waitcnt vmcnt(0)" ::: "memory");
  __syncthreads();
  const int qt = w & 1, kh = w >> 1;
  f32x16 p0 = {}, p1 = {};
#pragma unroll 4
  for (int s = 0; s < 24; ++s) {
    v4u a0 = *(const v4u*)(lds + L_NK + koff(64 * kh + r, 2 * s + hh));
    v4u a1 = *(const v4u*)(lds + L_NK + koff(64 * kh + 32 + r, 2 * s + hh));
    v4u bq = *(const v4u*)(lds + L_H + ((qt * 24 + s) * 64 + lane) * 16);
    p0 = mfma32(a0, bq, p0);
    p1 = mfma32(a1, bq, p1);
  }
  float mx = p0[0];
#pragma unroll
  for (int i = 1; i < 16; ++i) mx = fmaxf(mx, p0[i]);
#pragma unroll
  for (int i = 0; i < 16; ++i) mx = fmaxf(mx, p1[i]);
  mx = fmaxf(mx, __shfl_xor(mx, 32));
  float* mxl = (float*)(lds + L_MX);
  if (hh == 0) mxl[w * 32 + r] = mx;
  __syncthreads();
  mx = fmaxf(mx, mxl[(w ^ 2) * 32 + r]);
  float sum = 0.f;
  v4u pf[4];
  {
    float e0[16], e1[16];
#pragma unroll
    for (int i = 0; i < 16; ++i) { e0[i] = __builtin_amdgcn_exp2f(p0[i] - mx); e1[i] = __builtin_amdgcn_exp2f(p1[i] - mx); }
#pragma unroll
    for (int i = 0; i < 4; ++i) {
      pf[0][i] = cvtpk(e0[2 * i], e0[2 * i + 1]); pf[1][i] = cvtpk(e0[8 + 2 * i], e0[8 + 2 * i + 1]);
      pf[2][i] = cvtpk(e1[2 * i], e1[2 * i + 1]); pf[3][i] = cvtpk(e1[8 + 2 * i], e1[8 + 2 * i + 1]);
    }
#pragma unroll
    for (int f = 0; f < 4; ++f)
#pragma unroll
      for (int i = 0; i < 4; ++i) sum += bflo(pf[f][i]) + bfhi(pf[f][i]);
  }
  sum += __shfl_xor(sum, 32);
#pragma unroll
  for (int f = 0; f < 4; ++f) *(v4u*)(lds + L_H + ((w * 4 + f) * 64 + lane) * 16) = pf[f];
  if (hh == 0) mxl[128 + w * 32 + r] = sum;
  __syncthreads();
  sum += mxl[128 + (w ^ 2) * 32 + r];
  const int dh = kh;
  v4u pall[8];
#pragma unroll
  for (int f = 0; f < 4; ++f) {
    v4u o = *(const v4u*)(lds + L_H + (((w ^ 2) * 4 + f) * 64 + lane) * 16);
    pall[f] = kh ? o : pf[f];
    pall[4 + f] = kh ? pf[f] : o;
  }
  const float inv = 1.f / sum;
  unsigned char* op = ws + WS_OP + ((size_t)(ks * 64 + qb * 2 + qt) * 24) * 1024 + lane * 16;
  {
    const unsigned blk = (lane >> 4) & 1, qq = (lane & 15) >> 2, pp = lane & 3;
    const unsigned ldsbase = (unsigned)(size_t)(LAS unsigned char*)lds;
#pragma unroll 1
    for (int dt = 0; dt < 6; ++dt) {
      const int c = 6 * dh + dt;
      f32x16 o = {};
      s16x4 lo[8], hi[8];
#pragma unroll
      for (int s = 0; s < 8; ++s) {
        const unsigned a0 = ldsbase + L_NK + koff(16 * s + 4 * hh + qq, 4 * c + 2 * blk + (pp >> 1)) + 8 * (pp & 1);
        const unsigned a1 = ldsbase + L_NK + koff(16 * s + 8 + 4 * hh + qq, 4 * c + 2 * blk + (pp >> 1)) + 8 * (pp & 1);
        asm volatile("ds_read_b64_tr_b16 %0, %1" : "=v"(lo[s]) : "v"(a0) : "memory");
        asm volatile("ds_read_b64_tr_b16 %0, %1" : "=v"(hi[s]) : "v"(a1) : "memory");
      }
      asm volatile("s_waitcnt lgkmcnt(0)" ::: "memory");
      __builtin_amdgcn_sched_barrier(0);
#pragma unroll
      for (int s = 0; s < 8; ++s) {
        bf16x8 a = __builtin_shufflevector(lo[s], hi[s], 0, 1, 2, 3, 4, 5, 6, 7);
        o = __builtin_amdgcn_mfma_f32_32x32x16_bf16(a, __builtin_bit_cast(bf16x8, pall[s]), o, 0, 0, 0);
      }
#pragma unroll
      for (int s2 = 0; s2 < 2; ++s2) {
        const f32x4 sa = *(const f32x4*)(s1l + c * 32 + 16 * s2 + 4 * hh), sb = *(const f32x4*)(s1l + c * 32 + 16 * s2 + 8 + 4 * hh);
        v4u ov;
        ov[0] = cvtpk(o[8 * s2 + 0] * inv * sa[0], o[8 * s2 + 1] * inv * sa[1]);
        ov[1] = cvtpk(o[8 * s2 + 2] * inv * sa[2], o[8 * s2 + 3] * inv * sa[3]);
        ov[2] = cvtpk(o[8 * s2 + 4] * inv * sb[0], o[8 * s2 + 5] * inv * sb[1]);
        ov[3] = cvtpk(o[8 * s2 + 6] * inv * sb[2], o[8 * s2 + 7] * inv * sb[3]);
        *(v4u*)(op + (size_t)(2 * c + s2) * 1024) = ov;
      }
    }
  }
  if (dh == 0 && hh == 0) {
    float2* ml = (float2*)(ws + WS_ML) + (size_t)ks * NR + q0 + qt * 32 + r;
    *ml = make_float2(mx, sum);
  }
}

__global__ __launch_bounds__(256, 1) void k3_out(const u16* __restrict__ x, const unsigned char* __restrict__ ws, u16* __restrict__ out) {
  __shared__ __attribute__((aligned(16))) float red[4 * 32 * 100];
  const int tid = threadIdx.x, lane = tid & 63, w = __builtin_amdgcn_readfirstlane(tid >> 6);
  const int r = lane & 31, hh = lane >> 5;
  const int bid = blockIdx.x, xg = bid & 7, rest = bid >> 3;
  const int qt = xg * 8 + (rest & 7);
  const int eg = rest >> 3;
  const int b = qt >> 5;
  const float* mod = (const float*)(ws + WS_MOD) + b * 1152;
  v4u part[6][NSPLIT];
  const unsigned char* op = ws + WS_OP + ((size_t)qt * 24 + 6 * w) * 1024 + lane * 16;
#pragma unroll
  for (int s = 0; s < 6; ++s)
#pragma unroll
    for (int k = 0; k < NSPLIT; ++k) part[s][k] = *(const v4u*)(op + (size_t)k * 64 * 24 * 1024 + s * 1024);
  float2 ml[NSPLIT];
#pragma unroll
  for (int k = 0; k < NSPLIT; ++k) ml[k] = ((const float2*)(ws + WS_ML))[(size_t)k * NR + qt * 32 + r];
  v4u wf[3][6];
  const v4u* w2 = (const v4u*)(ws + WS_W2T) + ((size_t)(3 * eg) * 24 + 6 * w) * 64 + lane;
#pragma unroll
  for (int et = 0; et < 3; ++et)
#pragma unroll
    for (int s = 0; s < 6; ++s) wf[et][s] = w2[(et * 24 + s) * 64];
  float m = ml[0].x;
#pragma unroll
  for (int k = 1; k < NSPLIT; ++k) m = fmaxf(m, ml[k].x);
  float wk[NSPLIT], tot = 0.f;
#pragma unroll
  for (int k = 0; k < NSPLIT; ++k) { wk[k] = ml[k].y * __builtin_amdgcn_exp2f(ml[k].x - m); tot += wk[k]; }
  const float inv = 1.f / tot;
#pragma unroll
  for (int k = 0; k < NSPLIT; ++k) wk[k] *= inv;
  f32x16 acc[3] = {};
#pragma unroll
  for (int s = 0; s < 6; ++s) {
    const int j0 = (6 * w + s) * 16 + 4 * hh;
    const f32x4 ta = *(const f32x4*)(mod + j0), tb = *(const f32x4*)(mod + j0 + 8);
    float v[8] = {ta[0], ta[1], ta[2], ta[3], tb[0], tb[1], tb[2], tb[3]};
#pragma unroll
    for (int k = 0; k < NSPLIT; ++k)
#pragma unroll
      for (int e = 0; e < 4; ++e) {
        v[2 * e] = fmaf(wk[k], bflo(part[s][k][e]), v[2 * e]);
        v[2 * e + 1] = fmaf(wk[k], bfhi(part[s][k][e]), v[2 * e + 1]);
      }
    v4u bq = {cvtpk(v[0], v[1]), cvtpk(v[2], v[3]), cvtpk(v[4], v[5]), cvtpk(v[6], v[7])};
#pragma unroll
    for (int et = 0; et < 3; ++et) acc[et] = mfma32(wf[et][s], bq, acc[et]);
  }
#pragma unroll
  for (int et = 0; et < 3; ++et)
#pragma unroll
    for (int g = 0; g < 4; ++g)
      *(f32x4*)&red[(w * 32 + r) * 100 + et * 32 + 8 * g + 4 * hh] = f32x4{acc[et][4 * g], acc[et][4 * g + 1], acc[et][4 * g + 2], acc[et][4 * g + 3]};
  __syncthreads();
#pragma unroll
  for (int i = 0; i < 3; ++i) {
    const int task = tid + 256 * i, q = task / 24, e4 = (task % 24) * 4;
    f32x4 t = *(f32x4*)&red[q * 100 + e4];
#pragma unroll
    for (int ww = 1; ww < 4; ++ww) t += *(f32x4*)&red[(ww * 32 + q) * 100 + e4];
    const int col = eg * 96 + e4;
    const f32x4 g4 = *(const f32x4*)(mod + 768 + col);
    const size_t o = (size_t)(qt * 32 + q) * D + col;
    const v2u xv = *(const v2u*)(x + o);
    v2u ov = {cvtpk(bflo(xv[0]) + g4[0] * t[0], bfhi(xv[0]) + g4[1] * t[1]), cvtpk(bflo(xv[1]) + g4[2] * t[2], bfhi(xv[1]) + g4[3] * t[3])};
    *(v2u*)(out + o) = ov;
  }
}

extern "C" void kernel_launch(void* const* d_in, const int* in_sizes, int n_in, void* d_out, int out_size, void* d_ws, size_t ws_size,
                              hipStream_t stream) {
  const u16* x = (const u16*)d_in[0];
  const u16* c = (const u16*)d_in[1];
  const u16* wqkv = (const u16*)d_in[2];
  const u16* wo = (const u16*)d_in[3];
  const u16* wmod = (const u16*)d_in[4];
  unsigned char* ws = (unsigned char*)d_ws;
  (void)hipFuncSetAttribute((const void*)k2_attn, hipFuncAttributeMaxDynamicSharedMemorySize, L_K2);
  k1_prep<<<K1_MOD + K1_M + K1_W2 + K1_LN, 256, 0, stream>>>(x, c, wqkv, wo, wmod, ws);
  k2_attn<<<256, 256, L_K2, stream>>>(ws);
  k3_out<<<256, 256, 0, stream>>>(x, ws, (u16*)d_out);
}
```

```cpp
#include <hip/hip_runtime.h>
#include <hip/hip_bf16.h>

typedef short bf16x8 __attribute__((ext_vector_type(8)));
typedef short s16x4 __attribute__((ext_vector_type(4)));
typedef float f32x4 __attribute__((ext_vector_type(4)));
typedef float f32x2 __attribute__((ext_vector_type(2)));
typedef unsigned v4u __attribute__((ext_vector_type(4)));
typedef unsigned v2u __attribute__((ext_vector_type(2)));
typedef __bf16 bf2 __attribute__((ext_vector_type(2)));
typedef unsigned short u16;
#define DI __device__ __forceinline__
#define LAS __attribute__((address_space(3)))
#define GAS __attribute__((address_space(1)))
typedef GAS unsigned gu32;
#define STDECL
#define STAMP(i)
#define STFLUSH()
#define PRE 2
#define DLY2 0
#define DLY3 12

constexpr int O_EPOCH = 0;
constexpr int O_CL = 1024;
constexpr int O_MOD = 4096;
constexpr int O_MT = 65536;
constexpr int O_W2 = O_MT + 589824;
constexpr int O_QF = O_W2 + 589824;
constexpr int O_VF = O_QF + 1572864;
constexpr int O_PF = O_VF + 1572864;
constexpr int O_MV = O_PF + 4194304;
constexpr int CLR_PER_WG = 30720;
constexpr int WS_BYTES = O_QF + 256 * CLR_PER_WG;
static_assert(O_MV + 32768 <= WS_BYTES, "");

constexpr int L_P0 = 0;
constexpr int L_SG = 0, L_MX = 512, L_LS = 1536;
constexpr int L_RING = 61440;
constexpr int RING = 12288;
constexpr int R_MS = 0, R_QS = 3072, R_BN = 7168, R_ST = 8192, R_CS = 8448;
constexpr int L_TAG = L_RING + 8 * RING;
constexpr int L_TOTAL = L_TAG + 16;

constexpr int AUX_SC1 = 16;
constexpr unsigned SPIN_CAP = 1u << 16;

DI float bflo(unsigned v) { return __uint_as_float(v << 16); }
DI float bfhi(unsigned v) { return __uint_as_float(v & 0xffff0000u); }
DI unsigned cvtpk(float lo, float hi) {
  f32x2 x = {lo, hi};
  return __builtin_bit_cast(unsigned, __builtin_convertvector(x, bf2));
}
DI float bfround(float x) { return bflo(cvtpk(x, 0.f)); }
DI unsigned pkhi(unsigned lo, unsigned hi) { return __builtin_amdgcn_perm(hi, lo, 0x07060302u); }
DI unsigned tagged(float v, unsigned tag16) { return (cvtpk(0.f, v) & 0xffff0000u) | tag16; }
DI unsigned nosent(unsigned v) { return min(v, 0xfffffffeu); }
DI unsigned max4(unsigned a, v4u v) { return max(max(max(a, v[0]), v[1]), max(v[2], v[3])); }
DI float dot2(unsigned a, unsigned b, float c) { return __builtin_amdgcn_fdot2_f32_bf16(__builtin_bit_cast(bf2, a), __builtin_bit_cast(bf2, b), c, false); }
DI f32x4 mfma(v4u a, v4u b, f32x4 c) {
  return __builtin_amdgcn_mfma_f32_16x16x32_bf16(__builtin_bit_cast(bf16x8, a), __builtin_bit_cast(bf16x8, b), c, 0, 0, 0);
}
DI float sum_g(float v) {
  auto r = __builtin_amdgcn_permlane16_swap(__float_as_uint(v), __float_as_uint(v), false, false);
  v = __uint_as_float(r[0]) + __uint_as_float(r[1]);
  auto q = __builtin_amdgcn_permlane32_swap(__float_as_uint(v), __float_as_uint(v), false, false);
  return __uint_as_float(q[0]) + __uint_as_float(q[1]);
}
DI float max_g(float v) {
  auto r = __builtin_amdgcn_permlane16_swap(__float_as_uint(v), __float_as_uint(v), false, false);
  v = fmaxf(__uint_as_float(r[0]), __uint_as_float(r[1]));
  auto q = __builtin_amdgcn_permlane32_swap(__float_as_uint(v), __float_as_uint(v), false, false);
  return fmaxf(__uint_as_float(q[0]), __uint_as_float(q[1]));
}
#define BAR() do { asm volatile("s_waitcnt lgkmcnt(0)" ::: "memory"); __builtin_amdgcn_s_barrier(); asm volatile("" ::: "memory"); } while (0)
#define VMWAIT(n) asm volatile("s_waitcnt vmcnt(" #n ")" ::: "memory")
#define SB() __builtin_amdgcn_sched_barrier(0)
#define CBAR() asm volatile("" ::: "memory")
#define LD128(off) __builtin_amdgcn_raw_buffer_load_b128(rws, (off), 0, AUX_SC1)
#define LD32(off) __builtin_amdgcn_raw_buffer_load_b32(rws, (off), 0, AUX_SC1)
#define ST128(v, off) __builtin_amdgcn_raw_buffer_store_b128((v), rws, (off), 0, AUX_SC1)
#define ST64(v, off) __builtin_amdgcn_raw_buffer_store_b64((v), rws, (off), 0, AUX_SC1)
#define ST32(v, off) __builtin_amdgcn_raw_buffer_store_b32((v), rws, (off), 0, AUX_SC1)
DI void dma(unsigned ldsaddr, int voff, __amdgpu_buffer_rsrc_t rs, int soff) {
  asm volatile("s_mov_b32 m0, %0\n\ts_nop 0\n\tbuffer_load_dwordx4 %1, %2, %3 offen lds" :: "s"(ldsaddr), "v"(voff), "s"(rs), "s"(soff) : "memory");
}

__global__ __launch_bounds__(512, 2) void dit_kernel(const u16* __restrict__ x, const u16* __restrict__ c, const u16* __restrict__ wqkv,
                                                     const u16* __restrict__ wo, const u16* __restrict__ wmod, unsigned char* ws,
                                                     u16* __restrict__ out) {
  extern __shared__ __attribute__((aligned(16))) unsigned char lds_[];
  LAS unsigned char* lds = (LAS unsigned char*)lds_;
  const unsigned ldsb = (unsigned)(size_t)lds;
  STDECL;
  STAMP(0);
  const int tid = threadIdx.x, lane = tid & 63, w = __builtin_amdgcn_readfirstlane(tid >> 6);
  const int n16 = lane & 15, g = lane >> 4;
  const int bid = blockIdx.x, xg = bid & 7, r = bid >> 3;
  const int b = xg >> 2, ql = r & 7, kb = r >> 3;
  const int qt = b * 32 + (xg & 3) * 8 + ql;
  const bool act1 = ql < 6;
  const int cb = (xg & 3) * 6 + (act1 ? ql : ql - 2);
  const int prow = 8 * (n16 >> 2) + (n16 & 3);
  const __amdgpu_buffer_rsrc_t rws = __builtin_amdgcn_make_buffer_rsrc(ws, 0, WS_BYTES, 0x00020000);
  const __amdgpu_buffer_rsrc_t rx = __builtin_amdgcn_make_buffer_rsrc((void*)x, 0, 2048 * 768, 0x00020000);
  LAS unsigned char* ring = lds + L_RING + w * RING;
  const int dch = 16 * ((lane & 3) ^ ((lane & 32) ? 3 : 0));
  const int rslot = (4 * n16 + (g ^ ((n16 & 8) ? 3 : 0))) * 16;

  const int mu = (r - 24) * 8 + xg;
  const int role = r < 12 ? 0 : (r < 24 ? 1 : (mu < 36 ? 2 : 3));
  const int ru = role == 1 ? r - 12 : r;
  const int u12 = 6 * (xg & 1) + ru % 6, u8 = 2 * (xg >> 1) + ru / 6;

  const int xv = (b * 1024 + kb * 256 + 32 * w + 8 * (lane >> 4) + ((lane >> 2) & 3)) * 768 + dch;
  auto xgroup = [&](int p) {
#pragma unroll
    for (int j = 0; j < 4; ++j) dma(ldsb + L_RING + w * RING + ((p % 3) * 4 + j) * 1024, xv, rx, (j >> 1) * 3072 + (2 * p + (j & 1)) * 64);
  };
  asm volatile("s_nop 4" ::: "memory");
  if (w < 6) {
    unsigned cv = 0;
    if (role == 2) cv = ((const unsigned*)c)[tid];
    if (role < 2) {
      const __amdgpu_buffer_rsrc_t rq = __builtin_amdgcn_make_buffer_rsrc((void*)wqkv, 0, 384 * 2304, 0x00020000);
      const int v = (lane >> 2) * 2304 + dch;
      const int nt = role ? 3 : 5;
#pragma unroll 1
      for (int t = 0; t < nt; ++t) {
        const int so = (role ? (48 * u8 + 16 * t) * 2304 + 1536 : (t < 2 ? (32 * u12 + 16 * t) * 2304 : (48 * u8 + 16 * (t - 2)) * 2304 + 768)) + 128 * w;
        dma(ldsb + L_P0 + (t * 12 + 2 * w) * 1024, v, rq, so);
        dma(ldsb + L_P0 + (t * 12 + 2 * w + 1) * 1024, v, rq, so + 64);
      }
    }
    if (role == 1 || role == 2) {
      const int ld = role == 1 ? 768 : 4608;
      const __amdgpu_buffer_rsrc_t rc = __builtin_amdgcn_make_buffer_rsrc((void*)(role == 1 ? wo : wmod), 0, 384 * ld, 0x00020000);
      const int v = (lane >> 2) * ld + dch;
#pragma unroll
      for (int j = 0; j < 4; ++j) dma(ldsb + L_P0 + (36 + 4 * w + j) * 1024, v, rc, (role == 1 ? u12 : mu) * 64 + (4 * w + j) * 16 * ld);
    }
    if (role == 2) {
      const float f0 = bflo(cv), f1 = bfhi(cv);
      const int bb = tid >= 192, i2 = 2 * (tid - 192 * bb);
      *(LAS unsigned*)(lds + L_P0 + (i2 >> 5) * 1024 + (bb * 4 + ((i2 >> 3) & 3)) * 16 + (i2 & 7) * 2) = cvtpk(f0 / (1.f + __expf(-f0)), f1 / (1.f + __expf(-f1)));
    }
  }
  if (w == 7) {
    const unsigned e = __hip_atomic_load((gu32*)(ws + O_EPOCH), __ATOMIC_RELAXED, __HIP_MEMORY_SCOPE_AGENT);
    const v4u sent = {~0u, ~0u, ~0u, ~0u};
#pragma unroll 2
    for (int i = 0; i < 30; ++i) ST128(sent, O_QF + bid * CLR_PER_WG + i * 1024 + lane * 16);
    *(LAS unsigned*)(lds + L_TAG) = e + 1u;
    VMWAIT(0);
    if (lane == 0) __hip_atomic_store((gu32*)(ws + O_CL + bid * 4), e + 1u, __ATOMIC_RELAXED, __HIP_MEMORY_SCOPE_AGENT);
  }
#pragma unroll
  for (int p = 0; p < PRE; ++p) xgroup(p);
  STAMP(1);
  VMWAIT(8);
  STAMP(2);
  BAR();
  STAMP(3);
  const unsigned tag = __builtin_amdgcn_readfirstlane(*(LAS unsigned*)(lds + L_TAG));
  const unsigned tag16 = tag & 0xffffu;
  if (w < (role == 2 ? 2 : 6) && role != 3) {
    const int rt = role == 0 ? (w & 1) : (w >> 1), ct = role == 0 ? (w >> 1) : (w & 1);
    LAS unsigned char* ap = lds + L_P0 + (role == 2 ? ((n16 & 1) * 4 + g) * 16 : rt * 12288 + rslot);
    f32x4 acc = {};
    if (role == 0) {
      LAS unsigned char* bp = lds + L_P0 + (2 + ct) * 12288 + rslot;
#pragma unroll
      for (int k = 0; k < 12; ++k) acc = mfma(*(LAS v4u*)(ap + k * 1024), *(LAS v4u*)(bp + k * 1024), acc);
    } else {
      LAS unsigned char* tb = lds + L_P0 + 36864 + (8 * g + (n16 >> 2)) * 64 + 16 * ((2 * ct + ((n16 >> 1) & 1)) ^ ((g & 1) ? 3 : 0)) + 8 * (n16 & 1);
#pragma unroll
      for (int k = 0; k < 12; ++k) {
        const s16x4 lo = __builtin_amdgcn_ds_read_tr16_b64_v4i16((LAS s16x4*)(tb + k * 2048));
        const s16x4 hi = __builtin_amdgcn_ds_read_tr16_b64_v4i16((LAS s16x4*)(tb + k * 2048 + 256));
        acc = mfma(*(LAS v4u*)(ap + k * 1024), __builtin_bit_cast(v4u, __builtin_shufflevector(lo, hi, 0, 1, 2, 3, 4, 5, 6, 7)), acc);
      }
    }
    if (role != 2) {
      const v4u o = {tagged(acc[0], tag16), tagged(acc[1], tag16), tagged(acc[2], tag16), tagged(acc[3], tag16)};
      const int blk = role == 0 ? (3 * u8 + ct) * 24 + 2 * u12 + rt : (2 * u12 + ct) * 24 + 3 * u8 + rt;
      ST128(o, (role ? O_W2 : O_MT) + blk * 1024 + lane * 16);
    } else if (g < 2) {
      float v = bfround(g ? acc[1] : acc[0]);
      if (mu >= 12 && mu < 24) v += 1.f;
      ST32(tagged(v, tag16), O_MOD + (g * 1152 + 32 * mu + 16 * ct + n16) * 4);
    }
  }
  STAMP(4);
#pragma unroll
  for (int p = PRE; p < 3; ++p) xgroup(p);
  v4u xf[2][12];
  float rq[2], mq[2];
  f32x4 rk[2], mk[2];
  {
    unsigned one2 = 0x3f803f80u;
    asm volatile("" : "+s"(one2));
    float s[2] = {0.f, 0.f}, q[2] = {0.f, 0.f};
#pragma unroll
    for (int p = 0; p < 6; ++p) {
      if (p < 4) VMWAIT(8); else if (p == 4) VMWAIT(4); else VMWAIT(0);
#pragma unroll
      for (int j = 0; j < 4; ++j) xf[j >> 1][2 * p + (j & 1)] = *(LAS v4u*)(ring + ((p % 3) * 4 + j) * 1024 + rslot);
      if (p < 3) {
#pragma unroll
        for (int j = 0; j < 4; ++j) asm volatile("" : "+v"(xf[j >> 1][2 * p + (j & 1)]));
        xgroup(p + 3);
      }
#pragma unroll
      for (int j = 0; j < 4; ++j)
#pragma unroll
        for (int e = 0; e < 4; ++e) {
          const unsigned d = xf[j >> 1][2 * p + (j & 1)][e];
          s[j >> 1] = dot2(d, one2, s[j >> 1]); q[j >> 1] = dot2(d, d, q[j >> 1]);
        }
      SB();
    }
#pragma unroll
    for (int t = 0; t < 2; ++t) {
      const float m = sum_g(s[t]) * (1.f / 384.f);
      const float var = fmaxf(sum_g(q[t]) * (1.f / 384.f) - m * m, 0.f);
      mq[t] = m; rq[t] = rsqrtf(var + 1e-5f);
      if (g == 0) { *(LAS float*)(ring + R_ST + (prow + 4 * t) * 4) = rq[t]; *(LAS float*)(ring + R_ST + 128 + (prow + 4 * t) * 4) = m; }
    }
    CBAR();
#pragma unroll
    for (int t = 0; t < 2; ++t) {
      rk[t] = *(LAS f32x4*)(ring + R_ST + (8 * g + 4 * t) * 4);
      mk[t] = *(LAS f32x4*)(ring + R_ST + 128 + (8 * g + 4 * t) * 4);
    }
  }
  STAMP(5);
  const v4u ones = {0x3f803f80u, 0x3f803f80u, 0x3f803f80u, 0x3f803f80u};
  {
    v4u mf[3][2], mv4;
    const int s0 = 3 * (w & 3);
    const int mo = O_MOD + (b * 1152 + (lane < 24 ? 384 + 32 * s0 + 4 * lane : (lane < 48 ? 32 * s0 + 4 * (lane - 24) : ((lane & 4) ? 768 : 384) + 16 * cb + 4 * (lane & 3)))) * 4;
    const int fo = (w < 4 ? O_MT : O_W2) + (cb * 24 + 2 * s0 + (g >> 1)) * 1024 + ((2 * (g & 1)) * 16 + n16) * 16;
#pragma unroll 1
    for (unsigned it = 0; it < SPIN_CAP; ++it) {
      CBAR();
      mv4 = LD128(mo);
#pragma unroll
      for (int i = 0; i < 3; ++i) { mf[i][0] = LD128(fo + i * 2048); mf[i][1] = LD128(fo + i * 2048 + 256); }
      unsigned bad = 0;
      if (w == 6) { const v4u cl = LD128(O_CL + lane * 16); const unsigned d = (cl[0] ^ tag) | (cl[1] ^ tag) | (cl[2] ^ tag) | (cl[3] ^ tag); bad = d | (d >> 16); }
#pragma unroll
      for (int e = 0; e < 4; ++e) bad |= mv4[e] ^ tag;
#pragma unroll
      for (int i = 0; i < 3; ++i)
#pragma unroll
        for (int e = 0; e < 4; ++e) bad |= (mf[i][0][e] ^ tag) | (mf[i][1][e] ^ tag);
      if (__all((bad & 0xffffu) == 0)) break;
      __builtin_amdgcn_s_sleep(6);
    }
    STAMP(6);
    if (bid == 0 && tid == 384) __hip_atomic_store((gu32*)(ws + O_EPOCH), tag, __ATOMIC_RELAXED, __HIP_MEMORY_SCOPE_AGENT);
    if (lane >= 24 && lane < 48) *(LAS v2u*)(ring + R_BN + 512 + (lane - 24) * 8) = v2u{pkhi(mv4[0], mv4[1]), pkhi(mv4[2], mv4[3])};
    else *(LAS v4u*)(ring + R_BN + (lane < 24 ? lane : (lane & 7) + 24) * 16) = mv4 & 0xffff0000u;
    CBAR();
    f32x4 acs = {}, au = {};
#pragma unroll
    for (int i = 0; i < 3; ++i) {
      const f32x4 sa = *(LAS f32x4*)(ring + R_BN + (32 * i + 8 * g) * 4), sb = *(LAS f32x4*)(ring + R_BN + (32 * i + 8 * g) * 4 + 16);
      const v4u tf = *(LAS v4u*)(ring + R_BN + 512 + (32 * i + 8 * g) * 2);
      const v4u o = {cvtpk(bfhi(mf[i][0][0]) * sa[0], bfhi(mf[i][0][1]) * sa[1]), cvtpk(bfhi(mf[i][0][2]) * sa[2], bfhi(mf[i][0][3]) * sa[3]),
                     cvtpk(bfhi(mf[i][1][0]) * sb[0], bfhi(mf[i][1][1]) * sb[1]), cvtpk(bfhi(mf[i][1][2]) * sb[2], bfhi(mf[i][1][3]) * sb[3])};
      const v4u raw = {pkhi(mf[i][0][0], mf[i][0][1]), pkhi(mf[i][0][2], mf[i][0][3]), pkhi(mf[i][1][0], mf[i][1][1]), pkhi(mf[i][1][2], mf[i][1][3])};
      *(LAS v4u*)(ring + R_MS + i * 1024 + lane * 16) = o;
      acs = mfma(o, ones, acs);
      au = mfma(raw, tf, au);
    }
    if (n16 == 0) { *(LAS f32x4*)(ring + R_CS + g * 16) = acs; *(LAS f32x4*)(ring + R_CS + 64 + g * 16) = au; }
    f32x4 s1o = *(LAS f32x4*)(ring + R_BN + 384 + g * 16);
    const float gate = *(LAS float*)(ring + R_BN + 448 + n16 * 4);
    STAMP(7);
    BAR();
    const int qrow = b * 32 + kb * 8 + w;
    const int dead = act1 ? 0 : 0x40000000;
    {
      f32x4 acc[2] = {};
#pragma unroll
      for (int s = 0; s < 12; ++s) {
        const v4u a = *(LAS v4u*)(lds + L_RING + (s / 3) * RING + R_MS + (s % 3) * 1024 + lane * 16);
        acc[0] = mfma(a, xf[0][s], acc[0]);
        acc[1] = mfma(a, xf[1][s], acc[1]);
      }
      f32x4 cs = {}, uu = {};
#pragma unroll
      for (int k = 0; k < 4; ++k) { cs += *(LAS f32x4*)(lds + L_RING + k * RING + R_CS + g * 16); uu += *(LAS f32x4*)(lds + L_RING + k * RING + R_CS + 64 + g * 16); }
      s1o *= 0.05103103630798288f * 1.4426950408889634f;
#pragma unroll
      for (int t = 0; t < 2; ++t) {
        float v[4];
#pragma unroll
        for (int i = 0; i < 4; ++i) v[i] = fmaf(fmaf(-mq[t], cs[i], acc[t][i]), rq[t], uu[i]) * s1o[i];
        const v2u o = {nosent(cvtpk(v[0], v[1])), nosent(cvtpk(v[2], v[3]))};
        ST64(o, dead + O_QF + ((qrow * 2 + t) * 12 + (cb >> 1)) * 1024 + ((2 * (cb & 1) + (g >> 1)) * 16 + n16) * 16 + 8 * (g & 1));
      }
    }
    STAMP(8);
    SB();
    {
      f32x4 acc[2] = {};
#pragma unroll
      for (int s = 0; s < 12; ++s) {
        const v4u bb = *(LAS v4u*)(lds + L_RING + (4 + s / 3) * RING + R_MS + (s % 3) * 1024 + lane * 16);
        acc[0] = mfma(xf[0][s], bb, acc[0]);
        acc[1] = mfma(xf[1][s], bb, acc[1]);
      }
      float cs = 0.f, z = 0.f;
#pragma unroll
      for (int k = 4; k < 8; ++k) { cs += *(LAS float*)(lds + L_RING + k * RING + R_CS + n16 * 4); z += *(LAS float*)(lds + L_RING + k * RING + R_CS + 64 + n16 * 4); }
      float v[2][4];
#pragma unroll
      for (int t = 0; t < 2; ++t)
#pragma unroll
        for (int i = 0; i < 4; ++i) v[t][i] = fmaf(fmaf(-mk[t][i], cs, acc[t][i]), rk[t][i], z) * gate;
      const v4u o = {nosent(cvtpk(v[0][0], v[0][1])), nosent(cvtpk(v[0][2], v[0][3])), nosent(cvtpk(v[1][0], v[1][1])), nosent(cvtpk(v[1][2], v[1][3]))};
      ST128(o, dead + O_VF + (qrow * 24 + cb) * 1024 + lane * 16);
    }
  }
  STAMP(9);
  if (DLY2) __builtin_amdgcn_s_sleep(DLY2);
  {
    const int u = w >> 2;
    v4u qf[3];
#pragma unroll 1
    for (unsigned it = 0; it < SPIN_CAP; ++it) {
      CBAR();
#pragma unroll
      for (int i = 0; i < 3; ++i) qf[i] = LD128(O_QF + ((qt * 2 + u) * 12 + 3 * (w & 3) + i) * 1024 + lane * 16);
      if (__all(max4(max4(max4(0u, qf[0]), qf[1]), qf[2]) != ~0u)) break;
      __builtin_amdgcn_s_sleep(4);
    }
    STAMP(10);
    f32x4 sg = {};
#pragma unroll
    for (int i = 0; i < 3; ++i) {
      *(LAS v4u*)(ring + R_QS + i * 1024 + lane * 16) = qf[i];
      sg = mfma(ones, qf[i], sg);
    }
    if (g == 0) *(LAS float*)(lds + L_SG + (u * 16 + n16) * 16 + (w & 3) * 4) = sg[0];
  }
  v4u vf[4][6];
  const int vo = O_VF + ((b * 32 + 4 * w) * 24 + 6 * kb) * 1024 + lane * 16;
#pragma unroll
  for (int s = 0; s < 4; ++s)
#pragma unroll
    for (int et = 0; et < 6; ++et) vf[s][et] = LD128(vo + (s * 24 + et) * 1024);
  const int oq = tid / 12, oc = tid % 12;
  const int ooff = ((qt * 32 + oq) * 384 + 96 * kb + 8 * oc) * 2;
  const v4u xr = __builtin_amdgcn_raw_buffer_load_b128(rx, tid < 384 ? ooff : 0, 0, 0);
  STAMP(11);
  BAR();
  {
    f32x4 acc[2][2] = {};
#pragma unroll
    for (int s = 0; s < 12; ++s) {
      const v4u q0 = *(LAS v4u*)(lds + L_RING + (s / 3) * RING + R_QS + (s % 3) * 1024 + lane * 16);
      const v4u q1 = *(LAS v4u*)(lds + L_RING + (4 + s / 3) * RING + R_QS + (s % 3) * 1024 + lane * 16);
      acc[0][0] = mfma(xf[0][s], q0, acc[0][0]); acc[0][1] = mfma(xf[1][s], q0, acc[0][1]);
      acc[1][0] = mfma(xf[0][s], q1, acc[1][0]); acc[1][1] = mfma(xf[1][s], q1, acc[1][1]);
    }
    float sc[2][8];
#pragma unroll
    for (int u = 0; u < 2; ++u) {
      const f32x4 sp = *(LAS f32x4*)(lds + L_SG + (u * 16 + n16) * 16);
      const float sig = (sp[0] + sp[1]) + (sp[2] + sp[3]);
      float mx = -3.0e38f;
#pragma unroll
      for (int t = 0; t < 2; ++t)
#pragma unroll
        for (int i = 0; i < 4; ++i) { sc[u][4 * t + i] = fmaf(-mk[t][i], sig, acc[u][t][i]) * rk[t][i]; mx = fmaxf(mx, sc[u][4 * t + i]); }
      mx = max_g(mx);
      if (g == 0) *(LAS float*)(lds + L_MX + (u * 16 + n16) * 32 + w * 4) = mx;
    }
    STAMP(12);
    BAR();
#pragma unroll
    for (int u = 0; u < 2; ++u) {
      const f32x4 ma = *(LAS f32x4*)(lds + L_MX + (u * 16 + n16) * 32), mb = *(LAS f32x4*)(lds + L_MX + (u * 16 + n16) * 32 + 16);
      const float m = fmaxf(fmaxf(fmaxf(ma[0], ma[1]), fmaxf(ma[2], ma[3])), fmaxf(fmaxf(mb[0], mb[1]), fmaxf(mb[2], mb[3])));
      float p[8];
#pragma unroll
      for (int i = 0; i < 8; ++i) p[i] = __builtin_amdgcn_exp2f(sc[u][i] - m);
      const v4u o = {nosent(cvtpk(p[0], p[1])), nosent(cvtpk(p[2], p[3])), nosent(cvtpk(p[4], p[5])), nosent(cvtpk(p[6], p[7]))};
      ST128(o, O_PF + ((qt * 32 + kb * 8 + w) * 2 + u) * 1024 + lane * 16);
      if (w == 0 && g == 0) ST32(nosent(__float_as_uint(m)), O_MV + ((qt * 4 + kb) * 32 + u * 16 + n16) * 4);
    }
  }
  STAMP(13);
#pragma unroll 1
  for (unsigned it = 0; it < SPIN_CAP; ++it) {
    unsigned m = 0;
#pragma unroll
    for (int s = 0; s < 4; ++s)
#pragma unroll
      for (int et = 0; et < 6; ++et) m = max4(m, vf[s][et]);
    if (__all(m != ~0u)) break;
    CBAR();
#pragma unroll
    for (int s = 0; s < 4; ++s)
#pragma unroll
      for (int et = 0; et < 6; ++et) vf[s][et] = LD128(vo + (s * 24 + et) * 1024);
  }
  STAMP(14);
  if (DLY3) __builtin_amdgcn_s_sleep(DLY3);
  {
    f32x4 acc[2][6];
    float ls[2];
    unsigned mv[2], mo[2];
    unsigned one2 = 0x3f803f80u;
    asm volatile("" : "+s"(one2));
#pragma unroll 1
    for (unsigned it = 0; it < SPIN_CAP; ++it) {
      CBAR();
      v4u pf[4][2];
#pragma unroll
      for (int s = 0; s < 4; ++s)
#pragma unroll
        for (int u = 0; u < 2; ++u) pf[s][u] = LD128(O_PF + ((qt * 32 + 4 * w + s) * 2 + u) * 1024 + lane * 16);
#pragma unroll
      for (int u = 0; u < 2; ++u) mv[u] = LD32(O_MV + ((qt * 4 + g) * 32 + u * 16 + n16) * 4);
      SB();
      unsigned m = 0;
#pragma unroll
      for (int u = 0; u < 2; ++u) {
        ls[u] = 0.f;
#pragma unroll
        for (int et = 0; et < 6; ++et) acc[u][et] = f32x4{0.f, 0.f, 0.f, 0.f};
      }
#pragma unroll
      for (int s = 0; s < 4; ++s)
#pragma unroll
        for (int u = 0; u < 2; ++u) {
#pragma unroll
          for (int et = 0; et < 6; ++et) acc[u][et] = mfma(vf[s][et], pf[s][u], acc[u][et]);
#pragma unroll
          for (int e = 0; e < 4; ++e) ls[u] = dot2(pf[s][u][e], one2, ls[u]);
          m = max4(m, pf[s][u]);
        }
      m = max(m, max(mv[0], mv[1]));
      if (__all(m != ~0u)) break;
    }
    STAMP(15);
#pragma unroll
    for (int u = 0; u < 2; ++u) {
      mo[u] = __builtin_amdgcn_ds_bpermute(((w >> 1) * 16 + n16) * 4, mv[u]);
      const float f = __builtin_amdgcn_exp2f(__uint_as_float(mo[u]) - max_g(__uint_as_float(mv[u])));
      const float l = sum_g(ls[u]) * f;
      const int q = prow + 4 * u;
      if (g == 0) *(LAS float*)(lds + L_LS + q * 32 + w * 4) = l;
#pragma unroll
      for (int et = 0; et < 6; ++et) {
        const v2u o = {cvtpk(acc[u][et][0] * f, acc[u][et][1] * f), cvtpk(acc[u][et][2] * f, acc[u][et][3] * f)};
        *(LAS v2u*)(ring + q * 208 + (16 * et + 4 * g) * 2) = o;
      }
    }
  }
  STAMP(16);
  BAR();
  if (tid < 384) {
    const f32x4 la = *(LAS f32x4*)(lds + L_LS + oq * 32), lb = *(LAS f32x4*)(lds + L_LS + oq * 32 + 16);
    const float inv = 1.f / (((la[0] + la[1]) + (la[2] + la[3])) + ((lb[0] + lb[1]) + (lb[2] + lb[3])));
    unsigned klo = 0x00003f80u, khi = 0x3f800000u;
    asm volatile("" : "+s"(klo), "+s"(khi));
    float o[8] = {};
#pragma unroll
    for (int k = 0; k < 8; ++k) {
      const v4u p = *(LAS v4u*)(lds + L_RING + k * RING + oq * 208 + oc * 16);
#pragma unroll
      for (int e = 0; e < 4; ++e) { o[2 * e] = dot2(p[e], klo, o[2 * e]); o[2 * e + 1] = dot2(p[e], khi, o[2 * e + 1]); }
    }
    v4u ov;
#pragma unroll
    for (int e = 0; e < 4; ++e) ov[e] = cvtpk(fmaf(o[2 * e], inv, bflo(xr[e])), fmaf(o[2 * e + 1], inv, bfhi(xr[e])));
    *(v4u*)((unsigned char*)out + ooff) = ov;
  }
  STAMP(17);
  STFLUSH();
}

extern "C" void kernel_launch(void* const* d_in, const int* in_sizes, int n_in, void* d_out, int out_size, void* d_ws, size_t ws_size,
                              hipStream_t stream) {
  (void)hipFuncSetAttribute((const void*)dit_kernel, hipFuncAttributeMaxDynamicSharedMemorySize, L_TOTAL);
  dit_kernel<<<256, 512, L_TOTAL, stream>>>((const u16*)d_in[0], (const u16*)d_in[1], (const u16*)d_in[2], (const u16*)d_in[3],
                                            (const u16*)d_in[4], (unsigned char*)d_ws, (u16*)d_out);
}
```

```cpp
#include <hip/hip_runtime.h>
#include <hip/hip_bf16.h>

typedef short bf16x8 __attribute__((ext_vector_type(8)));
typedef short s16x4 __attribute__((ext_vector_type(4)));
typedef float f32x4 __attribute__((ext_vector_type(4)));
typedef float f32x2 __attribute__((ext_vector_type(2)));
typedef unsigned v4u __attribute__((ext_vector_type(4)));
typedef unsigned v2u __attribute__((ext_vector_type(2)));
typedef __bf16 bf2 __attribute__((ext_vector_type(2)));
typedef unsigned short u16;
#define DI __device__ __forceinline__
#define LAS __attribute__((address_space(3)))
#define GAS __attribute__((address_space(1)))
typedef GAS unsigned gu32;
typedef LAS unsigned char* lp;
#define STDECL
#define STAMP(i)
#define STFLUSH()
#define RETRY(k)
#define DLY0 8
#define DLY1 0
#define DLY2 0
#define PRE 1
#define HP 4
#define MP 2
#define TOUCH 1
#ifndef NVS
#define NVS 2
#endif

constexpr int O_EPOCH = 0;
constexpr int O_MOD = 4096;
constexpr int O_CL = 16384;
constexpr int O_MV = 65536;
constexpr int O_ST = 131072;
constexpr int O_MT = 163840;
constexpr int O_W2 = O_MT + 294912;
constexpr int O_QF = O_W2 + 294912;
constexpr int O_VF = O_QF + 1572864;
constexpr int O_PF = O_VF + 1572864;
constexpr int O_RM = O_PF + 4194304;
constexpr int O_RQ = O_RM + 1152 * 128;
constexpr int O_RV = O_RQ + 1536 * 128;
constexpr int WS_BYTES = O_RV + 1536 * 128;
constexpr int OOB = 0x40000000;

constexpr int L_P0 = 0;
constexpr int L_MS = 0, L_QS = 24576, L_CS = 49152, L_SG = 50176, L_MX = 51200, L_LS = 52224, L_BN = 53248;
constexpr int L_RING = 61440;
constexpr int RING = 12288;
constexpr int L_TAG = L_RING + 8 * RING;
constexpr int L_TOTAL = L_TAG + 16;

constexpr int AUX_SC1 = 16;
constexpr unsigned SPIN_CAP = 1u << 16;

DI float bflo(unsigned v) { return __uint_as_float(v << 16); }
DI float bfhi(unsigned v) { return __uint_as_float(v & 0xffff0000u); }
DI unsigned cvtpk(float lo, float hi) {
  f32x2 x = {lo, hi};
  return __builtin_bit_cast(unsigned, __builtin_convertvector(x, bf2));
}
DI float bfround(float x) { return bflo(cvtpk(x, 0.f)); }
DI unsigned pkhi(unsigned lo, unsigned hi) { return __builtin_amdgcn_perm(hi, lo, 0x07060302u); }
DI unsigned tagged(float v, unsigned tag16) { return (cvtpk(0.f, v) & 0xffff0000u) | tag16; }
DI unsigned tchk(unsigned a, v4u v, unsigned tag) { return a | (v[0] ^ tag) | (v[1] ^ tag) | (v[2] ^ tag) | (v[3] ^ tag); }
DI unsigned sum4(unsigned a, v4u v) { return a + v[0] + v[1] + v[2] + v[3]; }
DI unsigned scan63(unsigned v) {
  v += __builtin_amdgcn_update_dpp(0u, v, 0x111, 0xf, 0xf, true);
  v += __builtin_amdgcn_update_dpp(0u, v, 0x112, 0xf, 0xf, true);
  v += __builtin_amdgcn_update_dpp(0u, v, 0x114, 0xf, 0xf, true);
  v += __builtin_amdgcn_update_dpp(0u, v, 0x118, 0xf, 0xf, true);
  v += __builtin_amdgcn_update_dpp(0u, v, 0x142, 0xa, 0xf, true);
  v += __builtin_amdgcn_update_dpp(0u, v, 0x143, 0xc, 0xf, true);
  return v;
}
DI float rsumf(float v) {
#define FSTEP(ctl) v += __uint_as_float(__builtin_amdgcn_update_dpp(0u, __float_as_uint(v), ctl, 0xf, 0xf, true))
  FSTEP(0x128); FSTEP(0x124); FSTEP(0x122); FSTEP(0x121);
#undef FSTEP
  return v;
}
DI unsigned wsum(unsigned v) { return __builtin_amdgcn_readlane(scan63(v), 63); }
DI float max3(float a, float b, float c) { float r; asm("v_max3_f32 %0, %1, %2, %3" : "=v"(r) : "v"(a), "v"(b), "v"(c)); return r; }
DI float max2(float a, float b) { float r; asm("v_max_f32 %0, %1, %2" : "=v"(r) : "v"(a), "v"(b)); return r; }
DI float dot2(unsigned a, unsigned b, float c) { return __builtin_amdgcn_fdot2_f32_bf16(__builtin_bit_cast(bf2, a), __builtin_bit_cast(bf2, b), c, false); }
DI f32x4 mfma(v4u a, v4u b, f32x4 c) {
  return __builtin_amdgcn_mfma_f32_16x16x32_bf16(__builtin_bit_cast(bf16x8, a), __builtin_bit_cast(bf16x8, b), c, 0, 0, 0);
}
DI float sum_g(float v) {
  auto r = __builtin_amdgcn_permlane16_swap(__float_as_uint(v), __float_as_uint(v), false, false);
  v = __uint_as_float(r[0]) + __uint_as_float(r[1]);
  auto q = __builtin_amdgcn_permlane32_swap(__float_as_uint(v), __float_as_uint(v), false, false);
  return __uint_as_float(q[0]) + __uint_as_float(q[1]);
}
DI float max_g(float v) {
  auto r = __builtin_amdgcn_permlane16_swap(__float_as_uint(v), __float_as_uint(v), false, false);
  v = max2(__uint_as_float(r[0]), __uint_as_float(r[1]));
  auto q = __builtin_amdgcn_permlane32_swap(__float_as_uint(v), __float_as_uint(v), false, false);
  return max2(__uint_as_float(q[0]), __uint_as_float(q[1]));
}
template <typename T> DI T opq(T v) { asm volatile("" : "+v"(v)); return v; }
#define BAR() do { asm volatile("s_waitcnt lgkmcnt(0)" ::: "memory"); __builtin_amdgcn_s_barrier(); asm volatile("" ::: "memory"); } while (0)
#define VMWAIT(n) asm volatile("s_waitcnt vmcnt(%0)" :: "i"(n) : "memory")
#define SB() __builtin_amdgcn_sched_barrier(0)
#define CBAR() asm volatile("" ::: "memory")
#define LD128(off, so) __builtin_amdgcn_raw_buffer_load_b128(rws, (off), (so), AUX_SC1)
#define LD64(off) __builtin_amdgcn_raw_buffer_load_b64(rws, (off), 0, AUX_SC1)
#define LD32(off) __builtin_amdgcn_raw_buffer_load_b32(rws, (off), 0, AUX_SC1)
#define ST128(v, off) __builtin_amdgcn_raw_buffer_store_b128((v), rws, (off), 0, AUX_SC1)
#define ST64(v, off) __builtin_amdgcn_raw_buffer_store_b64((v), rws, (off), 0, AUX_SC1)
#define ST32(v, off) __builtin_amdgcn_raw_buffer_store_b32((v), rws, (off), 0, AUX_SC1)
#define ST16(v, off) __builtin_amdgcn_raw_buffer_store_b16((v), rws, (off), 0, AUX_SC1)
#define LDS128(p, off) (*(LAS v4u*)((p) + (off)))
#define LDSF4(p, off) (*(LAS f32x4*)((p) + (off)))
DI void dma(unsigned ldsaddr, int voff, __amdgpu_buffer_rsrc_t rs, int soff) {
  asm volatile("s_mov_b32 m0, %0\n\ts_nop 0\n\tbuffer_load_dwordx4 %1, %2, %3 offen lds" :: "s"(ldsaddr), "v"(voff), "s"(rs), "s"(soff) : "memory");
}

DI void touch(unsigned ldsaddr, int voff, __amdgpu_buffer_rsrc_t rs) {
  asm volatile("s_mov_b32 m0, %0\n\ts_nop 0\n\tbuffer_load_dword %1, %2, 0 offen lds" :: "s"(ldsaddr), "v"(voff), "s"(rs) : "memory");
}
#define UNLIKELY(c) __builtin_expect(!!(c), 0)

__global__ __launch_bounds__(512, 2) void dit_kernel(const u16* __restrict__ x, const u16* __restrict__ c, const u16* __restrict__ wqkv,
                                                     const u16* __restrict__ wo, const u16* __restrict__ wmod, unsigned char* ws,
                                                     u16* __restrict__ out) {
  extern __shared__ __attribute__((aligned(16))) unsigned char lds_[];
  lp lds = (lp)lds_;
  const unsigned ldsb = (unsigned)(size_t)lds;
  STDECL;
  STAMP(0);
  const int tid = threadIdx.x, lane = tid & 63, w = __builtin_amdgcn_readfirstlane(tid >> 6);
  const int n16 = lane & 15, g = lane >> 4;
  const int bid = blockIdx.x, xg = bid & 7, r = bid >> 3;
  const int b = xg >> 2, ql = r & 7, kb = r >> 3;
  const int qt = b * 32 + (xg & 3) * 8 + ql;
  const bool act1 = ql < 6;
  const int cb = (xg & 3) * 6 + (act1 ? ql : ql - 2);
  const int prow = 8 * (n16 >> 2) + (n16 & 3);
  const __amdgpu_buffer_rsrc_t rws = __builtin_amdgcn_make_buffer_rsrc(ws, 0, WS_BYTES, 0x00020000);
  const __amdgpu_buffer_rsrc_t rx = __builtin_amdgcn_make_buffer_rsrc((void*)x, 0, 2048 * 768, 0x00020000);
  lp ring = lds + L_RING + w * RING;
  const int dch = 16 * ((lane & 3) ^ ((lane & 32) ? 3 : 0));
  const int rslot = (4 * n16 + (g ^ ((n16 & 8) ? 3 : 0))) * 16;

  const int mu = bid - 192;
  const int role = r < 12 ? 0 : (r < 24 ? 1 : (mu < 36 ? 2 : 3));
  const int ru = role == 1 ? r - 12 : r;
  const int u12 = 6 * (xg & 1) + ru % 6, u8 = 2 * (xg >> 1) + ru / 6;

  const int xv = (b * 1024 + kb * 256 + 32 * w + 8 * (lane >> 4) + ((lane >> 2) & 3)) * 768 + dch;
  auto xgroup = [&](int p) {
#pragma unroll
    for (int j = 0; j < 4; ++j) dma(ldsb + L_RING + w * RING + ((p % 3) * 4 + j) * 1024, xv, rx, (j >> 1) * 3072 + (2 * p + (j & 1)) * 64);
  };
  asm volatile("s_nop 4" ::: "memory");
  if (w < 6) {
    unsigned cv = 0;
    if (role == 2) cv = ((const unsigned*)c)[tid];
    if (role < 2) {
      const __amdgpu_buffer_rsrc_t rq = __builtin_amdgcn_make_buffer_rsrc((void*)wqkv, 0, 384 * 2304, 0x00020000);
      const int v = (lane >> 2) * 2304 + dch;
      const int s3 = 48 * u8 * 2304 + (role ? 1536 : 768) + 128 * w;
#pragma unroll
      for (int t = 0; t < 3; ++t) {
        dma(ldsb + L_P0 + (t * 12 + 2 * w) * 1024, v, rq, s3 + 16 * t * 2304);
        dma(ldsb + L_P0 + (t * 12 + 2 * w + 1) * 1024, v, rq, s3 + 16 * t * 2304 + 64);
      }
      if (role == 0) {
        const int s2 = 32 * u12 * 2304 + 128 * w;
#pragma unroll
        for (int t = 0; t < 2; ++t) {
          dma(ldsb + L_P0 + ((3 + t) * 12 + 2 * w) * 1024, v, rq, s2 + 16 * t * 2304);
          dma(ldsb + L_P0 + ((3 + t) * 12 + 2 * w + 1) * 1024, v, rq, s2 + 16 * t * 2304 + 64);
        }
      }
    }
    if (role == 1 || role == 2) {
      const int ld = role == 1 ? 768 : 4608;
      const __amdgpu_buffer_rsrc_t rc = __builtin_amdgcn_make_buffer_rsrc((void*)(role == 1 ? wo : wmod), 0, 384 * ld, 0x00020000);
      const int v = (lane >> 2) * ld + dch;
#pragma unroll
      for (int j = 0; j < 4; ++j) dma(ldsb + L_P0 + (36 + 4 * w + j) * 1024, v, rc, (role == 1 ? u12 : mu) * 64 + (4 * w + j) * 16 * ld);
    }
    if (role == 2) {
      const float f0 = bflo(cv), f1 = bfhi(cv);
      const int bb = tid >= 192, i2 = 2 * (tid - 192 * bb);
      *(LAS unsigned*)(lds + L_P0 + (i2 >> 5) * 1024 + (bb * 4 + ((i2 >> 3) & 3)) * 16 + (i2 & 7) * 2) =
          cvtpk(f0 * __builtin_amdgcn_rcpf(1.f + __expf(-f0)), f1 * __builtin_amdgcn_rcpf(1.f + __expf(-f1)));
    }
  } else {
    const unsigned scr = ldsb + L_RING + w * RING + 8192;
    if (TOUCH & 1) {
      const __amdgpu_buffer_rsrc_t rq = __builtin_amdgcn_make_buffer_rsrc((void*)wqkv, 0, 384 * 2304, 0x00020000);
      const __amdgpu_buffer_rsrc_t ro = __builtin_amdgcn_make_buffer_rsrc((void*)wo, 0, 384 * 768, 0x00020000);
      const int dead = lane < 36 ? 0 : OOB;
      if (w == 7) {
        touch(scr, dead + (96 * (xg >> 1) + 3 * r + lane / 12) * 2304 + 768 + (lane % 12) * 128, rq);
        touch(scr, dead + (12 * r + lane / 3) * 768 + 384 * (xg & 1) + (lane % 3) * 128, ro);
      } else touch(scr, dead + (192 * (xg & 1) + 6 * r + lane / 6) * 2304 + (lane % 6) * 128, rq);
    }
    const unsigned e1 = __hip_atomic_load((gu32*)(ws + O_EPOCH), __ATOMIC_RELAXED, __HIP_MEMORY_SCOPE_AGENT) + 1u;
    const int srow = b * 1024 + ((xg & 3) * 32 + r) * 8 + (w & 1) * 4 + g;
    v4u xs[3];
#pragma unroll
    for (int i = 0; i < 3; ++i) xs[i] = __builtin_amdgcn_raw_buffer_load_b128(rx, srow * 768 + n16 * 16 + i * 256, 0, 0);
    if (TOUCH & 2) {
#pragma unroll
      for (int i = 0; i < 2; ++i) touch(scr, (lane < 48 ? 0 : OOB) + b * 786432 + (r * 192 + ((w & 1) * 2 + i) * 48 + lane) * 128, rx);
    }
    float f[24], sm = 0.f, q = 0.f;
#pragma unroll
    for (int i = 0; i < 12; ++i) { f[2 * i] = bflo(xs[i >> 2][i & 3]); f[2 * i + 1] = bfhi(xs[i >> 2][i & 3]); sm += f[2 * i] + f[2 * i + 1]; }
    sm = rsumf(sm) * (1.f / 384.f);
#pragma unroll
    for (int i = 0; i < 24; ++i) { const float d = f[i] - sm; q = fmaf(d, d, q); }
    const float rs = __builtin_amdgcn_rsqf(rsumf(q) * (1.f / 384.f) + 1e-5f);
    ST128((v4u{__float_as_uint(rs), e1, __float_as_uint(sm), e1}), n16 == 0 ? O_ST + srow * 16 : OOB);
    *(LAS unsigned*)(lds + L_TAG) = e1;
  }
#pragma unroll
  for (int p = 0; p < PRE; ++p) xgroup(p);
  STAMP(1);
  if (w < 6) VMWAIT(4 * PRE);
  STAMP(2);
  BAR();
  STAMP(3);
  const unsigned tag = __builtin_amdgcn_readfirstlane(*(LAS unsigned*)(lds + L_TAG));
  const int l63 = lane == 63 ? 0 : OOB;
  if (w < (role == 2 ? 2 : 6) && role != 3) {
    const int t3 = w >> 1, t2 = w & 1;
    f32x4 acc = {};
    v4u a[12], bq[12];
    if (role == 0) {
      lp ap = opq(lds + L_P0 + (3 + t2) * 12288 + rslot), bp = opq(lds + L_P0 + t3 * 12288 + rslot);
#pragma unroll
      for (int k = 0; k < 12; ++k) { a[k] = LDS128(ap, k * 1024); bq[k] = LDS128(bp, k * 1024); }
    } else {
      lp ap = opq(lds + L_P0 + (role == 2 ? ((n16 & 1) * 4 + g) * 16 : t3 * 12288 + rslot));
      lp tb = opq(lds + L_P0 + 36864 + (8 * g + (n16 >> 2)) * 64 + 16 * ((2 * t2 + ((n16 >> 1) & 1)) ^ ((g & 1) ? 3 : 0)) + 8 * (n16 & 1));
#pragma unroll
      for (int k = 0; k < 12; ++k) {
        a[k] = LDS128(ap, k * 1024);
        const s16x4 lo = __builtin_amdgcn_ds_read_tr16_b64_v4i16((LAS s16x4*)(tb + k * 2048));
        const s16x4 hi = __builtin_amdgcn_ds_read_tr16_b64_v4i16((LAS s16x4*)(tb + k * 2048 + 256));
        bq[k] = __builtin_bit_cast(v4u, __builtin_shufflevector(lo, hi, 0, 1, 2, 3, 4, 5, 6, 7));
      }
    }
    SB();
#pragma unroll
    for (int k = 0; k < 12; ++k) acc = mfma(a[k], bq[k], acc);
    if (role != 2) {
      const int blk = role * 576 + (role == 0 ? (3 * u8 + t3) * 24 + 2 * u12 + t2 : (2 * u12 + t2) * 24 + 3 * u8 + t3);
      const unsigned o0 = cvtpk(acc[0], acc[1]), o1 = cvtpk(acc[2], acc[3]);
      auto r0 = __builtin_amdgcn_permlane16_swap(o0, o0, false, false);
      auto r1 = __builtin_amdgcn_permlane16_swap(o1, o1, false, false);
      ST128((v4u{r0[0], r1[0], r0[1], r1[1]}), ((g & 1) ? OOB : 0) + O_MT + blk * 512 + ((g >> 1) * 16 + n16) * 16);
      ST32(scan63(o0 + o1) + tag, l63 + O_RM + blk * 128);
    } else if (g < 2) {
      float v = bfround(g ? acc[1] : acc[0]);
      if (mu >= 12 && mu < 24) v += 1.f;
      ST32(tagged(v, tag & 0xffffu), O_MOD + (g * 1152 + 32 * mu + 16 * t2 + n16) * 4);
    }
  }
  if (tid == 448) __hip_atomic_store((gu32*)(ws + O_CL + bid * 128), tag, __ATOMIC_RELAXED, __HIP_MEMORY_SCOPE_AGENT);
  STAMP(4);
#pragma unroll
  for (int p = PRE; p < 3; ++p) xgroup(p);
  v4u xf[2][12];
  float rq[2], mq[2];
  f32x4 rk[2], mk[2];
  const v4u ones = {0x3f803f80u, 0x3f803f80u, 0x3f803f80u, 0x3f803f80u};
  lp fl = opq(lds + lane * 16);
  f32x4 s1o; float gate;
  {
    const int s0 = 3 * (w & 3);
    const int fb = (w >> 2) * 576 + cb * 24 + 2 * s0;
    const int mo = O_MOD + (b * 1152 + (lane < 24 ? 384 + 32 * s0 + 4 * lane : (lane < 48 ? 32 * s0 + 4 * (lane - 24) : ((lane & 4) ? 768 : 384) + 16 * cb + 4 * (lane & 3)))) * 4;
    const int ro = lane < 6 ? O_RM + (fb + lane) * 128 : OOB;
    const int fo = O_MT + fb * 512 + lane * 16;
    const int so = O_ST + (b * 1024 + kb * 256 + 32 * w + (lane & 31)) * 16;
    v4u mf[3], mv4, sv4; unsigned mr;
    f32x4 sa[3], sb[3]; v4u tf[3];
    lp rp = opq(ring + rslot);
    lp bn = lds + L_BN + w * 1024;
#define HOP1_LOADS() do { _Pragma("unroll") for (int i = 0; i < 3; ++i) mf[i] = LD128(fo + i * 1024, 0); mr = LD32(ro); } while (0)
#define MODOK() __all(((tchk(0u, mv4, tag) & 0xffffu) | (sv4[1] ^ tag) | (sv4[3] ^ tag)) == 0u)
#pragma unroll
    for (int p = 0; p < 6; ++p) {
      if (p < 4) VMWAIT(8 + (p > HP ? 4 : 0)); else if (p == 4) VMWAIT(4 + (p > HP ? 4 : 0)); else VMWAIT(4);
#pragma unroll
      for (int j = 0; j < 4; ++j) xf[j >> 1][2 * p + (j & 1)] = LDS128(rp, ((p % 3) * 4 + j) * 1024);
      if (p == MP) {
        asm volatile("buffer_load_dwordx4 %0, %1, %2, 0 offen sc1" : "=v"(mv4) : "v"(mo), "s"(rws) : "memory");
        asm volatile("buffer_load_dwordx4 %0, %1, %2, 0 offen sc1" : "=v"(sv4) : "v"(so), "s"(rws) : "memory");
      }
      if (p < 3) {
#pragma unroll
        for (int j = 0; j < 4; ++j) asm volatile("" : "+v"(xf[j >> 1][2 * p + (j & 1)]));
        xgroup(p + 3);
      }
      if (p == 2) { __builtin_amdgcn_s_barrier(); STAMP(5); }
      if (p == HP) { if (DLY1) __builtin_amdgcn_s_sleep(DLY1); HOP1_LOADS(); SB(); }
      if (p == 4) {
        asm volatile("" : "+v"(mv4), "+v"(sv4));
        if (UNLIKELY(!MODOK())) {
          unsigned it = 0;
          do { RETRY(0); __builtin_amdgcn_s_sleep(8); CBAR(); mv4 = LD128(mo, 0); sv4 = LD128(so, 0); } while (!MODOK() && ++it < SPIN_CAP);
        }
        if (lane >= 24 && lane < 48) *(LAS v2u*)(bn + 512 + (lane - 24) * 8) = v2u{pkhi(mv4[0], mv4[1]), pkhi(mv4[2], mv4[3])};
        else LDS128(bn, (lane < 24 ? lane : (lane & 7) + 24) * 16) = mv4 & 0xffff0000u;
        *(LAS unsigned*)(bn + 768 + lane * 4) = lane < 32 ? sv4[0] : sv4[2];
        CBAR();
#pragma unroll
        for (int i = 0; i < 3; ++i) {
          sa[i] = LDSF4(bn, (32 * i + 8 * g) * 4); sb[i] = LDSF4(bn, (32 * i + 8 * g) * 4 + 16);
          tf[i] = LDS128(bn, 512 + (32 * i + 8 * g) * 2);
        }
        s1o = LDSF4(bn, 384 + g * 16);
        gate = *(LAS float*)(bn + 448 + n16 * 4);
#pragma unroll
        for (int t = 0; t < 2; ++t) {
          rk[t] = LDSF4(bn, 768 + (8 * g + 4 * t) * 4); mk[t] = LDSF4(bn, 896 + (8 * g + 4 * t) * 4);
          rq[t] = *(LAS float*)(bn + 768 + (prow + 4 * t) * 4); mq[t] = *(LAS float*)(bn + 896 + (prow + 4 * t) * 4);
        }
      }
      SB();
    }
    STAMP(6);
    s1o *= 0.05103103630798288f * 1.4426950408889634f;
    s1o = opq(s1o);
    lp csw = opq(lds + L_CS + w * 128 + g * 16);
    lp msw = opq(fl + L_MS + 3 * w * 1024);
    STAMP(7);
#pragma unroll 1
    for (unsigned it = 0;; ++it) {
      f32x4 acs = {}, au = {};
#pragma unroll
      for (int i = 0; i < 3; ++i) {
        const v4u o = {cvtpk(bflo(mf[i][0]) * sa[i][0], bfhi(mf[i][0]) * sa[i][1]), cvtpk(bflo(mf[i][1]) * sa[i][2], bfhi(mf[i][1]) * sa[i][3]),
                       cvtpk(bflo(mf[i][2]) * sb[i][0], bfhi(mf[i][2]) * sb[i][1]), cvtpk(bflo(mf[i][3]) * sb[i][2], bfhi(mf[i][3]) * sb[i][3])};
        LDS128(msw, i * 1024) = o;
        au = mfma(mf[i], tf[i], au);
        acs = mfma(o, ones, acs);
      }
      const unsigned h = wsum(sum4(sum4(sum4(-mr, mf[0]), mf[1]), mf[2]));
      LDSF4(csw, 0) = acs; LDSF4(csw, 64) = au;
      if (h + 6u * tag == 0u || it >= SPIN_CAP) break;
      RETRY(1);
      __builtin_amdgcn_s_sleep(8);
      CBAR();
      HOP1_LOADS();
    }
  }
  const unsigned spat = (tag & 1u) ? 0x00008000u : 0x80000000u;
  const int oq = tid / 12, oc = tid % 12;
  const int ooff = ((qt * 32 + oq) * 384 + 96 * kb + 8 * oc) * 2;
  const v4u xr = __builtin_amdgcn_raw_buffer_load_b128(rx, tid < 384 ? ooff : 0, 0, 0);
  v4u qf[3]; unsigned qr;
  const int qlo = opq(O_QF + (qt * 24 + 3 * w) * 1024 + lane * 16);
  const int qro = opq(lane < 3 ? O_RQ + (qt * 24 + 3 * w + lane) * 128 : OOB);
#define HOP2_LOADS() do { _Pragma("unroll") for (int i = 0; i < 3; ++i) qf[i] = LD128(qlo + i * 1024, 0); qr = LD32(qro); } while (0)
  {
    const int blk = (b * 32 + kb * 8 + w) * 24 + cb;
    const int dead = act1 ? 0 : OOB;
    const int qso = opq(dead + O_QF + blk * 1024 + ((g & 1) * 32 + (g >> 1) * 16 + n16) * 16);
    const int vso = opq(dead + O_VF + blk * 1024 + lane * 16);
    const int rso = opq((dead | l63) + O_RQ + blk * 128);
    lp cg = opq(lds + L_CS + g * 16), cn = opq(lds + L_CS + 512 + n16 * 4);
    STAMP(9);
    __builtin_amdgcn_s_setprio(2);
    BAR();
    v4u a[12], bb[12];
    f32x4 cs[4], uu[4]; float c2[4], zz[4];
#pragma unroll
    for (int s = 0; s < 12; ++s) a[s] = LDS128(fl, L_MS + s * 1024);
    SB();
    f32x4 aq[2] = {}, av[2] = {};
#pragma unroll
    for (int s = 0; s < 4; ++s) { aq[0] = mfma(a[s], xf[0][s], aq[0]); aq[1] = mfma(a[s], xf[1][s], aq[1]); }
    SB();
#pragma unroll
    for (int k = 0; k < 4; ++k) { cs[k] = LDSF4(cg, k * 128); uu[k] = LDSF4(cg, k * 128 + 64); }
    SB();
#pragma unroll
    for (int s = 4; s < 8; ++s) { aq[0] = mfma(a[s], xf[0][s], aq[0]); aq[1] = mfma(a[s], xf[1][s], aq[1]); }
    SB();
    cs[0] = (cs[0] + cs[1]) + (cs[2] + cs[3]); uu[0] = ((uu[0] + uu[1]) + (uu[2] + uu[3])) * s1o;
    f32x4 ka[2], kc[2];
#pragma unroll
    for (int t = 0; t < 2; ++t) { ka[t] = s1o * rq[t]; kc[t] = uu[0] - cs[0] * ka[t] * mq[t]; }
#pragma unroll
    for (int s = 8; s < 12; ++s) { aq[0] = mfma(a[s], xf[0][s], aq[0]); aq[1] = mfma(a[s], xf[1][s], aq[1]); }
    SB();
#pragma unroll
    for (int s = 0; s < 4; ++s) bb[s] = LDS128(fl, L_MS + (12 + s) * 1024);
    SB();
    {
      const f32x4 v0 = aq[0] * ka[0] + kc[0], v1 = aq[1] * ka[1] + kc[1];
      auto r0 = __builtin_amdgcn_permlane16_swap(cvtpk(v0[0], v0[1]), cvtpk(v1[0], v1[1]), false, false);
      auto r1 = __builtin_amdgcn_permlane16_swap(cvtpk(v0[2], v0[3]), cvtpk(v1[2], v1[3]), false, false);
      const v4u ov = {r0[0], r1[0], r0[1], r1[1]};
      ST128(ov, qso);
      ST32(scan63(sum4(0u, ov)) + tag, rso);
    }
    __builtin_amdgcn_s_setprio(0);
    STAMP(10);
    SB();
#pragma unroll
    for (int s = 4; s < 12; ++s) bb[s] = LDS128(fl, L_MS + (12 + s) * 1024);
    SB();
#pragma unroll
    for (int s = 0; s < 4; ++s) { av[0] = mfma(xf[0][s], bb[s], av[0]); av[1] = mfma(xf[1][s], bb[s], av[1]); }
    SB();
#pragma unroll
    for (int k = 0; k < 4; ++k) { c2[k] = *(LAS float*)(cn + k * 128); zz[k] = *(LAS float*)(cn + k * 128 + 64); }
    SB();
#pragma unroll
    for (int s = 4; s < 8; ++s) { av[0] = mfma(xf[0][s], bb[s], av[0]); av[1] = mfma(xf[1][s], bb[s], av[1]); }
    SB();
    const float c2s = (c2[0] + c2[1]) + (c2[2] + c2[3]), zs = ((zz[0] + zz[1]) + (zz[2] + zz[3])) * gate;
#pragma unroll
    for (int t = 0; t < 2; ++t) { ka[t] = rk[t] * gate; kc[t] = zs - ka[t] * mk[t] * c2s; }
#pragma unroll
    for (int s = 8; s < 12; ++s) { av[0] = mfma(xf[0][s], bb[s], av[0]); av[1] = mfma(xf[1][s], bb[s], av[1]); }
    SB();
    if (DLY2) __builtin_amdgcn_s_sleep(DLY2);
    HOP2_LOADS();
    SB();
    const f32x4 v0 = av[0] * ka[0] + kc[0], v1 = av[1] * ka[1] + kc[1];
    const v4u o = {cvtpk(v0[0], v0[1]), cvtpk(v0[2], v0[3]), cvtpk(v1[0], v1[1]), cvtpk(v1[2], v1[3])};
    ST128(o, vso);
    ST32(scan63(sum4(0u, o)) + tag, rso + (O_RV - O_RQ));
  }
  STAMP(11);
  const int vo = opq(O_VF + ((b * 32 + 4 * w) * 24 + 6 * kb) * 1024 + lane * 16);
  const int vro = opq(lane < 24 ? O_RV + ((b * 32 + 4 * w + lane / 6) * 24 + 6 * kb + lane % 6) * 128 : OOB);
  const int plo = opq(O_PF + (qt * 32 + 4 * w) * 2048 + lane * 16);
  const int pso = opq(O_PF + (qt * 32 + kb * 8 + w) * 2048 + lane * 16);
  const int mso = opq((w == 0 && g < 2) ? O_MV + ((qt * 4 + kb) * 32 + lane) * 8 : OOB);
  lp mxw = opq(lds + L_MX + n16 * 32 + w * 4), mxr = opq(lds + L_MX + n16 * 32), sgr = opq(lds + L_SG + n16 * 32);
  {
    lp qw = opq(lds + L_QS + (lane >> 5) * 12288 + 3 * w * 512 + (lane & 31) * 16);
    lp sgw = opq(lds + L_SG + ((g >> 1) * 16 + n16) * 32 + w * 4);
    const unsigned selv = ((n16 >> 3) == (g >> 1)) ? 0x3f803f80u : 0u;
    const v4u sel = {selv, selv, selv, selv};
    SB();
#define QBAD() (wsum(sum4(sum4(sum4(-qr, qf[0]), qf[1]), qf[2])) + 3u * tag != 0u)
    if (UNLIKELY(QBAD())) {
      unsigned it = 0;
      do { RETRY(2); __builtin_amdgcn_s_sleep(3); CBAR(); HOP2_LOADS(); } while (QBAD() && ++it < SPIN_CAP);
    }
    STAMP(12);
    f32x4 sg = {};
#pragma unroll
    for (int i = 0; i < 3; ++i) {
      sg = mfma(sel, qf[i], sg);
      LDS128(qw, i * 512) = qf[i];
    }
    *(LAS float*)(sgw) = sg[0];
  }
  v4u vf[4][6];
#define VLOAD(i) vf[(i) / 6][(i) % 6] = LD128(vo + ((i) % 6 & 3) * 1024, (((i) / 6) * 24 + ((i) % 6 & 4)) * 1024)
  STAMP(13);
  BAR();
  if (w >= 4) {
#pragma unroll
    for (int i = 0; i < 12; ++i) VLOAD(i);
  }
  SB();
  {
    v4u qa[6], qb[6];
    f32x4 sp[4];
#pragma unroll
    for (int i = 0; i < 6; ++i) qa[i] = LDS128(fl, L_QS + ((i & 1) * 12 + (i >> 1)) * 1024);
#pragma unroll
    for (int i = 0; i < 6; ++i) qb[i] = LDS128(fl, L_QS + ((i & 1) * 12 + 3 + (i >> 1)) * 1024);
    SB();
    f32x4 acc[2][2] = {};
#define SSTAGE(Q, S0) _Pragma("unroll") for (int i = 0; i < 6; ++i) { acc[i & 1][0] = mfma(xf[0][(S0) + (i >> 1)], Q[i], acc[i & 1][0]); acc[i & 1][1] = mfma(xf[1][(S0) + (i >> 1)], Q[i], acc[i & 1][1]); }
    SSTAGE(qa, 0);
    SB();
#pragma unroll
    for (int i = 0; i < 6; ++i) qa[i] = LDS128(fl, L_QS + ((i & 1) * 12 + 6 + (i >> 1)) * 1024);
    SB();
    SSTAGE(qb, 3);
    SB();
#pragma unroll
    for (int i = 0; i < 6; ++i) qb[i] = LDS128(fl, L_QS + ((i & 1) * 12 + 9 + (i >> 1)) * 1024);
#pragma unroll
    for (int i = 0; i < 4; ++i) sp[i] = LDSF4(sgr, (i >> 1) * 512 + (i & 1) * 16);
    SB();
    SSTAGE(qa, 6);
    SB();
    sp[0] += sp[1]; sp[2] += sp[3];
    const float sig0 = -((sp[0][0] + sp[0][1]) + (sp[0][2] + sp[0][3])), sig1 = -((sp[2][0] + sp[2][1]) + (sp[2][2] + sp[2][3]));
    const f32x4 mr0 = mk[0] * rk[0], mr1 = mk[1] * rk[1];
    const f32x4 kc[2][2] = {{mr0 * sig0, mr1 * sig0}, {mr0 * sig1, mr1 * sig1}};
    SSTAGE(qb, 9);
    SB();
#pragma unroll
    for (int u = 0; u < 2; ++u) {
      acc[u][0] = acc[u][0] * rk[0] + kc[u][0]; acc[u][1] = acc[u][1] * rk[1] + kc[u][1];
      float mx = max3(max3(acc[u][0][0], acc[u][0][1], acc[u][0][2]), acc[u][0][3], acc[u][1][0]);
      mx = max_g(max2(max3(mx, acc[u][1][1], acc[u][1][2]), acc[u][1][3]));
      *(LAS float*)(mxw + u * 512) = mx;
    }
    SB();
    STAMP(14);
    if (w < 4) {
#pragma unroll
      for (int i = 0; i < 12; ++i) VLOAD(i);
    }
    BAR();
    f32x4 ma[2], mb[2];
#pragma unroll
    for (int u = 0; u < 2; ++u) { ma[u] = LDSF4(mxr, u * 512); mb[u] = LDSF4(mxr, u * 512 + 16); }
    SB();
    float m[2];
#pragma unroll
    for (int u = 0; u < 2; ++u) {
      m[u] = max2(max3(max3(ma[u][0], ma[u][1], ma[u][2]), ma[u][3], mb[u][0]), max3(mb[u][1], mb[u][2], mb[u][3]));
      float p[8];
#pragma unroll
      for (int i = 0; i < 8; ++i) p[i] = __builtin_amdgcn_exp2f(acc[u][i >> 2][i & 3] - m[u]);
      const v4u o = (v4u{cvtpk(p[0], p[1]), cvtpk(p[2], p[3]), cvtpk(p[4], p[5]), cvtpk(p[6], p[7])} & 0x7fff7fffu) | spat;
      ST128(o, pso + u * 1024);
    }
    ST64((v2u{__float_as_uint(g ? m[1] : m[0]), tag}), mso);
  }
  __builtin_amdgcn_s_barrier();
  STAMP(15);
  {
    f32x4 acc[2][6] = {};
    float ls[2] = {0.f, 0.f};
    v2u mv[2]; unsigned vr;
    unsigned one2 = 0x3f803f80u;
    asm volatile("" : "+s"(one2));
    v4u pf[4][2];
    const int mlo = O_MV + ((qt * 4 + g) * 32 + n16) * 8;
#define PLOAD(s) _Pragma("unroll") for (int u_ = 0; u_ < 2; ++u_) pf[s][u_] = LD128(plo + (((s) & 1) * 2 + u_) * 1024, ((s) >> 1) * 4096)
#define VSUM(H, OUT) do { unsigned h_ = (lane < 12) == ((H) == 0) ? -vr : 0u; \
      _Pragma("unroll") for (int i = 12 * (H); i < 12 * (H) + 12; ++i) h_ = sum4(h_, vf[i / 6][i % 6]); \
      OUT = wsum(h_) + 12u * tag; } while (0)
#define VCHECK(H) do { unsigned bad_; VSUM(H, bad_); \
      if (UNLIKELY(bad_ != 0u)) { unsigned it = 0; \
        do { RETRY(3 + (H)); __builtin_amdgcn_s_sleep(2); CBAR(); \
          _Pragma("unroll") for (int i = 12 * (H); i < 12 * (H) + 12; ++i) VLOAD(i); \
          vr = LD32(vro); VSUM(H, bad_); } while (bad_ != 0u && ++it < SPIN_CAP); } } while (0)
#define POK(s) (pf[s][0] ^= spat, pf[s][1] ^= spat, __all((tchk(tchk(0u, pf[s][0], 0u), pf[s][1], 0u) & 0x80008000u) == 0u))
#define PSTEP(s) do { if (UNLIKELY(!POK(s))) { unsigned it = 0; \
        do { RETRY(5 + (s)); __builtin_amdgcn_s_sleep(2); CBAR(); PLOAD(s); } while (!POK(s) && ++it < SPIN_CAP); } \
      _Pragma("unroll") for (int u = 0; u < 2; ++u) { \
        _Pragma("unroll") for (int et = 0; et < 6; ++et) acc[u][et] = mfma(vf[s][et], pf[s][u], acc[u][et]); \
        _Pragma("unroll") for (int e = 0; e < 4; ++e) ls[u] = dot2(pf[s][u][e], one2, ls[u]); } \
      asm volatile("" : "+v"(ls[0]), "+v"(ls[1])); } while (0)
    vr = LD32(vro);
    SB();
#pragma unroll
    for (int i = 12; i < 18; ++i) VLOAD(i);
    PLOAD(0); PLOAD(1);
    SB();
    STAMP(16);
    VCHECK(0);
#pragma unroll
    for (int i = 18; i < 24; ++i) VLOAD(i);
    mv[0] = LD64(mlo); mv[1] = LD64(mlo + 128);
    SB();
    PSTEP(0);
    PLOAD(2); PLOAD(3);
    SB();
    PSTEP(1);
    VCHECK(1);
    if (UNLIKELY(!__all(mv[0][1] == tag && mv[1][1] == tag))) {
      unsigned it = 0;
      do { RETRY(9); CBAR(); mv[0] = LD64(mlo); mv[1] = LD64(mlo + 128); } while (!__all(mv[0][1] == tag && mv[1][1] == tag) && ++it < SPIN_CAP);
    }
    float f[2];
#pragma unroll
    for (int u = 0; u < 2; ++u) {
      const float mo_ = __uint_as_float(__builtin_amdgcn_ds_bpermute(((w >> 1) * 16 + n16) * 4, mv[u][0]));
      f[u] = opq(__builtin_amdgcn_exp2f(mo_ - max_g(__uint_as_float(mv[u][0]))));
    }
    lp pw = opq(ring + prow * 208 + 8 * g), lw = opq(lds + L_LS + prow * 32 + w * 4);
    PSTEP(2);
    PSTEP(3);
    STAMP(17);
#pragma unroll
    for (int u = 0; u < 2; ++u) {
      *(LAS float*)(lw + u * 128) = sum_g(ls[u]) * f[u];
#pragma unroll
      for (int et = 0; et < 6; ++et) {
        const f32x4 t = acc[u][et] * f[u];
        *(LAS v2u*)(pw + u * 832 + 32 * et) = v2u{cvtpk(t[0], t[1]), cvtpk(t[2], t[3])};
      }
    }
  }
  STAMP(18);
  BAR();
  if (tid < 384) {
    lp pb = opq(lds + L_RING + oq * 208 + oc * 16), pb2 = opq(pb + 4 * RING);
    v4u p[8];
#pragma unroll
    for (int k = 0; k < 4; ++k) { p[k] = LDS128(pb, k * RING); p[4 + k] = LDS128(pb2, k * RING); }
    const f32x4 la = LDSF4(lds, L_LS + oq * 32), lb = LDSF4(lds, L_LS + oq * 32 + 16);
    SB();
    unsigned klo = 0x00003f80u, khi = 0x3f800000u;
    asm volatile("" : "+s"(klo), "+s"(khi));
    float o[8] = {};
#pragma unroll
    for (int k = 0; k < 8; ++k)
#pragma unroll
      for (int e = 0; e < 4; ++e) { o[2 * e] = dot2(p[k][e], klo, o[2 * e]); o[2 * e + 1] = dot2(p[k][e], khi, o[2 * e + 1]); }
    const float inv = __builtin_amdgcn_rcpf(((la[0] + la[1]) + (la[2] + la[3])) + ((lb[0] + lb[1]) + (lb[2] + lb[3])));
    v4u ov;
#pragma unroll
    for (int e = 0; e < 4; ++e) ov[e] = cvtpk(fmaf(o[2 * e], inv, bflo(xr[e])), fmaf(o[2 * e + 1], inv, bfhi(xr[e])));
    *(v4u*)((unsigned char*)out + ooff) = ov;
  } else if (bid == 0 && w == 7) {
#pragma unroll 1
    for (unsigned it = 0; it < SPIN_CAP; ++it) {
      CBAR();
      unsigned bad = 0;
#pragma unroll
      for (int k = 0; k < 4; ++k) bad |= LD32(O_CL + (k * 64 + lane) * 128) ^ tag;
      if (__all(bad == 0)) break;
    }
    if (lane == 0) __hip_atomic_store((gu32*)(ws + O_EPOCH), tag, __ATOMIC_RELAXED, __HIP_MEMORY_SCOPE_AGENT);
  }
  STAMP(19);
  STFLUSH();
}

extern "C" void kernel_launch(void* const* d_in, const int* in_sizes, int n_in, void* d_out, int out_size, void* d_ws, size_t ws_size,
                              hipStream_t stream) {
  (void)hipFuncSetAttribute((const void*)dit_kernel, hipFuncAttributeMaxDynamicSharedMemorySize, L_TOTAL);
  dit_kernel<<<256, 512, L_TOTAL, stream>>>((const u16*)d_in[0], (const u16*)d_in[1], (const u16*)d_in[2], (const u16*)d_in[3],
                                            (const u16*)d_in[4], (unsigned char*)d_ws, (u16*)d_out);
}
```

```cpp
#include <hip/hip_runtime.h>
#include <hip/hip_bf16.h>

typedef short bf16x8 __attribute__((ext_vector_type(8)));
typedef short s16x4 __attribute__((ext_vector_type(4)));
typedef float f32x4 __attribute__((ext_vector_type(4)));
typedef float f32x2 __attribute__((ext_vector_type(2)));
typedef unsigned v4u __attribute__((ext_vector_type(4)));
typedef unsigned v2u __attribute__((ext_vector_type(2)));
typedef __bf16 bf2 __attribute__((ext_vector_type(2)));
typedef unsigned short u16;
#define DI __device__ __forceinline__
#define LAS __attribute__((address_space(3)))
#define GAS __attribute__((address_space(1)))
typedef GAS unsigned gu32;
typedef LAS unsigned char* lp;
#define STDECL
#define STAMP(i)
#define STFLUSH()
#define RETRY(k)
#define DLY0 8
#define DLY1 0
#define DLY2 0
#define PRE 1
#define HP 4
#define MP 2
#define TOUCH 1
#define CODEPF 17408
#ifndef NVS
#define NVS 2
#endif

constexpr int O_EPOCH = 0;
constexpr int O_MOD = 4096;
constexpr int O_CL = 16384;
constexpr int O_MV = 65536;
constexpr int O_ST = 131072;
constexpr int O_MT = 163840;
constexpr int O_W2 = O_MT + 294912;
constexpr int O_QF = O_W2 + 294912;
constexpr int O_VF = O_QF + 1572864;
constexpr int O_PF = O_VF + 1572864;
constexpr int O_RM = O_PF + 4194304;
constexpr int O_RQ = O_RM + 1152 * 128;
constexpr int O_RV = O_RQ + 1536 * 128;
constexpr int WS_BYTES = O_RV + 1536 * 128;
constexpr int OOB = 0x40000000;

constexpr int L_P0 = 0;
constexpr int L_MS = 0, L_QS = 24576, L_CS = 49152, L_SG = 50176, L_MX = 51200, L_LS = 52224, L_BN = 53248;
constexpr int L_RING = 61440;
constexpr int RING = 12288;
constexpr int L_TAG = L_RING + 8 * RING;
constexpr int L_TOTAL = L_TAG + 16;

constexpr int AUX_SC1 = 16;
constexpr unsigned SPIN_CAP = 1u << 16;

DI float bflo(unsigned v) { return __uint_as_float(v << 16); }
DI float bfhi(unsigned v) { return __uint_as_float(v & 0xffff0000u); }
DI unsigned cvtpk(float lo, float hi) {
  f32x2 x = {lo, hi};
  return __builtin_bit_cast(unsigned, __builtin_convertvector(x, bf2));
}
DI float bfround(float x) { return bflo(cvtpk(x, 0.f)); }
DI unsigned pkhi(unsigned lo, unsigned hi) { return __builtin_amdgcn_perm(hi, lo, 0x07060302u); }
DI unsigned tagged(float v, unsigned tag16) { return (cvtpk(0.f, v) & 0xffff0000u) | tag16; }
DI unsigned tchk(unsigned a, v4u v, unsigned tag) { return a | (v[0] ^ tag) | (v[1] ^ tag) | (v[2] ^ tag) | (v[3] ^ tag); }
DI unsigned sum4(unsigned a, v4u v) { return a + v[0] + v[1] + v[2] + v[3]; }
DI unsigned scan63(unsigned v) {
  v += __builtin_amdgcn_update_dpp(0u, v, 0x111, 0xf, 0xf, true);
  v += __builtin_amdgcn_update_dpp(0u, v, 0x112, 0xf, 0xf, true);
  v += __builtin_amdgcn_update_dpp(0u, v, 0x114, 0xf, 0xf, true);
  v += __builtin_amdgcn_update_dpp(0u, v, 0x118, 0xf, 0xf, true);
  v += __builtin_amdgcn_update_dpp(0u, v, 0x142, 0xa, 0xf, true);
  v += __builtin_amdgcn_update_dpp(0u, v, 0x143, 0xc, 0xf, true);
  return v;
}
DI float rsumf(float v) {
#define FSTEP(ctl) v += __uint_as_float(__builtin_amdgcn_update_dpp(0u, __float_as_uint(v), ctl, 0xf, 0xf, true))
  FSTEP(0x128); FSTEP(0x124); FSTEP(0x122); FSTEP(0x121);
#undef FSTEP
  return v;
}
DI unsigned wsum(unsigned v) { return __builtin_amdgcn_readlane(scan63(v), 63); }
DI float max3(float a, float b, float c) { float r; asm("v_max3_f32 %0, %1, %2, %3" : "=v"(r) : "v"(a), "v"(b), "v"(c)); return r; }
DI float max2(float a, float b) { float r; asm("v_max_f32 %0, %1, %2" : "=v"(r) : "v"(a), "v"(b)); return r; }
DI float dot2(unsigned a, unsigned b, float c) { return __builtin_amdgcn_fdot2_f32_bf16(__builtin_bit_cast(bf2, a), __builtin_bit_cast(bf2, b), c, false); }
DI f32x4 mfma(v4u a, v4u b, f32x4 c) {
  return __builtin_amdgcn_mfma_f32_16x16x32_bf16(__builtin_bit_cast(bf16x8, a), __builtin_bit_cast(bf16x8, b), c, 0, 0, 0);
}
DI float sum_g(float v) {
  auto r = __builtin_amdgcn_permlane16_swap(__float_as_uint(v), __float_as_uint(v), false, false);
  v = __uint_as_float(r[0]) + __uint_as_float(r[1]);
  auto q = __builtin_amdgcn_permlane32_swap(__float_as_uint(v), __float_as_uint(v), false, false);
  return __uint_as_float(q[0]) + __uint_as_float(q[1]);
}
DI float max_g(float v) {
  auto r = __builtin_amdgcn_permlane16_swap(__float_as_uint(v), __float_as_uint(v), false, false);
  v = max2(__uint_as_float(r[0]), __uint_as_float(r[1]));
  auto q = __builtin_amdgcn_permlane32_swap(__float_as_uint(v), __float_as_uint(v), false, false);
  return max2(__uint_as_float(q[0]), __uint_as_float(q[1]));
}
template <typename T> DI T opq(T v) { asm volatile("" : "+v"(v)); return v; }
#define BAR() do { asm volatile("s_waitcnt lgkmcnt(0)" ::: "memory"); __builtin_amdgcn_s_barrier(); asm volatile("" ::: "memory"); } while (0)
#define VMWAIT(n) asm volatile("s_waitcnt vmcnt(%0)" :: "i"(n) : "memory")
#define SB() __builtin_amdgcn_sched_barrier(0)
#define CBAR() asm volatile("" ::: "memory")
#define LD128(off, so) __builtin_amdgcn_raw_buffer_load_b128(rws, (off), (so), AUX_SC1)
#define LD64(off) __builtin_amdgcn_raw_buffer_load_b64(rws, (off), 0, AUX_SC1)
#define LD32(off) __builtin_amdgcn_raw_buffer_load_b32(rws, (off), 0, AUX_SC1)
#define ST128(v, off) __builtin_amdgcn_raw_buffer_store_b128((v), rws, (off), 0, AUX_SC1)
#define ST64(v, off) __builtin_amdgcn_raw_buffer_store_b64((v), rws, (off), 0, AUX_SC1)
#define ST32(v, off) __builtin_amdgcn_raw_buffer_store_b32((v), rws, (off), 0, AUX_SC1)
#define ST16(v, off) __builtin_amdgcn_raw_buffer_store_b16((v), rws, (off), 0, AUX_SC1)
#define LDS128(p, off) (*(LAS v4u*)((p) + (off)))
#define LDSF4(p, off) (*(LAS f32x4*)((p) + (off)))
DI void dma(unsigned ldsaddr, int voff, __amdgpu_buffer_rsrc_t rs, int soff) {
  asm volatile("s_mov_b32 m0, %0\n\ts_nop 0\n\tbuffer_load_dwordx4 %1, %2, %3 offen lds" :: "s"(ldsaddr), "v"(voff), "s"(rs), "s"(soff) : "memory");
}

DI void touch(unsigned ldsaddr, int voff, __amdgpu_buffer_rsrc_t rs) {
  asm volatile("s_mov_b32 m0, %0\n\ts_nop 0\n\tbuffer_load_dword %1, %2, 0 offen lds" :: "s"(ldsaddr), "v"(voff), "s"(rs) : "memory");
}
#define UNLIKELY(c) __builtin_expect(!!(c), 0)

__global__ __launch_bounds__(512, 2) void dit_kernel(const u16* __restrict__ x, const u16* __restrict__ c, const u16* __restrict__ wqkv,
                                                     const u16* __restrict__ wo, const u16* __restrict__ wmod, unsigned char* ws,
                                                     u16* __restrict__ out) {
  extern __shared__ __attribute__((aligned(16))) unsigned char lds_[];
  lp lds = (lp)lds_;
  const unsigned ldsb = (unsigned)(size_t)lds;
  STDECL;
  STAMP(0);
  const unsigned long long pc0 = __builtin_amdgcn_s_getpc();
  const int tid = threadIdx.x, lane = tid & 63, w = __builtin_amdgcn_readfirstlane(tid >> 6);
  const int n16 = lane & 15, g = lane >> 4;
  const int bid = blockIdx.x, xg = bid & 7, r = bid >> 3;
  const int b = xg >> 2, ql = r & 7, kb = r >> 3;
  const int qt = b * 32 + (xg & 3) * 8 + ql;
  const bool act1 = ql < 6;
  const int cb = (xg & 3) * 6 + (act1 ? ql : ql - 2);
  const int prow = 8 * (n16 >> 2) + (n16 & 3);
  const __amdgpu_buffer_rsrc_t rws = __builtin_amdgcn_make_buffer_rsrc(ws, 0, WS_BYTES, 0x00020000);
  const __amdgpu_buffer_rsrc_t rx = __builtin_amdgcn_make_buffer_rsrc((void*)x, 0, 2048 * 768, 0x00020000);
  lp ring = lds + L_RING + w * RING;
  const int dch = 16 * ((lane & 3) ^ ((lane & 32) ? 3 : 0));
  const int rslot = (4 * n16 + (g ^ ((n16 & 8) ? 3 : 0))) * 16;

  const int mu = bid - 192;
  const int role = r < 12 ? 0 : (r < 24 ? 1 : (mu < 36 ? 2 : 3));
  const int ru = role == 1 ? r - 12 : r;
  const int u12 = 6 * (xg & 1) + ru % 6, u8 = 2 * (xg >> 1) + ru / 6;

  const int xv = (b * 1024 + kb * 256 + 32 * w + 8 * (lane >> 4) + ((lane >> 2) & 3)) * 768 + dch;
  auto xgroup = [&](int p) {
#pragma unroll
    for (int j = 0; j < 4; ++j) dma(ldsb + L_RING + w * RING + ((p % 3) * 4 + j) * 1024, xv, rx, (j >> 1) * 3072 + (2 * p + (j & 1)) * 64);
  };
  asm volatile("s_nop 4" ::: "memory");
  if (w < 6) {
    unsigned cv = 0;
    if (role == 2) cv = ((const unsigned*)c)[tid];
    if (role < 2) {
      const __amdgpu_buffer_rsrc_t rq = __builtin_amdgcn_make_buffer_rsrc((void*)wqkv, 0, 384 * 2304, 0x00020000);
      const int v = (lane >> 2) * 2304 + dch;
      const int s3 = 48 * u8 * 2304 + (role ? 1536 : 768) + 128 * w;
#pragma unroll
      for (int t = 0; t < 3; ++t) {
        dma(ldsb + L_P0 + (t * 12 + 2 * w) * 1024, v, rq, s3 + 16 * t * 2304);
        dma(ldsb + L_P0 + (t * 12 + 2 * w + 1) * 1024, v, rq, s3 + 16 * t * 2304 + 64);
      }
      if (role == 0) {
        const int s2 = 32 * u12 * 2304 + 128 * w;
#pragma unroll
        for (int t = 0; t < 2; ++t) {
          dma(ldsb + L_P0 + ((3 + t) * 12 + 2 * w) * 1024, v, rq, s2 + 16 * t * 2304);
          dma(ldsb + L_P0 + ((3 + t) * 12 + 2 * w + 1) * 1024, v, rq, s2 + 16 * t * 2304 + 64);
        }
      }
    }
    if (role == 1 || role == 2) {
      const int ld = role == 1 ? 768 : 4608;
      const __amdgpu_buffer_rsrc_t rc = __builtin_amdgcn_make_buffer_rsrc((void*)(role == 1 ? wo : wmod), 0, 384 * ld, 0x00020000);
      const int v = (lane >> 2) * ld + dch;
#pragma unroll
      for (int j = 0; j < 4; ++j) dma(ldsb + L_P0 + (36 + 4 * w + j) * 1024, v, rc, (role == 1 ? u12 : mu) * 64 + (4 * w + j) * 16 * ld);
    }
    if (role == 2) {
      const float f0 = bflo(cv), f1 = bfhi(cv);
      const int bb = tid >= 192, i2 = 2 * (tid - 192 * bb);
      *(LAS unsigned*)(lds + L_P0 + (i2 >> 5) * 1024 + (bb * 4 + ((i2 >> 3) & 3)) * 16 + (i2 & 7) * 2) =
          cvtpk(f0 * __builtin_amdgcn_rcpf(1.f + __expf(-f0)), f1 * __builtin_amdgcn_rcpf(1.f + __expf(-f1)));
    }
  } else {
    const unsigned scr = ldsb + L_RING + w * RING + 8192;
    if (w == 6) {
      const __amdgpu_buffer_rsrc_t rpc = __builtin_amdgcn_make_buffer_rsrc((void*)(pc0 & ~127ull), 0, CODEPF, 0x00020000);
      touch(scr, lane < 5 ? (r * 5 + lane) * 128 : OOB, rpc);
    }
    if (TOUCH & 1) {
      const __amdgpu_buffer_rsrc_t rq = __builtin_amdgcn_make_buffer_rsrc((void*)wqkv, 0, 384 * 2304, 0x00020000);
      const __amdgpu_buffer_rsrc_t ro = __builtin_amdgcn_make_buffer_rsrc((void*)wo, 0, 384 * 768, 0x00020000);
      const int dead = lane < 36 ? 0 : OOB;
      if (w == 7) {
        touch(scr, dead + (96 * (xg >> 1) + 3 * r + lane / 12) * 2304 + 768 + (lane % 12) * 128, rq);
        touch(scr, dead + (12 * r + lane / 3) * 768 + 384 * (xg & 1) + (lane % 3) * 128, ro);
      } else touch(scr, dead + (192 * (xg & 1) + 6 * r + lane / 6) * 2304 + (lane % 6) * 128, rq);
    }
    const unsigned e1 = __hip_atomic_load((gu32*)(ws + O_EPOCH), __ATOMIC_RELAXED, __HIP_MEMORY_SCOPE_AGENT) + 1u;
    const int srow = b * 1024 + ((xg & 3) * 32 + r) * 8 + (w & 1) * 4 + g;
    v4u xs[3];
#pragma unroll
    for (int i = 0; i < 3; ++i) xs[i] = __builtin_amdgcn_raw_buffer_load_b128(rx, srow * 768 + n16 * 16 + i * 256, 0, 0);
    if (TOUCH & 2) {
#pragma unroll
      for (int i = 0; i < 2; ++i) touch(scr, (lane < 48 ? 0 : OOB) + b * 786432 + (r * 192 + ((w & 1) * 2 + i) * 48 + lane) * 128, rx);
    }
    float f[24], sm = 0.f, q = 0.f;
#pragma unroll
    for (int i = 0; i < 12; ++i) { f[2 * i] = bflo(xs[i >> 2][i & 3]); f[2 * i + 1] = bfhi(xs[i >> 2][i & 3]); sm += f[2 * i] + f[2 * i + 1]; }
    sm = rsumf(sm) * (1.f / 384.f);
#pragma unroll
    for (int i = 0; i < 24; ++i) { const float d = f[i] - sm; q = fmaf(d, d, q); }
    const float rs = __builtin_amdgcn_rsqf(rsumf(q) * (1.f / 384.f) + 1e-5f);
    ST128((v4u{__float_as_uint(rs), e1, __float_as_uint(sm), e1}), n16 == 0 ? O_ST + srow * 16 : OOB);
    *(LAS unsigned*)(lds + L_TAG) = e1;
  }
#pragma unroll
  for (int p = 0; p < PRE; ++p) xgroup(p);
  STAMP(1);
  if (w < 6) VMWAIT(4 * PRE);
  STAMP(2);
  BAR();
  STAMP(3);
  const unsigned tag = __builtin_amdgcn_readfirstlane(*(LAS unsigned*)(lds + L_TAG));
  const int l63 = lane == 63 ? 0 : OOB;
  if (w < (role == 2 ? 2 : 6) && role != 3) {
    const int t3 = w >> 1, t2 = w & 1;
    f32x4 acc = {};
    v4u a[12], bq[12];
    if (role == 0) {
      lp ap = opq(lds + L_P0 + (3 + t2) * 12288 + rslot), bp = opq(lds + L_P0 + t3 * 12288 + rslot);
#pragma unroll
      for (int k = 0; k < 12; ++k) { a[k] = LDS128(ap, k * 1024); bq[k] = LDS128(bp, k * 1024); }
    } else {
      lp ap = opq(lds + L_P0 + (role == 2 ? ((n16 & 1) * 4 + g) * 16 : t3 * 12288 + rslot));
      lp tb = opq(lds + L_P0 + 36864 + (8 * g + (n16 >> 2)) * 64 + 16 * ((2 * t2 + ((n16 >> 1) & 1)) ^ ((g & 1) ? 3 : 0)) + 8 * (n16 & 1));
#pragma unroll
      for (int k = 0; k < 12; ++k) {
        a[k] = LDS128(ap, k * 1024);
        const s16x4 lo = __builtin_amdgcn_ds_read_tr16_b64_v4i16((LAS s16x4*)(tb + k * 2048));
        const s16x4 hi = __builtin_amdgcn_ds_read_tr16_b64_v4i16((LAS s16x4*)(tb + k * 2048 + 256));
        bq[k] = __builtin_bit_cast(v4u, __builtin_shufflevector(lo, hi, 0, 1, 2, 3, 4, 5, 6, 7));
      }
    }
    SB();
#pragma unroll
    for (int k = 0; k < 12; ++k) acc = mfma(a[k], bq[k], acc);
    if (role != 2) {
      const int blk = role * 576 + (role == 0 ? (3 * u8 + t3) * 24 + 2 * u12 + t2 : (2 * u12 + t2) * 24 + 3 * u8 + t3);
      const unsigned o0 = cvtpk(acc[0], acc[1]), o1 = cvtpk(acc[2], acc[3]);
      auto r0 = __builtin_amdgcn_permlane16_swap(o0, o0, false, false);
      auto r1 = __builtin_amdgcn_permlane16_swap(o1, o1, false, false);
      ST128((v4u{r0[0], r1[0], r0[1], r1[1]}), ((g & 1) ? OOB : 0) + O_MT + blk * 512 + ((g >> 1) * 16 + n16) * 16);
      ST32(scan63(o0 + o1) + tag, l63 + O_RM + blk * 128);
    } else if (g < 2) {
      float v = bfround(g ? acc[1] : acc[0]);
      if (mu >= 12 && mu < 24) v += 1.f;
      ST32(tagged(v, tag & 0xffffu), O_MOD + (g * 1152 + 32 * mu + 16 * t2 + n16) * 4);
    }
  }
  if (tid == 448) __hip_atomic_store((gu32*)(ws + O_CL + bid * 128), tag, __ATOMIC_RELAXED, __HIP_MEMORY_SCOPE_AGENT);
  STAMP(4);
#pragma unroll
  for (int p = PRE; p < 3; ++p) xgroup(p);
  v4u xf[2][12];
  float rq[2], mq[2];
  f32x4 rk[2], mk[2];
  const v4u ones = {0x3f803f80u, 0x3f803f80u, 0x3f803f80u, 0x3f803f80u};
  lp fl = opq(lds + lane * 16);
  f32x4 s1o; float gate;
  {
    const int s0 = 3 * (w & 3);
    const int fb = (w >> 2) * 576 + cb * 24 + 2 * s0;
    const int mo = O_MOD + (b * 1152 + (lane < 24 ? 384 + 32 * s0 + 4 * lane : (lane < 48 ? 32 * s0 + 4 * (lane - 24) : ((lane & 4) ? 768 : 384) + 16 * cb + 4 * (lane & 3)))) * 4;
    const int ro = lane < 6 ? O_RM + (fb + lane) * 128 : OOB;
    const int fo = O_MT + fb * 512 + lane * 16;
    const int so = O_ST + (b * 1024 + kb * 256 + 32 * w + (lane & 31)) * 16;
    v4u mf[3], mv4, sv4; unsigned mr;
    f32x4 sa[3], sb[3]; v4u tf[3];
    lp rp = opq(ring + rslot);
    lp bn = lds + L_BN + w * 1024;
#define HOP1_LOADS() do { _Pragma("unroll") for (int i = 0; i < 3; ++i) mf[i] = LD128(fo + i * 1024, 0); mr = LD32(ro); } while (0)
#define MODOK() __all(((tchk(0u, mv4, tag) & 0xffffu) | (sv4[1] ^ tag) | (sv4[3] ^ tag)) == 0u)
#pragma unroll
    for (int p = 0; p < 6; ++p) {
      if (p < 4) VMWAIT(8 + (p > HP ? 4 : 0)); else if (p == 4) VMWAIT(4 + (p > HP ? 4 : 0)); else VMWAIT(4);
#pragma unroll
      for (int j = 0; j < 4; ++j) xf[j >> 1][2 * p + (j & 1)] = LDS128(rp, ((p % 3) * 4 + j) * 1024);
      if (p == MP) {
        asm volatile("buffer_load_dwordx4 %0, %1, %2, 0 offen sc1" : "=v"(mv4) : "v"(mo), "s"(rws) : "memory");
        asm volatile("buffer_load_dwordx4 %0, %1, %2, 0 offen sc1" : "=v"(sv4) : "v"(so), "s"(rws) : "memory");
      }
      if (p < 3) {
#pragma unroll
        for (int j = 0; j < 4; ++j) asm volatile("" : "+v"(xf[j >> 1][2 * p + (j & 1)]));
        xgroup(p + 3);
      }
      if (p == 2) { __builtin_amdgcn_s_barrier(); STAMP(5); }
      if (p == HP) { if (DLY1) __builtin_amdgcn_s_sleep(DLY1); HOP1_LOADS(); SB(); }
      if (p == 4) {
        asm volatile("" : "+v"(mv4), "+v"(sv4));
        if (UNLIKELY(!MODOK())) {
          unsigned it = 0;
          do { RETRY(0); __builtin_amdgcn_s_sleep(8); CBAR(); mv4 = LD128(mo, 0); sv4 = LD128(so, 0); } while (!MODOK() && ++it < SPIN_CAP);
        }
        if (lane >= 24 && lane < 48) *(LAS v2u*)(bn + 512 + (lane - 24) * 8) = v2u{pkhi(mv4[0], mv4[1]), pkhi(mv4[2], mv4[3])};
        else LDS128(bn, (lane < 24 ? lane : (lane & 7) + 24) * 16) = mv4 & 0xffff0000u;
        *(LAS unsigned*)(bn + 768 + lane * 4) = lane < 32 ? sv4[0] : sv4[2];
        CBAR();
#pragma unroll
        for (int i = 0; i < 3; ++i) {
          sa[i] = LDSF4(bn, (32 * i + 8 * g) * 4); sb[i] = LDSF4(bn, (32 * i + 8 * g) * 4 + 16);
          tf[i] = LDS128(bn, 512 + (32 * i + 8 * g) * 2);
        }
        s1o = LDSF4(bn, 384 + g * 16);
        gate = *(LAS float*)(bn + 448 + n16 * 4);
#pragma unroll
        for (int t = 0; t < 2; ++t) {
          rk[t] = LDSF4(bn, 768 + (8 * g + 4 * t) * 4); mk[t] = LDSF4(bn, 896 + (8 * g + 4 * t) * 4);
          rq[t] = *(LAS float*)(bn + 768 + (prow + 4 * t) * 4); mq[t] = *(LAS float*)(bn + 896 + (prow + 4 * t) * 4);
        }
      }
      SB();
    }
    STAMP(6);
    s1o *= 0.05103103630798288f * 1.4426950408889634f;
    s1o = opq(s1o);
    lp csw = opq(lds + L_CS + w * 128 + g * 16);
    lp msw = opq(fl + L_MS + 3 * w * 1024);
    STAMP(7);
#pragma unroll 1
    for (unsigned it = 0;; ++it) {
      f32x4 acs = {}, au = {};
#pragma unroll
      for (int i = 0; i < 3; ++i) {
        const v4u o = {cvtpk(bflo(mf[i][0]) * sa[i][0], bfhi(mf[i][0]) * sa[i][1]), cvtpk(bflo(mf[i][1]) * sa[i][2], bfhi(mf[i][1]) * sa[i][3]),
                       cvtpk(bflo(mf[i][2]) * sb[i][0], bfhi(mf[i][2]) * sb[i][1]), cvtpk(bflo(mf[i][3]) * sb[i][2], bfhi(mf[i][3]) * sb[i][3])};
        LDS128(msw, i * 1024) = o;
        au = mfma(mf[i], tf[i], au);
        acs = mfma(o, ones, acs);
      }
      const unsigned h = wsum(sum4(sum4(sum4(-mr, mf[0]), mf[1]), mf[2]));
      LDSF4(csw, 0) = acs; LDSF4(csw, 64) = au;
      if (h + 6u * tag == 0u || it >= SPIN_CAP) break;
      RETRY(1);
      __builtin_amdgcn_s_sleep(8);
      CBAR();
      HOP1_LOADS();
    }
  }
  const unsigned spat = (tag & 1u) ? 0x00008000u : 0x80000000u;
  const int oq = tid / 12, oc = tid % 12;
  const int ooff = ((qt * 32 + oq) * 384 + 96 * kb + 8 * oc) * 2;
  const v4u xr = __builtin_amdgcn_raw_buffer_load_b128(rx, tid < 384 ? ooff : 0, 0, 0);
  v4u qf[3]; unsigned qr;
  const int qlo = opq(O_QF + (qt * 24 + 3 * w) * 1024 + lane * 16);
  const int qro = opq(lane < 3 ? O_RQ + (qt * 24 + 3 * w + lane) * 128 : OOB);
#define HOP2_LOADS() do { _Pragma("unroll") for (int i = 0; i < 3; ++i) qf[i] = LD128(qlo + i * 1024, 0); qr = LD32(qro); } while (0)
  {
    const int blk = (b * 32 + kb * 8 + w) * 24 + cb;
    const int dead = act1 ? 0 : OOB;
    const int qso = opq(dead + O_QF + blk * 1024 + ((g & 1) * 32 + (g >> 1) * 16 + n16) * 16);
    const int vso = opq(dead + O_VF + blk * 1024 + lane * 16);
    const int rso = opq((dead | l63) + O_RQ + blk * 128);
    lp cg = opq(lds + L_CS + g * 16), cn = opq(lds + L_CS + 512 + n16 * 4);
    STAMP(9);
    __builtin_amdgcn_s_setprio(2);
    BAR();
    v4u a[12], bb[12];
    f32x4 cs[4], uu[4]; float c2[4], zz[4];
#pragma unroll
    for (int s = 0; s < 12; ++s) a[s] = LDS128(fl, L_MS + s * 1024);
    SB();
    f32x4 aq[2] = {}, av[2] = {};
#pragma unroll
    for (int s = 0; s < 4; ++s) { aq[0] = mfma(a[s], xf[0][s], aq[0]); aq[1] = mfma(a[s], xf[1][s], aq[1]); }
    SB();
#pragma unroll
    for (int k = 0; k < 4; ++k) { cs[k] = LDSF4(cg, k * 128); uu[k] = LDSF4(cg, k * 128 + 64); }
    SB();
#pragma unroll
    for (int s = 4; s < 8; ++s) { aq[0] = mfma(a[s], xf[0][s], aq[0]); aq[1] = mfma(a[s], xf[1][s], aq[1]); }
    SB();
    cs[0] = (cs[0] + cs[1]) + (cs[2] + cs[3]); uu[0] = ((uu[0] + uu[1]) + (uu[2] + uu[3])) * s1o;
    f32x4 ka[2], kc[2];
#pragma unroll
    for (int t = 0; t < 2; ++t) { ka[t] = s1o * rq[t]; kc[t] = uu[0] - cs[0] * ka[t] * mq[t]; }
#pragma unroll
    for (int s = 8; s < 12; ++s) { aq[0] = mfma(a[s], xf[0][s], aq[0]); aq[1] = mfma(a[s], xf[1][s], aq[1]); }
    SB();
#pragma unroll
    for (int s = 0; s < 4; ++s) bb[s] = LDS128(fl, L_MS + (12 + s) * 1024);
    SB();
    {
      const f32x4 v0 = aq[0] * ka[0] + kc[0], v1 = aq[1] * ka[1] + kc[1];
      auto r0 = __builtin_amdgcn_permlane16_swap(cvtpk(v0[0], v0[1]), cvtpk(v1[0], v1[1]), false, false);
      auto r1 = __builtin_amdgcn_permlane16_swap(cvtpk(v0[2], v0[3]), cvtpk(v1[2], v1[3]), false, false);
      const v4u ov = {r0[0], r1[0], r0[1], r1[1]};
      ST128(ov, qso);
      ST32(scan63(sum4(0u, ov)) + tag, rso);
    }
    __builtin_amdgcn_s_setprio(0);
    STAMP(10);
    SB();
#pragma unroll
    for (int s = 4; s < 12; ++s) bb[s] = LDS128(fl, L_MS + (12 + s) * 1024);
    SB();
#pragma unroll
    for (int s = 0; s < 4; ++s) { av[0] = mfma(xf[0][s], bb[s], av[0]); av[1] = mfma(xf[1][s], bb[s], av[1]); }
    SB();
#pragma unroll
    for (int k = 0; k < 4; ++k) { c2[k] = *(LAS float*)(cn + k * 128); zz[k] = *(LAS float*)(cn + k * 128 + 64); }
    SB();
#pragma unroll
    for (int s = 4; s < 8; ++s) { av[0] = mfma(xf[0][s], bb[s], av[0]); av[1] = mfma(xf[1][s], bb[s], av[1]); }
    SB();
    const float c2s = (c2[0] + c2[1]) + (c2[2] + c2[3]), zs = ((zz[0] + zz[1]) + (zz[2] + zz[3])) * gate;
#pragma unroll
    for (int t = 0; t < 2; ++t) { ka[t] = rk[t] * gate; kc[t] = zs - ka[t] * mk[t] * c2s; }
#pragma unroll
    for (int s = 8; s < 12; ++s) { av[0] = mfma(xf[0][s], bb[s], av[0]); av[1] = mfma(xf[1][s], bb[s], av[1]); }
    SB();
    if (DLY2) __builtin_amdgcn_s_sleep(DLY2);
    HOP2_LOADS();
    SB();
    const f32x4 v0 = av[0] * ka[0] + kc[0], v1 = av[1] * ka[1] + kc[1];
    const v4u o = {cvtpk(v0[0], v0[1]), cvtpk(v0[2], v0[3]), cvtpk(v1[0], v1[1]), cvtpk(v1[2], v1[3])};
    ST128(o, vso);
    ST32(scan63(sum4(0u, o)) + tag, rso + (O_RV - O_RQ));
  }
  STAMP(11);
  const int vo = opq(O_VF + ((b * 32 + 4 * w) * 24 + 6 * kb) * 1024 + lane * 16);
  const int vro = opq(lane < 24 ? O_RV + ((b * 32 + 4 * w + lane / 6) * 24 + 6 * kb + lane % 6) * 128 : OOB);
  const int plo = opq(O_PF + (qt * 32 + 4 * w) * 2048 + lane * 16);
  const int pso = opq(O_PF + (qt * 32 + kb * 8 + w) * 2048 + lane * 16);
  const int mso = opq((w == 0 && g < 2) ? O_MV + ((qt * 4 + kb) * 32 + lane) * 8 : OOB);
  lp mxw = opq(lds + L_MX + n16 * 32 + w * 4), mxr = opq(lds + L_MX + n16 * 32), sgr = opq(lds + L_SG + n16 * 32);
  {
    lp qw = opq(lds + L_QS + (lane >> 5) * 12288 + 3 * w * 512 + (lane & 31) * 16);
    lp sgw = opq(lds + L_SG + ((g >> 1) * 16 + n16) * 32 + w * 4);
    const unsigned selv = ((n16 >> 3) == (g >> 1)) ? 0x3f803f80u : 0u;
    const v4u sel = {selv, selv, selv, selv};
    SB();
#define QBAD() (wsum(sum4(sum4(sum4(-qr, qf[0]), qf[1]), qf[2])) + 3u * tag != 0u)
    if (UNLIKELY(QBAD())) {
      unsigned it = 0;
      do { RETRY(2); __builtin_amdgcn_s_sleep(3); CBAR(); HOP2_LOADS(); } while (QBAD() && ++it < SPIN_CAP);
    }
    STAMP(12);
    f32x4 sg = {};
#pragma unroll
    for (int i = 0; i < 3; ++i) {
      sg = mfma(sel, qf[i], sg);
      LDS128(qw, i * 512) = qf[i];
    }
    *(LAS float*)(sgw) = sg[0];
  }
  v4u vf[4][6];
#define VLOAD(i) vf[(i) / 6][(i) % 6] = LD128(vo + ((i) % 6 & 3) * 1024, (((i) / 6) * 24 + ((i) % 6 & 4)) * 1024)
  STAMP(13);
  BAR();
  if (w >= 4) {
#pragma unroll
    for (int i = 0; i < 12; ++i) VLOAD(i);
  }
  SB();
  {
    v4u qa[6], qb[6];
    f32x4 sp[4];
#pragma unroll
    for (int i = 0; i < 6; ++i) qa[i] = LDS128(fl, L_QS + ((i & 1) * 12 + (i >> 1)) * 1024);
#pragma unroll
    for (int i = 0; i < 6; ++i) qb[i] = LDS128(fl, L_QS + ((i & 1) * 12 + 3 + (i >> 1)) * 1024);
    SB();
    f32x4 acc[2][2] = {};
#define SSTAGE(Q, S0) _Pragma("unroll") for (int i = 0; i < 6; ++i) { acc[i & 1][0] = mfma(xf[0][(S0) + (i >> 1)], Q[i], acc[i & 1][0]); acc[i & 1][1] = mfma(xf[1][(S0) + (i >> 1)], Q[i], acc[i & 1][1]); }
    SSTAGE(qa, 0);
    SB();
#pragma unroll
    for (int i = 0; i < 6; ++i) qa[i] = LDS128(fl, L_QS + ((i & 1) * 12 + 6 + (i >> 1)) * 1024);
    SB();
    SSTAGE(qb, 3);
    SB();
#pragma unroll
    for (int i = 0; i < 6; ++i) qb[i] = LDS128(fl, L_QS + ((i & 1) * 12 + 9 + (i >> 1)) * 1024);
#pragma unroll
    for (int i = 0; i < 4; ++i) sp[i] = LDSF4(sgr, (i >> 1) * 512 + (i & 1) * 16);
    SB();
    SSTAGE(qa, 6);
    SB();
    sp[0] += sp[1]; sp[2] += sp[3];
    const float sig0 = -((sp[0][0] + sp[0][1]) + (sp[0][2] + sp[0][3])), sig1 = -((sp[2][0] + sp[2][1]) + (sp[2][2] + sp[2][3]));
    const f32x4 mr0 = mk[0] * rk[0], mr1 = mk[1] * rk[1];
    const f32x4 kc[2][2] = {{mr0 * sig0, mr1 * sig0}, {mr0 * sig1, mr1 * sig1}};
    SSTAGE(qb, 9);
    SB();
#pragma unroll
    for (int u = 0; u < 2; ++u) {
      acc[u][0] = acc[u][0] * rk[0] + kc[u][0]; acc[u][1] = acc[u][1] * rk[1] + kc[u][1];
      float mx = max3(max3(acc[u][0][0], acc[u][0][1], acc[u][0][2]), acc[u][0][3], acc[u][1][0]);
      mx = max_g(max2(max3(mx, acc[u][1][1], acc[u][1][2]), acc[u][1][3]));
      *(LAS float*)(mxw + u * 512) = mx;
    }
    SB();
    STAMP(14);
    if (w < 4) {
#pragma unroll
      for (int i = 0; i < 12; ++i) VLOAD(i);
    }
    BAR();
    f32x4 ma[2], mb[2];
#pragma unroll
    for (int u = 0; u < 2; ++u) { ma[u] = LDSF4(mxr, u * 512); mb[u] = LDSF4(mxr, u * 512 + 16); }
    SB();
    float m[2];
#pragma unroll
    for (int u = 0; u < 2; ++u) {
      m[u] = max2(max3(max3(ma[u][0], ma[u][1], ma[u][2]), ma[u][3], mb[u][0]), max3(mb[u][1], mb[u][2], mb[u][3]));
      float p[8];
#pragma unroll
      for (int i = 0; i < 8; ++i) p[i] = __builtin_amdgcn_exp2f(acc[u][i >> 2][i & 3] - m[u]);
      const v4u o = (v4u{cvtpk(p[0], p[1]), cvtpk(p[2], p[3]), cvtpk(p[4], p[5]), cvtpk(p[6], p[7])} & 0x7fff7fffu) | spat;
      ST128(o, pso + u * 1024);
    }
    ST64((v2u{__float_as_uint(g ? m[1] : m[0]), tag}), mso);
  }
  __builtin_amdgcn_s_barrier();
  STAMP(15);
  {
    f32x4 acc[2][6] = {};
    float ls[2] = {0.f, 0.f};
    v2u mv[2]; unsigned vr;
    unsigned one2 = 0x3f803f80u;
    asm volatile("" : "+s"(one2));
    v4u pf[4][2];
    const int mlo = O_MV + ((qt * 4 + g) * 32 + n16) * 8;
#define PLOAD(s) _Pragma("unroll") for (int u_ = 0; u_ < 2; ++u_) pf[s][u_] = LD128(plo + (((s) & 1) * 2 + u_) * 1024, ((s) >> 1) * 4096)
#define VSUM(H, OUT) do { unsigned h_ = (lane < 12) == ((H) == 0) ? -vr : 0u; \
      _Pragma("unroll") for (int i = 12 * (H); i < 12 * (H) + 12; ++i) h_ = sum4(h_, vf[i / 6][i % 6]); \
      OUT = wsum(h_) + 12u * tag; } while (0)
#define VCHECK(H) do { unsigned bad_; VSUM(H, bad_); \
      if (UNLIKELY(bad_ != 0u)) { unsigned it = 0; \
        do { RETRY(3 + (H)); __builtin_amdgcn_s_sleep(2); CBAR(); \
          _Pragma("unroll") for (int i = 12 * (H); i < 12 * (H) + 12; ++i) VLOAD(i); \
          vr = LD32(vro); VSUM(H, bad_); } while (bad_ != 0u && ++it < SPIN_CAP); } } while (0)
#define POK(s) (pf[s][0] ^= spat, pf[s][1] ^= spat, __all((tchk(tchk(0u, pf[s][0], 0u), pf[s][1], 0u) & 0x80008000u) == 0u))
#define PSTEP(s) do { if (UNLIKELY(!POK(s))) { unsigned it = 0; \
        do { RETRY(5 + (s)); __builtin_amdgcn_s_sleep(2); CBAR(); PLOAD(s); } while (!POK(s) && ++it < SPIN_CAP); } \
      _Pragma("unroll") for (int u = 0; u < 2; ++u) { \
        _Pragma("unroll") for (int et = 0; et < 6; ++et) acc[u][et] = mfma(vf[s][et], pf[s][u], acc[u][et]); \
        _Pragma("unroll") for (int e = 0; e < 4; ++e) ls[u] = dot2(pf[s][u][e], one2, ls[u]); } \
      asm volatile("" : "+v"(ls[0]), "+v"(ls[1])); } while (0)
    vr = LD32(vro);
    SB();
#pragma unroll
    for (int i = 12; i < 18; ++i) VLOAD(i);
    PLOAD(0); PLOAD(1);
    SB();
    STAMP(16);
    VCHECK(0);
#pragma unroll
    for (int i = 18; i < 24; ++i) VLOAD(i);
    mv[0] = LD64(mlo); mv[1] = LD64(mlo + 128);
    SB();
    PSTEP(0);
    PLOAD(2); PLOAD(3);
    SB();
    PSTEP(1);
    VCHECK(1);
    if (UNLIKELY(!__all(mv[0][1] == tag && mv[1][1] == tag))) {
      unsigned it = 0;
      do { RETRY(9); CBAR(); mv[0] = LD64(mlo); mv[1] = LD64(mlo + 128); } while (!__all(mv[0][1] == tag && mv[1][1] == tag) && ++it < SPIN_CAP);
    }
    float f[2];
#pragma unroll
    for (int u = 0; u < 2; ++u) {
      const float mo_ = __uint_as_float(__builtin_amdgcn_ds_bpermute(((w >> 1) * 16 + n16) * 4, mv[u][0]));
      f[u] = opq(__builtin_amdgcn_exp2f(mo_ - max_g(__uint_as_float(mv[u][0]))));
    }
    lp pw = opq(ring + prow * 208 + 8 * g), lw = opq(lds + L_LS + prow * 32 + w * 4);
    PSTEP(2);
    PSTEP(3);
    STAMP(17);
#pragma unroll
    for (int u = 0; u < 2; ++u) {
      *(LAS float*)(lw + u * 128) = sum_g(ls[u]) * f[u];
#pragma unroll
      for (int et = 0; et < 6; ++et) {
        const f32x4 t = acc[u][et] * f[u];
        *(LAS v2u*)(pw + u * 832 + 32 * et) = v2u{cvtpk(t[0], t[1]), cvtpk(t[2], t[3])};
      }
    }
  }
  STAMP(18);
  BAR();
  if (tid < 384) {
    lp pb = opq(lds + L_RING + oq * 208 + oc * 16), pb2 = opq(pb + 4 * RING);
    v4u p[8];
#pragma unroll
    for (int k = 0; k < 4; ++k) { p[k] = LDS128(pb, k * RING); p[4 + k] = LDS128(pb2, k * RING); }
    const f32x4 la = LDSF4(lds, L_LS + oq * 32), lb = LDSF4(lds, L_LS + oq * 32 + 16);
    SB();
    unsigned klo = 0x00003f80u, khi = 0x3f800000u;
    asm volatile("" : "+s"(klo), "+s"(khi));
    float o[8] = {};
#pragma unroll
    for (int k = 0; k < 8; ++k)
#pragma unroll
      for (int e = 0; e < 4; ++e) { o[2 * e] = dot2(p[k][e], klo, o[2 * e]); o[2 * e + 1] = dot2(p[k][e], khi, o[2 * e + 1]); }
    const float inv = __builtin_amdgcn_rcpf(((la[0] + la[1]) + (la[2] + la[3])) + ((lb[0] + lb[1]) + (lb[2] + lb[3])));
    v4u ov;
#pragma unroll
    for (int e = 0; e < 4; ++e) ov[e] = cvtpk(fmaf(o[2 * e], inv, bflo(xr[e])), fmaf(o[2 * e + 1], inv, bfhi(xr[e])));
    *(v4u*)((unsigned char*)out + ooff) = ov;
  } else if (bid == 0 && w == 7) {
#pragma unroll 1
    for (unsigned it = 0; it < SPIN_CAP; ++it) {
      CBAR();
      unsigned bad = 0;
#pragma unroll
      for (int k = 0; k < 4; ++k) bad |= LD32(O_CL + (k * 64 + lane) * 128) ^ tag;
      if (__all(bad == 0)) break;
    }
    if (lane == 0) __hip_atomic_store((gu32*)(ws + O_EPOCH), tag, __ATOMIC_RELAXED, __HIP_MEMORY_SCOPE_AGENT);
  }
  STAMP(19);
  STFLUSH();
}

extern "C" void kernel_launch(void* const* d_in, const int* in_sizes, int n_in, void* d_out, int out_size, void* d_ws, size_t ws_size,
                              hipStream_t stream) {
  (void)hipFuncSetAttribute((const void*)dit_kernel, hipFuncAttributeMaxDynamicSharedMemorySize, L_TOTAL);
  dit_kernel<<<256, 512, L_TOTAL, stream>>>((const u16*)d_in[0], (const u16*)d_in[1], (const u16*)d_in[2], (const u16*)d_in[3],
                                            (const u16*)d_in[4], (unsigned char*)d_ws, (u16*)d_out);
}
```

```cpp
#include <hip/hip_runtime.h>
#include <hip/hip_bf16.h>

typedef short bf16x8 __attribute__((ext_vector_type(8)));
typedef short s16x4 __attribute__((ext_vector_type(4)));
typedef float f32x4 __attribute__((ext_vector_type(4)));
typedef float f32x2 __attribute__((ext_vector_type(2)));
typedef unsigned v4u __attribute__((ext_vector_type(4)));
typedef unsigned v2u __attribute__((ext_vector_type(2)));
typedef __bf16 bf2 __attribute__((ext_vector_type(2)));
typedef unsigned short u16;
#define DI __device__ __forceinline__
#define LAS __attribute__((address_space(3)))
#define GAS __attribute__((address_space(1)))
typedef GAS unsigned gu32;
typedef LAS unsigned char* lp;
#define STDECL
#define STAMP(i)
#define STFLUSH()
#define RETRY(k)
#define DLY0 24
#define DLY1 8
#define DLY2 12
#define PRE 1
#define HP 3
#define MP 0
#define TOUCH 1
#define CODEPF 16384
#define XS 0
#define DLY3 0
#define PD 4
#define SLP 2
#define ROT 0
#define NX1 8
#define NEARLY 0
#ifndef NV0
#define NV0 2
#endif
#define AUXO 16
#define NH 12
#ifndef NV2
#define NV2 18
#endif
#ifndef NVS
#define NVS 13
#endif

constexpr int O_EPOCH = 0;
constexpr int O_MOD = 4096;
constexpr int O_CL = 16384;
constexpr int O_MT = 131072;
constexpr int O_NF = O_MT + 589824;
constexpr int O_QF = O_NF + 1572864;
constexpr int O_VF = O_QF + 1572864;
constexpr int O_OF = O_VF + 1572864;
constexpr int O_ML = O_OF + 6291456;
constexpr int O_RM = O_ML + 131072;
constexpr int O_RN = O_RM + 1152 * 128;
constexpr int O_RQ = O_RN + 512 * 128;
constexpr int O_RV = O_RQ + 1536 * 128;
constexpr int O_RO = O_RV + 1536 * 128;
constexpr int WS_BYTES = O_RO + 6144 * 128;
constexpr int OOB = 0x40000000;

constexpr int L_P0 = 0;
constexpr int L_MS = 61440, L_QS = L_MS + 24576, L_CS = L_QS + 24576, L_MX = L_CS + 512, L_LS = L_MX + 1024, L_BN = L_LS + 1024;
constexpr int L_SCR = L_BN + 6144, L_TAG = L_SCR + 256;
constexpr int L_TOTAL = L_TAG + 32;

static_assert(NV2 >= 16 && NV0 + NVS <= NV2 && NV2 <= 24, "");
constexpr int AUX_SC1 = 16;
constexpr unsigned SPIN_CAP = 1u << 16;

DI float bflo(unsigned v) { return __uint_as_float(v << 16); }
DI float bfhi(unsigned v) { return __uint_as_float(v & 0xffff0000u); }
DI unsigned cvtpk(float lo, float hi) {
  f32x2 x = {lo, hi};
  return __builtin_bit_cast(unsigned, __builtin_convertvector(x, bf2));
}
DI float bfround(float x) { return bflo(cvtpk(x, 0.f)); }
DI unsigned pkhi(unsigned lo, unsigned hi) { return __builtin_amdgcn_perm(hi, lo, 0x07060302u); }
DI unsigned tagged(float v, unsigned tag16) { return (cvtpk(0.f, v) & 0xffff0000u) | tag16; }
DI unsigned tchk(unsigned a, v4u v, unsigned tag) { return a | (v[0] ^ tag) | (v[1] ^ tag) | (v[2] ^ tag) | (v[3] ^ tag); }
DI unsigned sum4(unsigned a, v4u v) { return a + v[0] + v[1] + v[2] + v[3]; }
DI unsigned scan63(unsigned v) {
  v += __builtin_amdgcn_update_dpp(0u, v, 0x111, 0xf, 0xf, true);
  v += __builtin_amdgcn_update_dpp(0u, v, 0x112, 0xf, 0xf, true);
  v += __builtin_amdgcn_update_dpp(0u, v, 0x114, 0xf, 0xf, true);
  v += __builtin_amdgcn_update_dpp(0u, v, 0x118, 0xf, 0xf, true);
  v += __builtin_amdgcn_update_dpp(0u, v, 0x142, 0xa, 0xf, true);
  v += __builtin_amdgcn_update_dpp(0u, v, 0x143, 0xc, 0xf, true);
  return v;
}
DI float sum_g(float v);
DI float qsumf(float v) {
#define FSTEP(ctl) v += __uint_as_float(__builtin_amdgcn_update_dpp(0u, __float_as_uint(v), ctl, 0xf, 0xf, true))
  FSTEP(0x128); FSTEP(0x124);
#undef FSTEP
  return sum_g(v);
}
DI unsigned wsum(unsigned v) { return __builtin_amdgcn_readlane(scan63(v), 63); }
DI float max3(float a, float b, float c) { float r; asm("v_max3_f32 %0, %1, %2, %3" : "=v"(r) : "v"(a), "v"(b), "v"(c)); return r; }
DI float max2(float a, float b) { float r; asm("v_max_f32 %0, %1, %2" : "=v"(r) : "v"(a), "v"(b)); return r; }
DI float dot2(unsigned a, unsigned b, float c) { return __builtin_amdgcn_fdot2_f32_bf16(__builtin_bit_cast(bf2, a), __builtin_bit_cast(bf2, b), c, false); }
DI f32x4 mfma(v4u a, v4u b, f32x4 c) {
  return __builtin_amdgcn_mfma_f32_16x16x32_bf16(__builtin_bit_cast(bf16x8, a), __builtin_bit_cast(bf16x8, b), c, 0, 0, 0);
}
DI float sum_g(float v) {
  auto r = __builtin_amdgcn_permlane16_swap(__float_as_uint(v), __float_as_uint(v), false, false);
  v = __uint_as_float(r[0]) + __uint_as_float(r[1]);
  auto q = __builtin_amdgcn_permlane32_swap(__float_as_uint(v), __float_as_uint(v), false, false);
  return __uint_as_float(q[0]) + __uint_as_float(q[1]);
}
DI float max_g(float v) {
  auto r = __builtin_amdgcn_permlane16_swap(__float_as_uint(v), __float_as_uint(v), false, false);
  v = max2(__uint_as_float(r[0]), __uint_as_float(r[1]));
  auto q = __builtin_amdgcn_permlane32_swap(__float_as_uint(v), __float_as_uint(v), false, false);
  return max2(__uint_as_float(q[0]), __uint_as_float(q[1]));
}
DI int sopq(int v) { v = __builtin_amdgcn_readfirstlane(v); asm volatile("" : "+s"(v)); return v; }
template <typename T> DI T opq(T v) { asm volatile("" : "+v"(v)); return v; }
#define BAR() do { asm volatile("s_waitcnt lgkmcnt(0)" ::: "memory"); __builtin_amdgcn_s_barrier(); asm volatile("" ::: "memory"); } while (0)
#define VMWAIT(n) asm volatile("s_waitcnt vmcnt(%0)" :: "i"(n) : "memory")
#define SB() __builtin_amdgcn_sched_barrier(0)
#define CBAR() asm volatile("" ::: "memory")
#define LD128(off, so) __builtin_amdgcn_raw_buffer_load_b128(rws, (off), (so), AUX_SC1)
#define LD64(off) __builtin_amdgcn_raw_buffer_load_b64(rws, (off), 0, AUX_SC1)
#define LD32(off) __builtin_amdgcn_raw_buffer_load_b32(rws, (off), 0, AUX_SC1)
#define ST128(v, off) __builtin_amdgcn_raw_buffer_store_b128((v), rws, (off), 0, AUX_SC1)
#define ST128S(v, off, so) __builtin_amdgcn_raw_buffer_store_b128((v), rws, (off), (so), AUX_SC1)
#define ST64(v, off) __builtin_amdgcn_raw_buffer_store_b64((v), rws, (off), 0, AUX_SC1)
#define ST32(v, off) __builtin_amdgcn_raw_buffer_store_b32((v), rws, (off), 0, AUX_SC1)
#define ST16(v, off) __builtin_amdgcn_raw_buffer_store_b16((v), rws, (off), 0, AUX_SC1)
#define LDS128(p, off) (*(LAS v4u*)((p) + (off)))
#define LDSF4(p, off) (*(LAS f32x4*)((p) + (off)))
DI void dma(unsigned ldsaddr, int voff, __amdgpu_buffer_rsrc_t rs, int soff) {
  asm volatile("s_mov_b32 m0, %0\n\ts_nop 0\n\tbuffer_load_dwordx4 %1, %2, %3 offen lds" :: "s"(ldsaddr), "v"(voff), "s"(rs), "s"(soff) : "memory");
}

DI void touch(unsigned ldsaddr, int voff, __amdgpu_buffer_rsrc_t rs) {
  asm volatile("s_mov_b32 m0, %0\n\ts_nop 0\n\tbuffer_load_dword %1, %2, 0 offen lds" :: "s"(ldsaddr), "v"(voff), "s"(rs) : "memory");
}
#define UNLIKELY(c) __builtin_expect(!!(c), 0)

__global__ __launch_bounds__(512, 2) void dit_kernel(const u16* __restrict__ x, const u16* __restrict__ c, const u16* __restrict__ wqkv,
                                                     const u16* __restrict__ wo, const u16* __restrict__ wmod, unsigned char* ws,
                                                     u16* __restrict__ out) {
  extern __shared__ __attribute__((aligned(16))) unsigned char lds_[];
  lp lds = (lp)lds_;
  const unsigned ldsb = (unsigned)(size_t)lds;
  STDECL;
  STAMP(0);
  const unsigned long long pc0 = __builtin_amdgcn_s_getpc();
  const int tid = threadIdx.x, lane = tid & 63, w = __builtin_amdgcn_readfirstlane(tid >> 6);
  const int n16 = lane & 15, g = lane >> 4;
  const int bid = blockIdx.x, xg = bid & 7, r = bid >> 3;
  const int b = xg >> 2, ql = r & 7, kb = r >> 3;
  const int qt = b * 32 + (xg & 3) * 8 + ql;
  const int qt3 = b * 32 + (((xg & 3) + XS) & 3) * 8 + ql;
  const bool act1 = ql < 6;
  const int cb = (xg & 3) * 6 + (act1 ? ql : ql - 2);
  const __amdgpu_buffer_rsrc_t rws = __builtin_amdgcn_make_buffer_rsrc(ws, 0, WS_BYTES, 0x00020000);
  const __amdgpu_buffer_rsrc_t rx = __builtin_amdgcn_make_buffer_rsrc((void*)x, 0, 2048 * 768, 0x00020000);
  const int dch = 16 * ((lane & 3) ^ ((lane & 32) ? 3 : 0));
  const int rslot = (4 * n16 + (g ^ ((n16 & 8) ? 3 : 0))) * 16;
  const int l63 = lane == 63 ? 0 : OOB;

  const int mu = (((r - 24) >> 1) * 8 + xg) * 2 + (r & 1);
  const int role = r < 12 ? 0 : (r < 24 ? 1 : (mu < 36 ? 2 : 3));
  const int ru = role == 1 ? r - 12 : r;
  const int u12 = 6 * (xg & 1) + ru % 6, u8 = 2 * (xg >> 1) + ru / 6;
  unsigned ep;
  asm volatile("s_nop 4\n\tbuffer_load_dword %0, off, %1, 0 sc1" : "=v"(ep) : "s"(rws) : "memory");
  v4u nv[3];
  const int grp = bid * 2 + (w & 1);
  if (w < 6) {
    unsigned cv = 0;
    if (role == 2) cv = ((const unsigned*)c)[tid];
    if (role < 2) {
      const __amdgpu_buffer_rsrc_t rq = __builtin_amdgcn_make_buffer_rsrc((void*)wqkv, 0, 384 * 2304, 0x00020000);
      const int v = (lane >> 2) * 2304 + dch;
      const int s3 = 48 * u8 * 2304 + (role ? 1536 : 768) + 128 * w;
#pragma unroll
      for (int t = 0; t < 3; ++t) {
        dma(ldsb + L_P0 + (t * 12 + 2 * w) * 1024, v, rq, s3 + 16 * t * 2304);
        dma(ldsb + L_P0 + (t * 12 + 2 * w + 1) * 1024, v, rq, s3 + 16 * t * 2304 + 64);
      }
      if (role == 0) {
        const int s2 = 32 * u12 * 2304 + 128 * w;
#pragma unroll
        for (int t = 0; t < 2; ++t) {
          dma(ldsb + L_P0 + ((3 + t) * 12 + 2 * w) * 1024, v, rq, s2 + 16 * t * 2304);
          dma(ldsb + L_P0 + ((3 + t) * 12 + 2 * w + 1) * 1024, v, rq, s2 + 16 * t * 2304 + 64);
        }
      }
    }
    if (role == 1 || role == 2) {
      const int ld = role == 1 ? 768 : 4608;
      const __amdgpu_buffer_rsrc_t rc = __builtin_amdgcn_make_buffer_rsrc((void*)(role == 1 ? wo : wmod), 0, 384 * ld, 0x00020000);
      const int v = (lane >> 2) * ld + dch;
#pragma unroll
      for (int j = 0; j < 4; ++j) dma(ldsb + L_P0 + (36 + 4 * w + j) * 1024, v, rc, (role == 1 ? u12 : mu) * 64 + (4 * w + j) * 16 * ld);
    }
    if (role == 2) {
      const float f0 = bflo(cv), f1 = bfhi(cv);
      const int bb = tid >= 192, i2 = 2 * (tid - 192 * bb);
      *(LAS unsigned*)(lds + L_P0 + (i2 >> 5) * 1024 + (bb * 4 + ((i2 >> 3) & 3)) * 16 + (i2 & 7) * 2) =
          cvtpk(f0 * __builtin_amdgcn_rcpf(1.f + __expf(-f0)), f1 * __builtin_amdgcn_rcpf(1.f + __expf(-f1)));
    }
    VMWAIT(0);
  } else {
    __builtin_amdgcn_s_setprio(3);
    v4u xs[3];
    asm volatile("buffer_load_dwordx4 %0, %3, %4, 0 offen\n\tbuffer_load_dwordx4 %1, %3, %4, 0 offen offset:256\n\tbuffer_load_dwordx4 %2, %3, %4, 0 offen offset:512"
                 : "=&v"(xs[0]), "=&v"(xs[1]), "=&v"(xs[2]) : "v"((4 * grp + (lane & 3)) * 768 + (lane >> 2) * 16), "s"(rx) : "memory");
    {
      const unsigned scr = ldsb + L_SCR;
      const int dead = ((TOUCH & 1) && lane < 36) ? 0 : OOB;
      const __amdgpu_buffer_rsrc_t rq = __builtin_amdgcn_make_buffer_rsrc((void*)wqkv, 0, 384 * 2304, 0x00020000);
      touch(scr, dead + (w == 6 ? (96 * (xg >> 1) + 3 * r + lane / 12) * 2304 + 768 + (lane % 12) * 128 : (192 * (xg & 1) + 6 * r + lane / 6) * 2304 + (lane % 6) * 128), rq);
      const __amdgpu_buffer_rsrc_t r2 = __builtin_amdgcn_make_buffer_rsrc(w == 6 ? (void*)wo : (void*)(pc0 & ~127ull), 0, w == 6 ? 384 * 768 : CODEPF, 0x00020000);
      touch(scr, w == 6 ? dead + (12 * r + lane / 3) * 768 + 384 * (xg & 1) + (lane % 3) * 128 : (lane < 5 ? (r * 5 + lane) * 128 : OOB), r2);
    }
    if (w == 7) {
      const __amdgpu_buffer_rsrc_t r3 = __builtin_amdgcn_make_buffer_rsrc((void*)wmod, 0, 384 * 4608, 0x00020000);
      touch(ldsb + L_SCR, (lane < (xg < 2 ? 36 : 24) ? 0 : OOB) + (12 * r + lane % 12) * 4608 + ((lane / 12) * 8 + xg) * 128, r3);
    }
    else touch(ldsb + L_SCR, OOB, rx);
    asm volatile("s_waitcnt vmcnt(3)" : "+v"(xs[0]), "+v"(xs[1]), "+v"(xs[2]) :: "memory");
    float f[24], sm = 0.f, q = 0.f;
#pragma unroll
    for (int i = 0; i < 12; ++i) { f[2 * i] = bflo(xs[i >> 2][i & 3]); f[2 * i + 1] = bfhi(xs[i >> 2][i & 3]); sm += f[2 * i] + f[2 * i + 1]; }
    sm = qsumf(sm) * (1.f / 384.f);
#pragma unroll
    for (int i = 0; i < 24; ++i) { f[i] -= sm; q = fmaf(f[i], f[i], q); }
    const float rs = __builtin_amdgcn_rsqf(qsumf(q) * (1.f / 384.f) + 1e-5f);
#pragma unroll
    for (int i = 0; i < 3; ++i)
      nv[i] = opq(v4u{cvtpk(f[8 * i] * rs, f[8 * i + 1] * rs), cvtpk(f[8 * i + 2] * rs, f[8 * i + 3] * rs), cvtpk(f[8 * i + 4] * rs, f[8 * i + 5] * rs), cvtpk(f[8 * i + 6] * rs, f[8 * i + 7] * rs)});
  }
  asm volatile("" : "+v"(ep));
  const unsigned tag = __builtin_amdgcn_readfirstlane(ep) + 1u;
#define NSTORE() do { if (w >= 6) {     \
    const int no = O_NF + (((grp >> 3) * 2 + (grp & 1)) * 12 + g) * 1024 + (((lane >> 2) & 3) * 16 + ((grp >> 1) & 3) * 4 + (lane & 3)) * 16; \
    _Pragma("unroll") for (int i = 0; i < 3; ++i) ST128(nv[i], no + i * 4096); \
    ST64((v2u{scan63(sum4(sum4(sum4(0u, nv[0]), nv[1]), nv[2])) + tag, tag}), l63 + O_RN + grp * 128); \
    __builtin_amdgcn_s_setprio(0); } } while (0)
  if (NEARLY || role >= 2) NSTORE();
  STAMP(2);
  BAR();
  STAMP(3);
  if (!NEARLY && role < 2) NSTORE();
  const int rg = b * 32 + kb * 8 + w;
  const int nro = lane < 8 ? O_RN + (rg * 8 + lane) * 128 : OOB;
  if (w < (role == 2 ? 2 : 6) && role != 3) {
    const int t3 = w >> 1, t2 = w & 1;
    f32x4 acc = {};
    v4u a[12], bq[12];
    if (role == 0) {
      lp ap = opq(lds + L_P0 + (3 + t2) * 12288 + rslot), bp = opq(lds + L_P0 + t3 * 12288 + rslot);
#pragma unroll
      for (int k = 0; k < 12; ++k) {
        a[k] = LDS128(ap, k * 1024); bq[k] = LDS128(bp, k * 1024);
        if (k >= 5) acc = mfma(a[k - 5], bq[k - 5], acc);
        SB();
      }
    } else {
      lp ap = opq(lds + L_P0 + (role == 2 ? ((n16 & 1) * 4 + g) * 16 : t3 * 12288 + rslot));
      lp tb = opq(lds + L_P0 + 36864 + (8 * g + (n16 >> 2)) * 64 + 16 * ((2 * t2 + ((n16 >> 1) & 1)) ^ ((g & 1) ? 3 : 0)) + 8 * (n16 & 1));
#pragma unroll
      for (int k = 0; k < 12; ++k) {
        a[k] = LDS128(ap, k * 1024);
        bq[k] = __builtin_bit_cast(v4u, __builtin_shufflevector(__builtin_amdgcn_ds_read_tr16_b64_v4i16((LAS s16x4*)(tb + k * 2048)),
                                                                __builtin_amdgcn_ds_read_tr16_b64_v4i16((LAS s16x4*)(tb + k * 2048 + 256)), 0, 1, 2, 3, 4, 5, 6, 7));
        if (k >= 5) acc = mfma(a[k - 5], bq[k - 5], acc);
        SB();
      }
    }
#pragma unroll
    for (int k = 7; k < 12; ++k) acc = mfma(a[k], bq[k], acc);
    if (role != 2) {
      const int blk = role * 576 + (role == 0 ? (3 * u8 + t3) * 24 + 2 * u12 + t2 : (2 * u12 + t2) * 24 + 3 * u8 + t3);
      const unsigned o0 = cvtpk(acc[0], acc[1]), o1 = cvtpk(acc[2], acc[3]);
      auto r0 = __builtin_amdgcn_permlane16_swap(o0, o0, false, false);
      auto r1 = __builtin_amdgcn_permlane16_swap(o1, o1, false, false);
      ST128((v4u{r0[0], r1[0], r0[1], r1[1]}), ((g & 1) ? OOB : 0) + O_MT + blk * 512 + ((g >> 1) * 16 + n16) * 16);
      ST32(scan63(o0 + o1) + tag, l63 + O_RM + blk * 128);
    } else if (g < 2) {
      float v = bfround(g ? acc[1] : acc[0]);
      if (mu >= 12 && mu < 24) v += 1.f;
      ST32(tagged(v, tag & 0xffffu), O_MOD + (g * 1152 + 32 * mu + 16 * t2 + n16) * 4);
    }
  }
  STAMP(4);
  if (role >= 2) __builtin_amdgcn_s_sleep(DLY0);
  if (w >= 6) __builtin_amdgcn_s_sleep(DLY1);
  v2u nr = LD64(nro);
  SB();
  const int l16 = opq(lane * 16);
  const int s_n = O_NF + rg * 24576;
  const int rot = ROT ? (ql * 3) >> 1 : 0;
  const int l16b = opq(lane * 16 + 12288);
  v4u xf[2][12];
#define XF(i) xf[(i) & 1][(i) >> 1]
#define XLOAD(i) XF(i) = LD128(((i) & 1) ? l16b : l16, s_n + (((i) >> 1) + rot - (((i) >> 1) + rot >= 12 ? 12 : 0)) * 1024)
  lp fl = opq(lds + L_MS + lane * 16);
  v4u qf[3]; unsigned qr;
  const int qro = opq(lane < 3 ? O_RQ + (qt * 24 + 3 * w + lane) * 128 : OOB);
  const int s_ql = sopq(O_QF + (qt * 24 + 3 * w) * 1024);
#define HOP2_LOADS() do { _Pragma("unroll") for (int i = 0; i < 3; ++i) qf[i] = LD128(l16 + i * 1024, s_ql); qr = LD32(qro); } while (0)
  {
    const int s0 = 3 * (w & 3);
    const int mo = opq(O_MOD + (b * 1152 + (lane < 24 ? 384 + 32 * s0 + 4 * lane : (lane < 48 ? 32 * s0 + 4 * (lane - 24) : (w < 4 ? 384 : 768) + 16 * cb + 4 * (lane & 3)))) * 4);
    const int fb = (w >> 2) * 576 + cb * 24 + 2 * s0;
    const int ro = opq(lane < 6 ? O_RM + (fb + lane) * 128 : OOB);
    const int s_f = sopq(O_MT + fb * 512);
    const int blk1 = rg * 24 + cb, dead1 = act1 ? 0 : OOB;
    const int qso = opq(dead1 + O_QF + blk1 * 1024 + ((g & 1) * 32 + (g >> 1) * 16 + n16) * 16);
    const int vso = opq(dead1 + O_VF + blk1 * 1024 + lane * 16);
    const int rso = opq((dead1 | l63) + O_RQ + blk1 * 128);
    const int rvo = opq((dead1 | l63) + O_RV + (((b * 4 + kb) * 24 + cb) * 8 + w) * 128);
    lp cg = opq(lds + L_CS + g * 16), cn = opq(lds + L_CS + 256 + n16 * 4), csw = opq(lds + L_CS + w * 64 + n16 * 4);
    lp bn = lds + L_BN + w * 768, bg = opq(bn + g * 32), msw[3];
#pragma unroll
    for (int i = 0; i < 3; ++i) { int sl = s0 + i - rot; if (sl < 0) sl += 12; msw[i] = opq(fl + (w >> 2) * 12288 + sl * 1024); }
    SB();
    if (UNLIKELY(!__all(lane >= 8 || nr[1] == tag))) {
      unsigned it = 0;
      do { RETRY(0); __builtin_amdgcn_s_sleep(4); CBAR(); nr = LD64(nro); } while (!__all(lane >= 8 || nr[1] == tag) && ++it < SPIN_CAP);
    }
    STAMP(5);
    unsigned hx = -nr[0];
    v4u mf[3], mv4; unsigned mr;
    f32x4 sa[3], sb[3]; v4u tf[3]; float oc_;
#define MODOK() __all((tchk(0u, mv4, tag) & 0xffffu) == 0u)
#define HOP1_LOADS() do { _Pragma("unroll") for (int i = 0; i < 3; ++i) mf[i] = LD128(l16 + i * 1024, s_f); mr = LD32(ro); } while (0)
#pragma unroll
    for (int i = 0; i < NX1; ++i) {
      XLOAD(i);
      if (i >= 6) hx = sum4(hx, XF(i - 6));
      if (i == MP) mv4 = LD128(mo, 0);
      if (i == HP) HOP1_LOADS();
      if (i == MP + 4) {
        if (UNLIKELY(!MODOK())) {
          unsigned it = 0;
          do { RETRY(1); __builtin_amdgcn_s_sleep(8); CBAR(); mv4 = LD128(mo, 0); } while (!MODOK() && ++it < SPIN_CAP);
        }
        if (lane >= 24 && lane < 48) *(LAS v2u*)(bn + 512 + (lane - 24) * 8) = v2u{pkhi(mv4[0], mv4[1]), pkhi(mv4[2], mv4[3])};
        else LDS128(bn, (lane < 24 ? lane : (lane & 3) + 24) * 16) = mv4 & 0xffff0000u;
        CBAR();
#pragma unroll
        for (int k = 0; k < 3; ++k) { sa[k] = LDSF4(bg, 128 * k); sb[k] = LDSF4(bg, 128 * k + 16); tf[k] = LDS128(bn, 512 + 64 * k + 16 * g); }
        oc_ = *(LAS float*)(bn + 384 + n16 * 4);
      }
      if (i == MP + 5) { if (w < 4) oc_ *= 0.05103103630798288f * 1.4426950408889634f; sa[0] = opq(sa[0] * oc_); sb[0] = opq(sb[0] * oc_); }
      if (i == MP + 6) { sa[1] = opq(sa[1] * oc_); sb[1] = opq(sb[1] * oc_); }
      if (i == MP + 7) { sa[2] = opq(sa[2] * oc_); sb[2] = opq(sb[2] * oc_); }
      asm volatile("" : "+v"(hx));
      SB();
    }
    STAMP(6);
#define FBAD() (wsum(sum4(sum4(sum4(-mr, mf[0]), mf[1]), mf[2])) + 6u * tag != 0u)
    if (UNLIKELY(FBAD())) {
      unsigned it = 0;
      do { RETRY(9); __builtin_amdgcn_s_sleep(6); CBAR(); HOP1_LOADS(); } while (FBAD() && ++it < SPIN_CAP);
    }
    STAMP(7);
    {
      f32x4 au = {};
#pragma unroll
      for (int i = 0; i < 3; ++i) {
        au = mfma(tf[i], mf[i], au);
        const v4u o = {cvtpk(bflo(mf[i][0]) * sa[i][0], bfhi(mf[i][0]) * sa[i][1]), cvtpk(bflo(mf[i][1]) * sa[i][2], bfhi(mf[i][1]) * sa[i][3]),
                       cvtpk(bflo(mf[i][2]) * sb[i][0], bfhi(mf[i][2]) * sb[i][1]), cvtpk(bflo(mf[i][3]) * sb[i][2], bfhi(mf[i][3]) * sb[i][3])};
        LDS128(msw[i], 0) = o;
        if (NX1 + i < 24) XLOAD(NX1 + i);
        SB();
      }
      *(LAS float*)(csw) = au[0] * oc_;
    }
    STAMP(9);
    BAR();
    v4u a[24];
    f32x4 uu[4]; float zz[4], zs;
#define AREAD(i) a[i] = LDS128(fl, (((i) & 1) * 12 + ((i) >> 1)) * 1024)
#pragma unroll
    for (int i = 0; i < PD; ++i) AREAD(i);
    SB();
    f32x4 aq[2] = {}, av[2] = {};
#pragma unroll
    for (int i = 0; i < 24; ++i) {
      if (i + PD < 24) AREAD(i + PD);
      if (i >= 4 && i < 8) { uu[i - 4] = LDSF4(cg, (i - 4) * 64); zz[i - 4] = *(LAS float*)(cn + (i - 4) * 64); }
      if (NX1 + 3 + i < 24) XLOAD(NX1 + 3 + i);
      if (i & 1) { av[0] = mfma(xf[0][i >> 1], a[i], av[0]); av[1] = mfma(xf[1][i >> 1], a[i], av[1]); }
      else { aq[0] = mfma(a[i], xf[0][i >> 1], aq[0]); aq[1] = mfma(a[i], xf[1][i >> 1], aq[1]); }
      if (i >= NX1 - 6) hx = sum4(hx, XF(i));
      if (i == 12) { uu[0] = uu[0] + uu[1]; uu[2] = uu[2] + uu[3]; }
      if (i == 13) { uu[0] = uu[0] + uu[2]; zs = (zz[0] + zz[1]) + (zz[2] + zz[3]); }
      asm volatile("" : "+v"(hx));
      SB();
    }
    if (UNLIKELY(wsum(hx) + 8u * tag != 0u)) {
      unsigned it = 0;
      do { RETRY(4); __builtin_amdgcn_s_sleep(4); CBAR();
        hx = -nr[0];
#pragma unroll
        for (int i = 0; i < 24; ++i) XLOAD(i);
#pragma unroll
        for (int i = 0; i < 24; ++i) hx = sum4(hx, XF(i));
      } while (wsum(hx) + 8u * tag != 0u && ++it < SPIN_CAP);
      aq[0] = aq[1] = av[0] = av[1] = f32x4{0.f, 0.f, 0.f, 0.f};
#pragma unroll
      for (int s = 0; s < 12; ++s) {
        const v4u a0 = LDS128(fl, s * 1024), a1 = LDS128(fl, (12 + s) * 1024);
        aq[0] = mfma(a0, xf[0][s], aq[0]); aq[1] = mfma(a0, xf[1][s], aq[1]);
        av[0] = mfma(xf[0][s], a1, av[0]); av[1] = mfma(xf[1][s], a1, av[1]);
      }
    }
    {
      const f32x4 v0 = aq[0] + uu[0], v1 = aq[1] + uu[0];
      auto r0 = __builtin_amdgcn_permlane16_swap(cvtpk(v0[0], v0[1]), cvtpk(v1[0], v1[1]), false, false);
      auto r1 = __builtin_amdgcn_permlane16_swap(cvtpk(v0[2], v0[3]), cvtpk(v1[2], v1[3]), false, false);
      const v4u ov = {r0[0], r1[0], r0[1], r1[1]};
      ST128(ov, qso);
      const f32x4 w0 = av[0] + zs, w1 = av[1] + zs;
      const v4u o = {cvtpk(w0[0], w0[1]), cvtpk(w0[2], w0[3]), cvtpk(w1[0], w1[1]), cvtpk(w1[2], w1[3])};
      ST32(scan63(sum4(0u, ov)) + tag, rso);
      ST128(o, vso);
      ST32(scan63(sum4(0u, o)) + tag, rvo);
    }
    STAMP(10);
    SB();
  }
  STAMP(11);
  if (tid == 448) __hip_atomic_store((gu32*)(ws + O_CL + bid * 128), tag, __ATOMIC_RELAXED, __HIP_MEMORY_SCOPE_AGENT);
  const int vro = opq(lane < 24 ? O_RV + (((b * 4 + kb) * 8 + w) * 24 + lane) * 128 : OOB);
  const int mso = opq((w == 0 && g < 2) ? O_ML + ((qt * 4 + kb) * 32 + lane) * 8 : OOB);
  const int s_vl = sopq(O_VF + ((b * 32 + 8 * kb) * 24 + 3 * w) * 1024);
  const bool own = (w >> 1) == kb;
  const int oso = opq((own ? OOB : 0) + O_OF + ((qt * 24 + 3 * w) * 4 + kb) * 1024 + ((g & 1) * 32 + (g >> 1) * 16 + n16) * 16);
  const int roo = opq(((own ? OOB : 0) | l63) + O_RO + ((qt * 24 + 3 * w) * 4 + kb) * 128);
  lp mxr = opq(lds + L_MX + n16 * 32), mxw = opq(lds + L_MX + n16 * 32 + w * 4);
  lp pl = opq(lds + lane * 16);
  const int ooff = opq(((qt * 32 + 8 * (n16 >> 2) + 4 * (lane >> 5) + (n16 & 3)) * 384 + (6 * kb + w) * 16 + 8 * (g & 1)) * 2);
  const v4u xr = __builtin_amdgcn_raw_buffer_load_b128(rx, w < 6 ? ooff : OOB, 0, 0);
  const int mlo = opq(O_ML + (qt * 128 + (lane >> 5) * 16 + n16) * 8);
  const int s_ol = sopq(O_OF + (qt * 24 + 6 * kb + w) * 4096), rlo = opq((lane < 4 && lane != kb) ? O_RO + ((qt * 24 + 6 * kb + w) * 4 + lane) * 128 : OOB);
  v4u vf[8][3];
#define VF(i) vf[(i) & 7][(i) >> 3]
#define VLOAD(i) VF(i) = LD128(l16 + ((i) >> 3) * 1024, s_vl + ((i) & 7) * 24576)
  {
    lp qw = opq(lds + L_QS + (lane >> 5) * 12288 + 3 * w * 512 + (lane & 31) * 16);
    SB();
    if (DLY2) __builtin_amdgcn_s_sleep(DLY2);
    HOP2_LOADS();
#pragma unroll
    for (int i = 0; i < NV0; ++i) VLOAD(i);
    SB();
#define QBAD() (wsum(sum4(sum4(sum4(-qr, qf[0]), qf[1]), qf[2])) + 3u * tag != 0u)
#pragma unroll
    for (int i = 0; i < 3; ++i) LDS128(qw, i * 512) = qf[i];
    if (UNLIKELY(QBAD())) {
      unsigned it = 0;
      do { RETRY(2); __builtin_amdgcn_s_sleep(3); CBAR(); HOP2_LOADS(); } while (QBAD() && ++it < SPIN_CAP);
#pragma unroll
      for (int i = 0; i < 3; ++i) LDS128(qw, i * 512) = qf[i];
    }
  }
  STAMP(13);
  BAR();
  float m[2]; unsigned vr;
  {
    v4u q[24];
#define QREAD(i) q[i] = LDS128(fl, 24576 + (((i) & 1) * 12 + ((i) >> 1)) * 1024)
#pragma unroll
    for (int i = 0; i < PD; ++i) QREAD(i);
    SB();
    f32x4 acc[2][2] = {};
#pragma unroll
    for (int i = 0; i < 24; ++i) {
      if (i + PD < 24) QREAD(i + PD);
      acc[i & 1][0] = mfma(xf[0][i >> 1], q[i], acc[i & 1][0]); acc[i & 1][1] = mfma(xf[1][i >> 1], q[i], acc[i & 1][1]);
      if ((i * NVS) / 24 != ((i + 1) * NVS) / 24) VLOAD(NV0 + (i * NVS) / 24);
      SB();
    }
#pragma unroll
    for (int u = 0; u < 2; ++u) {
      float mx = max3(max3(acc[u][0][0], acc[u][0][1], acc[u][0][2]), acc[u][0][3], acc[u][1][0]);
      mx = max_g(max2(max3(mx, acc[u][1][1], acc[u][1][2]), acc[u][1][3]));
      *(LAS float*)(mxw + u * 512) = mx;
    }
    STAMP(14);
    BAR();
    f32x4 ma[2], mb[2];
#pragma unroll
    for (int u = 0; u < 2; ++u) { ma[u] = LDSF4(mxr, u * 512); mb[u] = LDSF4(mxr, u * 512 + 16); }
#pragma unroll
    for (int i = NV0 + NVS; i < NV2; ++i) VLOAD(i);
    vr = LD32(vro);
    SB();
    unsigned one2 = 0x3f803f80u;
    asm volatile("" : "+s"(one2));
#pragma unroll
    for (int u = 0; u < 2; ++u) {
      m[u] = max2(max3(max3(ma[u][0], ma[u][1], ma[u][2]), ma[u][3], mb[u][0]), max3(mb[u][1], mb[u][2], mb[u][3]));
      float p[8];
#pragma unroll
      for (int i = 0; i < 8; ++i) p[i] = __builtin_amdgcn_exp2f(acc[u][i >> 2][i & 3] - m[u]);
      const v4u o = {cvtpk(p[0], p[1]), cvtpk(p[2], p[3]), cvtpk(p[4], p[5]), cvtpk(p[6], p[7])};
      LDS128(pl, (w * 2 + u) * 1024) = o;
      *(LAS float*)(mxw + (L_LS - L_MX) + u * 512) = sum_g(dot2(o[0], one2, dot2(o[1], one2, 0.f)) + dot2(o[2], one2, dot2(o[3], one2, 0.f)));
    }
  }
  STAMP(15);
  BAR();
  {
    v4u pf[16];
    f32x4 acc[3][2] = {}, la[2], lb[2];
    float l[2], il[2];
    v4u ov[3];
    unsigned hv[3];
#define OSTORE(et) do { const f32x4 v0 = acc[et][0] * il[0], v1 = acc[et][1] * il[1]; \
      auto r0 = __builtin_amdgcn_permlane16_swap(cvtpk(v0[0], v0[1]), cvtpk(v1[0], v1[1]), false, false); \
      auto r1 = __builtin_amdgcn_permlane16_swap(cvtpk(v0[2], v0[3]), cvtpk(v1[2], v1[3]), false, false); \
      ov[et] = v4u{r0[0], r1[0], r0[1], r1[1]}; ST128S(ov[et], oso, (et) * 4096); \
      if (own) LDS128(lds + 16384 + ((w & 1) * 3 + (et)) * 1024, ((g & 1) * 32 + (g >> 1) * 16 + n16) * 16) = ov[et]; } while (0)
#define ORECORD(et) ST32(scan63(sum4(0u, ov[et])) + tag, roo + (et) * 512)
#define VCHECK(et) do { if (UNLIKELY(wsum(hv[et] - ((lane >> 3) == (et) ? vr : 0u)) + 8u * tag != 0u)) { unsigned it = 0; \
        do { RETRY(3); __builtin_amdgcn_s_sleep(2); CBAR(); \
          _Pragma("unroll") for (int i = 0; i < 8; ++i) VLOAD(8 * (et) + i); \
          vr = LD32(vro); hv[et] = 0; \
          _Pragma("unroll") for (int i = 0; i < 8; ++i) hv[et] = sum4(hv[et], VF(8 * (et) + i)); \
        } while (wsum(hv[et] - ((lane >> 3) == (et) ? vr : 0u)) + 8u * tag != 0u && ++it < SPIN_CAP); \
        acc[et][0] = acc[et][1] = f32x4{0.f, 0.f, 0.f, 0.f}; \
        _Pragma("unroll") for (int i = 0; i < 16; ++i) acc[et][i & 1] = mfma(vf[i >> 1][et], pf[i], acc[et][i & 1]); } } while (0)
#pragma unroll
    for (int i = 0; i < 8; ++i) pf[i] = LDS128(pl, i * 1024);
    SB();
    hv[0] = hv[1] = hv[2] = 0;
#pragma unroll
    for (int i = 0; i < 16; ++i) {
      if (i + 8 < 16) pf[i + 8] = LDS128(pl, (i + 8) * 1024);
      if (i == 8 || i == 9) { la[i & 1] = LDSF4(mxr, (L_LS - L_MX) + (i & 1) * 512); lb[i & 1] = LDSF4(mxr, (L_LS - L_MX) + (i & 1) * 512 + 16); }
      if ((i & 1) && NV2 + (i >> 1) < 24) VLOAD(NV2 + (i >> 1));
      acc[0][i & 1] = mfma(vf[i >> 1][0], pf[i], acc[0][i & 1]);
      if (i & 1) hv[0] = opq(sum4(hv[0], vf[i >> 1][0]));
      SB();
    }
#pragma unroll
    for (int u = 0; u < 2; ++u) { const f32x4 t = la[u] + lb[u]; l[u] = (t[0] + t[1]) + (t[2] + t[3]); il[u] = __builtin_amdgcn_rcpf(l[u]); }
    ST64((v2u{__float_as_uint((g ? m[1] : m[0]) + __builtin_amdgcn_logf(g ? l[1] : l[0])), tag}), mso);
    VCHECK(0);
#pragma unroll
    for (int i = 0; i < 16; ++i) {
      acc[1][i & 1] = mfma(vf[i >> 1][1], pf[i], acc[1][i & 1]);
      if (i & 1) hv[1] = opq(sum4(hv[1], vf[i >> 1][1]));
      if (i == 0) OSTORE(0);
      if (i == 6) ORECORD(0);
      if ((i & 3) == 3) SB();
    }
    VCHECK(1);
#pragma unroll
    for (int i = 0; i < 16; ++i) {
      acc[2][i & 1] = mfma(vf[i >> 1][2], pf[i], acc[2][i & 1]);
      if (i & 1) hv[2] = opq(sum4(hv[2], vf[i >> 1][2]));
      if (i == 0) OSTORE(1);
      if (i == 6) ORECORD(1);
      if ((i & 3) == 3) SB();
    }
    VCHECK(2);
    STAMP(16);
    OSTORE(2);
    ORECORD(2);
  }
  STAMP(17);
  BAR();
  if (w < 6) {
    v4u pp[4]; v2u ml[4]; unsigned orr;
#define ML_LOADS() do { _Pragma("unroll") for (int k = 0; k < 4; ++k) ml[k] = __builtin_amdgcn_raw_buffer_load_b64(rws, mlo, ((kb + k) & 3) * 256, AUX_SC1); } while (0)
#define MLOK() __all(((ml[0][1] ^ tag) | (ml[1][1] ^ tag) | (ml[2][1] ^ tag) | (ml[3][1] ^ tag)) == 0u)
#define HOP3_LOADS() do { _Pragma("unroll") for (int k = 1; k < 4; ++k) pp[k] = LD128(l16, s_ol + ((kb + k) & 3) * 1024); orr = LD32(rlo); } while (0)
#define OBAD() (wsum(sum4(sum4(sum4(-orr, pp[1]), pp[2]), pp[3])) + 3u * tag != 0u)
    ML_LOADS();
    pp[0] = LDS128(pl, 16384 + w * 1024);
    SB();
    if (DLY3) __builtin_amdgcn_s_sleep(DLY3);
    HOP3_LOADS();
    SB();
    STAMP(18);
    if (UNLIKELY(!MLOK())) {
      unsigned it = 0;
      do { RETRY(6); __builtin_amdgcn_s_sleep(2); CBAR(); ML_LOADS(); } while (!MLOK() && ++it < SPIN_CAP);
    }
    const float mm = max2(max2(__uint_as_float(ml[0][0]), __uint_as_float(ml[1][0])), max2(__uint_as_float(ml[2][0]), __uint_as_float(ml[3][0])));
    float wk[4], o[8];
#pragma unroll
    for (int k = 0; k < 4; ++k) wk[k] = __builtin_amdgcn_exp2f(__uint_as_float(ml[k][0]) - mm);
    const float inv = __builtin_amdgcn_rcpf((wk[0] + wk[1]) + (wk[2] + wk[3]));
#pragma unroll
    for (int k = 0; k < 4; ++k) wk[k] *= inv;
#pragma unroll
    for (int e = 0; e < 4; ++e) { o[2 * e] = opq(fmaf(wk[0], bflo(pp[0][e]), bflo(xr[e]))); o[2 * e + 1] = opq(fmaf(wk[0], bfhi(pp[0][e]), bfhi(xr[e]))); }
    SB();
    if (UNLIKELY(OBAD())) {
      unsigned it = 0;
      do { RETRY(5); __builtin_amdgcn_s_sleep(3); CBAR(); HOP3_LOADS(); } while (OBAD() && ++it < SPIN_CAP);
    }
    STAMP(1);
    v4u ov;
#pragma unroll
    for (int e = 0; e < 4; ++e) {
#pragma unroll
      for (int k = 1; k < 4; ++k) { o[2 * e] = fmaf(wk[k], bflo(pp[k][e]), o[2 * e]); o[2 * e + 1] = fmaf(wk[k], bfhi(pp[k][e]), o[2 * e + 1]); }
      ov[e] = cvtpk(o[2 * e], o[2 * e + 1]);
    }
    *(v4u*)((unsigned char*)out + ooff) = ov;
  } else if (UNLIKELY(bid == 255 && w == 7)) {
#pragma unroll 1
    for (unsigned it = 0; it < SPIN_CAP; ++it) {
      CBAR();
      unsigned bad = 0;
#pragma unroll
      for (int k = 0; k < 4; ++k) bad |= LD32(O_CL + (k * 64 + lane) * 128) ^ tag;
      if (__all(bad == 0)) break;
    }
    if (lane == 0) __hip_atomic_store((gu32*)(ws + O_EPOCH), tag, __ATOMIC_RELAXED, __HIP_MEMORY_SCOPE_AGENT);
  }
  STAMP(19);
  STFLUSH();
}

extern "C" void kernel_launch(void* const* d_in, const int* in_sizes, int n_in, void* d_out, int out_size, void* d_ws, size_t ws_size,
                              hipStream_t stream) {
  (void)hipFuncSetAttribute((const void*)dit_kernel, hipFuncAttributeMaxDynamicSharedMemorySize, L_TOTAL);
  dit_kernel<<<256, 512, L_TOTAL, stream>>>((const u16*)d_in[0], (const u16*)d_in[1], (const u16*)d_in[2], (const u16*)d_in[3],
                                            (const u16*)d_in[4], (unsigned char*)d_ws, (u16*)d_out);
}
```

```cpp
#include <hip/hip_runtime.h>
#include <hip/hip_bf16.h>

typedef short bf16x8 __attribute__((ext_vector_type(8)));
typedef short s16x4 __attribute__((ext_vector_type(4)));
typedef float f32x4 __attribute__((ext_vector_type(4)));
typedef float f32x2 __attribute__((ext_vector_type(2)));
typedef unsigned v4u __attribute__((ext_vector_type(4)));
typedef unsigned v2u __attribute__((ext_vector_type(2)));
typedef __bf16 bf2 __attribute__((ext_vector_type(2)));
typedef unsigned short u16;
#define DI __device__ __forceinline__
#define LAS __attribute__((address_space(3)))
#define GAS __attribute__((address_space(1)))
typedef GAS unsigned gu32;
typedef LAS unsigned char* lp;
#define STDECL
#define STAMP(i)
#define STFLUSH()
#define RETRY(k)
#define DLY0 30
#define DLY1 12
#define DLY2 0
#define PRE 1
#define HP 10
#define MP 8
#define TOUCH 1
#define CODEPF 21504
#define XS 0
#define DLY3 3
#define PD 4
#define SLP 2
#define ROT 0
#define NX1 19
#define NEARLY 0
#ifndef NV0
#define NV0 0
#endif
#define AUXO 16
#define NH 6
#ifndef NV2
#define NV2 18
#endif
#define HL 8
#define MC 14
#define DLY4 18
#define DLY5 4
#define BA 18
#define H2E 1
#define VS0 0
#ifndef NVS
#define NVS 13
#endif

constexpr int O_EPOCH = 0;
constexpr int O_MOD = 4096;
constexpr int O_CL = 16384;
constexpr int O_MT = 131072;
constexpr int O_NF = O_MT + 589824;
constexpr int O_QF = O_NF + 1572864;
constexpr int O_VF = O_QF + 1572864;
constexpr int O_OF = O_VF + 1572864;
constexpr int O_ML = O_OF + 6291456;
constexpr int O_RM = O_ML + 131072;
constexpr int O_RN = O_RM + 1152 * 128;
constexpr int O_RQ = O_RN + 512 * 128;
constexpr int O_RV = O_RQ + 1536 * 128;
constexpr int O_RO = O_RV + 1536 * 128;
constexpr int WS_BYTES = O_RO + 6144 * 128;
constexpr int OOB = 0x40000000;

constexpr int L_P0 = 0;
constexpr int L_MS = 61440, L_QS = L_MS + 24576, L_CS = L_QS + 24576, L_MX = L_CS + 512, L_LS = L_MX + 1024, L_BN = L_LS + 1024;
constexpr int L_SCR = L_BN + 6144, L_TAG = L_SCR + 256;
constexpr int L_TOTAL = L_TAG + 32;

static_assert(NV2 >= 16 && NV0 + NVS <= NV2 && NV2 <= 24, "");
static_assert(BA >= HL && ((BA - HL) & 1) == 0 && BA > 4 && BA <= 20 && MC >= 6 && MP >= 1 && HP >= 1 && MC >= 5 && MC + 3 < NX1 && MP < MC && HP < NX1 && NX1 >= 9 && NX1 <= 21, "");
static_assert(NVS <= 24 - VS0, "");
constexpr int AUX_SC1 = 16;
constexpr unsigned SPIN_CAP = 1u << 16;

DI float bflo(unsigned v) { return __uint_as_float(v << 16); }
DI float bfhi(unsigned v) { return __uint_as_float(v & 0xffff0000u); }
DI unsigned cvtpk(float lo, float hi) {
  f32x2 x = {lo, hi};
  return __builtin_bit_cast(unsigned, __builtin_convertvector(x, bf2));
}
DI float bfround(float x) { return bflo(cvtpk(x, 0.f)); }
DI unsigned pkhi(unsigned lo, unsigned hi) { return __builtin_amdgcn_perm(hi, lo, 0x07060302u); }
DI unsigned tagged(float v, unsigned tag16) { return (cvtpk(0.f, v) & 0xffff0000u) | tag16; }
DI unsigned tchk(unsigned a, v4u v, unsigned tag) { return a | (v[0] ^ tag) | (v[1] ^ tag) | (v[2] ^ tag) | (v[3] ^ tag); }
DI unsigned sum4(unsigned a, v4u v) { return a + v[0] + v[1] + v[2] + v[3]; }
DI unsigned scan63(unsigned v) {
  v += __builtin_amdgcn_update_dpp(0u, v, 0x111, 0xf, 0xf, true);
  v += __builtin_amdgcn_update_dpp(0u, v, 0x112, 0xf, 0xf, true);
  v += __builtin_amdgcn_update_dpp(0u, v, 0x114, 0xf, 0xf, true);
  v += __builtin_amdgcn_update_dpp(0u, v, 0x118, 0xf, 0xf, true);
  v += __builtin_amdgcn_update_dpp(0u, v, 0x142, 0xa, 0xf, true);
  v += __builtin_amdgcn_update_dpp(0u, v, 0x143, 0xc, 0xf, true);
  return v;
}
DI float sum_g(float v);
DI float qsumf(float v) {
#define FSTEP(ctl) v += __uint_as_float(__builtin_amdgcn_update_dpp(0u, __float_as_uint(v), ctl, 0xf, 0xf, true))
  FSTEP(0x128); FSTEP(0x124);
#undef FSTEP
  return sum_g(v);
}
DI unsigned wsum(unsigned v) { return __builtin_amdgcn_readlane(scan63(v), 63); }
DI float max3(float a, float b, float c) { float r; asm("v_max3_f32 %0, %1, %2, %3" : "=v"(r) : "v"(a), "v"(b), "v"(c)); return r; }
DI float max2(float a, float b) { float r; asm("v_max_f32 %0, %1, %2" : "=v"(r) : "v"(a), "v"(b)); return r; }
DI float dot2(unsigned a, unsigned b, float c) { return __builtin_amdgcn_fdot2_f32_bf16(__builtin_bit_cast(bf2, a), __builtin_bit_cast(bf2, b), c, false); }
DI f32x4 mfma(v4u a, v4u b, f32x4 c) {
  return __builtin_amdgcn_mfma_f32_16x16x32_bf16(__builtin_bit_cast(bf16x8, a), __builtin_bit_cast(bf16x8, b), c, 0, 0, 0);
}
DI float sum_g(float v) {
  auto r = __builtin_amdgcn_permlane16_swap(__float_as_uint(v), __float_as_uint(v), false, false);
  v = __uint_as_float(r[0]) + __uint_as_float(r[1]);
  auto q = __builtin_amdgcn_permlane32_swap(__float_as_uint(v), __float_as_uint(v), false, false);
  return __uint_as_float(q[0]) + __uint_as_float(q[1]);
}
DI float max_g(float v) {
  auto r = __builtin_amdgcn_permlane16_swap(__float_as_uint(v), __float_as_uint(v), false, false);
  v = max2(__uint_as_float(r[0]), __uint_as_float(r[1]));
  auto q = __builtin_amdgcn_permlane32_swap(__float_as_uint(v), __float_as_uint(v), false, false);
  return max2(__uint_as_float(q[0]), __uint_as_float(q[1]));
}
DI int sopq(int v) { v = __builtin_amdgcn_readfirstlane(v); asm volatile("" : "+s"(v)); return v; }
template <typename T> DI T opq(T v) { asm volatile("" : "+v"(v)); return v; }
#define BAR() do { asm volatile("s_waitcnt lgkmcnt(0)" ::: "memory"); __builtin_amdgcn_s_barrier(); asm volatile("" ::: "memory"); } while (0)
#define VMWAIT(n) asm volatile("s_waitcnt vmcnt(%0)" :: "i"(n) : "memory")
#define SB() __builtin_amdgcn_sched_barrier(0)
#define CBAR() asm volatile("" ::: "memory")
#define LD128(off, so) __builtin_amdgcn_raw_buffer_load_b128(rws, (off), (so), AUX_SC1)
#define LD64(off) __builtin_amdgcn_raw_buffer_load_b64(rws, (off), 0, AUX_SC1)
#define LD32(off) __builtin_amdgcn_raw_buffer_load_b32(rws, (off), 0, AUX_SC1)
#define ST128(v, off) __builtin_amdgcn_raw_buffer_store_b128((v), rws, (off), 0, AUX_SC1)
#define ST128S(v, off, so) __builtin_amdgcn_raw_buffer_store_b128((v), rws, (off), (so), AUX_SC1)
#define ST64(v, off) __builtin_amdgcn_raw_buffer_store_b64((v), rws, (off), 0, AUX_SC1)
#define ST32(v, off) __builtin_amdgcn_raw_buffer_store_b32((v), rws, (off), 0, AUX_SC1)
#define ST16(v, off) __builtin_amdgcn_raw_buffer_store_b16((v), rws, (off), 0, AUX_SC1)
#define LDS128(p, off) (*(LAS v4u*)((p) + (off)))
#define LDSF4(p, off) (*(LAS f32x4*)((p) + (off)))
DI void dma(unsigned ldsaddr, int voff, __amdgpu_buffer_rsrc_t rs, int soff) {
  asm volatile("s_mov_b32 m0, %0\n\ts_nop 0\n\tbuffer_load_dwordx4 %1, %2, %3 offen lds" :: "s"(ldsaddr), "v"(voff), "s"(rs), "s"(soff) : "memory");
}

DI void touch(unsigned ldsaddr, int voff, __amdgpu_buffer_rsrc_t rs) {
  asm volatile("s_mov_b32 m0, %0\n\ts_nop 0\n\tbuffer_load_dword %1, %2, 0 offen lds" :: "s"(ldsaddr), "v"(voff), "s"(rs) : "memory");
}
#define UNLIKELY(c) __builtin_expect(!!(c), 0)

__global__ __launch_bounds__(512, 2) void dit_kernel(const u16* __restrict__ x, const u16* __restrict__ c, const u16* __restrict__ wqkv,
                                                     const u16* __restrict__ wo, const u16* __restrict__ wmod, unsigned char* ws,
                                                     u16* __restrict__ out) {
  extern __shared__ __attribute__((aligned(16))) unsigned char lds_[];
  lp lds = (lp)lds_;
  const unsigned ldsb = (unsigned)(size_t)lds;
  STDECL;
  STAMP(0);
  const unsigned long long pc0 = __builtin_amdgcn_s_getpc();
  const int tid = threadIdx.x, lane = tid & 63, w = __builtin_amdgcn_readfirstlane(tid >> 6);
  const int n16 = lane & 15, g = lane >> 4;
  const int bid = blockIdx.x, xg = bid & 7, r = bid >> 3;
  const int b = xg >> 2, ql = r & 7, kb = r >> 3;
  const int qt = b * 32 + (xg & 3) * 8 + ql;
  const int qt3 = b * 32 + (((xg & 3) + XS) & 3) * 8 + ql;
  const bool act1 = ql < 6;
  const int cb = (xg & 3) * 6 + (act1 ? ql : ql - 2);
  const __amdgpu_buffer_rsrc_t rws = __builtin_amdgcn_make_buffer_rsrc(ws, 0, WS_BYTES, 0x00020000);
  const __amdgpu_buffer_rsrc_t rx = __builtin_amdgcn_make_buffer_rsrc((void*)x, 0, 2048 * 768, 0x00020000);
  const int dch = 16 * ((lane & 3) ^ ((lane & 32) ? 3 : 0));
  const int rslot = (4 * n16 + (g ^ ((n16 & 8) ? 3 : 0))) * 16;
  const int l63 = lane == 63 ? 0 : OOB;

  const int mu = (((r - 24) >> 1) * 8 + xg) * 2 + (r & 1);
  const int role = r < 12 ? 0 : (r < 24 ? 1 : (mu < 36 ? 2 : 3));
  const int ru = role == 1 ? r - 12 : r;
  const int u12 = 6 * (xg & 1) + ru % 6, u8 = 2 * (xg >> 1) + ru / 6;
  unsigned ep;
  asm volatile("s_nop 4\n\tbuffer_load_dword %0, off, %1, 0 sc1" : "=v"(ep) : "s"(rws) : "memory");
  v4u nv[3];
  const int grp = bid * 2 + (w & 1);
  if (w < 6) {
    unsigned cv = 0;
    if (role == 2) cv = ((const unsigned*)c)[tid];
    if (role < 2) {
      const __amdgpu_buffer_rsrc_t rq = __builtin_amdgcn_make_buffer_rsrc((void*)wqkv, 0, 384 * 2304, 0x00020000);
      const int v = (lane >> 2) * 2304 + dch;
      const int s3 = 48 * u8 * 2304 + (role ? 1536 : 768) + 128 * w;
#pragma unroll
      for (int t = 0; t < 3; ++t) {
        dma(ldsb + L_P0 + (t * 12 + 2 * w) * 1024, v, rq, s3 + 16 * t * 2304);
        dma(ldsb + L_P0 + (t * 12 + 2 * w + 1) * 1024, v, rq, s3 + 16 * t * 2304 + 64);
      }
      if (role == 0) {
        const int s2 = 32 * u12 * 2304 + 128 * w;
#pragma unroll
        for (int t = 0; t < 2; ++t) {
          dma(ldsb + L_P0 + ((3 + t) * 12 + 2 * w) * 1024, v, rq, s2 + 16 * t * 2304);
          dma(ldsb + L_P0 + ((3 + t) * 12 + 2 * w + 1) * 1024, v, rq, s2 + 16 * t * 2304 + 64);
        }
      }
    }
    if (role == 1 || role == 2) {
      const int ld = role == 1 ? 768 : 4608;
      const __amdgpu_buffer_rsrc_t rc = __builtin_amdgcn_make_buffer_rsrc((void*)(role == 1 ? wo : wmod), 0, 384 * ld, 0x00020000);
      const int v = (lane >> 2) * ld + dch;
#pragma unroll
      for (int j = 0; j < 4; ++j) dma(ldsb + L_P0 + (36 + 4 * w + j) * 1024, v, rc, (role == 1 ? u12 : mu) * 64 + (4 * w + j) * 16 * ld);
    }
    if (role == 2) {
      const float f0 = bflo(cv), f1 = bfhi(cv);
      const int bb = tid >= 192, i2 = 2 * (tid - 192 * bb);
      *(LAS unsigned*)(lds + L_P0 + (i2 >> 5) * 1024 + (bb * 4 + ((i2 >> 3) & 3)) * 16 + (i2 & 7) * 2) =
          cvtpk(f0 * __builtin_amdgcn_rcpf(1.f + __expf(-f0)), f1 * __builtin_amdgcn_rcpf(1.f + __expf(-f1)));
    }
    VMWAIT(0);
  } else {
    __builtin_amdgcn_s_setprio(3);
    v4u xs[3];
    asm volatile("buffer_load_dwordx4 %0, %3, %4, 0 offen\n\tbuffer_load_dwordx4 %1, %3, %4, 0 offen offset:256\n\tbuffer_load_dwordx4 %2, %3, %4, 0 offen offset:512"
                 : "=&v"(xs[0]), "=&v"(xs[1]), "=&v"(xs[2]) : "v"((4 * grp + (lane & 3)) * 768 + (lane >> 2) * 16), "s"(rx) : "memory");
    {
      const unsigned scr = ldsb + L_SCR;
      const int dead = ((TOUCH & 1) && lane < 36) ? 0 : OOB;
      const __amdgpu_buffer_rsrc_t rq = __builtin_amdgcn_make_buffer_rsrc((void*)wqkv, 0, 384 * 2304, 0x00020000);
      touch(scr, dead + (w == 6 ? (96 * (xg >> 1) + 3 * r + lane / 12) * 2304 + 768 + (lane % 12) * 128 : (192 * (xg & 1) + 6 * r + lane / 6) * 2304 + (lane % 6) * 128), rq);
      const __amdgpu_buffer_rsrc_t r2 = __builtin_amdgcn_make_buffer_rsrc(w == 6 ? (void*)wo : (void*)(pc0 & ~127ull), 0, w == 6 ? 384 * 768 : CODEPF, 0x00020000);
      touch(scr, w == 6 ? dead + (12 * r + lane / 3) * 768 + 384 * (xg & 1) + (lane % 3) * 128 : (lane < 6 ? (r * 6 + lane) * 128 : OOB), r2);
    }
    if (w == 7) {
      const __amdgpu_buffer_rsrc_t r3 = __builtin_amdgcn_make_buffer_rsrc((void*)wmod, 0, 384 * 4608, 0x00020000);
      touch(ldsb + L_SCR, (lane < (xg < 2 ? 36 : 24) ? 0 : OOB) + (12 * r + lane % 12) * 4608 + ((lane / 12) * 8 + xg) * 128, r3);
    }
    else touch(ldsb + L_SCR, OOB, rx);
    asm volatile("s_waitcnt vmcnt(3)" : "+v"(xs[0]), "+v"(xs[1]), "+v"(xs[2]) :: "memory");
    float f[24], sm = 0.f, q = 0.f;
#pragma unroll
    for (int i = 0; i < 12; ++i) { f[2 * i] = bflo(xs[i >> 2][i & 3]); f[2 * i + 1] = bfhi(xs[i >> 2][i & 3]); sm += f[2 * i] + f[2 * i + 1]; }
    sm = qsumf(sm) * (1.f / 384.f);
#pragma unroll
    for (int i = 0; i < 24; ++i) { f[i] -= sm; q = fmaf(f[i], f[i], q); }
    const float rs = __builtin_amdgcn_rsqf(qsumf(q) * (1.f / 384.f) + 1e-5f);
#pragma unroll
    for (int i = 0; i < 3; ++i)
      nv[i] = opq(v4u{cvtpk(f[8 * i] * rs, f[8 * i + 1] * rs), cvtpk(f[8 * i + 2] * rs, f[8 * i + 3] * rs), cvtpk(f[8 * i + 4] * rs, f[8 * i + 5] * rs), cvtpk(f[8 * i + 6] * rs, f[8 * i + 7] * rs)});
  }
  asm volatile("" : "+v"(ep));
  const unsigned tag = __builtin_amdgcn_readfirstlane(ep) + 1u;
#define NSTORE() do { if (w >= 6) {     \
    const int no = O_NF + (((grp >> 3) * 2 + (grp & 1)) * 12 + g) * 1024 + (((lane >> 2) & 3) * 16 + ((grp >> 1) & 3) * 4 + (lane & 3)) * 16; \
    _Pragma("unroll") for (int i = 0; i < 3; ++i) ST128(nv[i], no + i * 4096); \
    ST32(scan63(sum4(sum4(sum4(0u, nv[0]), nv[1]), nv[2])) + tag, l63 + O_RN + grp * 128); \
    __builtin_amdgcn_s_setprio(0); } } while (0)
  if (NEARLY || role >= 2) NSTORE();
  STAMP(2);
  BAR();
  STAMP(3);
  if (!NEARLY && role < 2) NSTORE();
  const int rg = b * 32 + kb * 8 + w;
  const int nro = lane < 8 ? O_RN + (rg * 8 + lane) * 128 : OOB;
  if (w < (role == 2 ? 2 : 6) && role != 3) {
    const int t3 = w >> 1, t2 = w & 1;
    f32x4 acc = {};
    v4u a[12], bq[12];
    if (role == 0) {
      lp ap = opq(lds + L_P0 + (3 + t2) * 12288 + rslot), bp = opq(lds + L_P0 + t3 * 12288 + rslot);
#pragma unroll
      for (int k = 0; k < 12; ++k) {
        a[k] = LDS128(ap, k * 1024); bq[k] = LDS128(bp, k * 1024);
        if (k >= 5) acc = mfma(a[k - 5], bq[k - 5], acc);
        SB();
      }
    } else {
      lp ap = opq(lds + L_P0 + (role == 2 ? ((n16 & 1) * 4 + g) * 16 : t3 * 12288 + rslot));
      lp tb = opq(lds + L_P0 + 36864 + (8 * g + (n16 >> 2)) * 64 + 16 * ((2 * t2 + ((n16 >> 1) & 1)) ^ ((g & 1) ? 3 : 0)) + 8 * (n16 & 1));
#pragma unroll
      for (int k = 0; k < 12; ++k) {
        a[k] = LDS128(ap, k * 1024);
        bq[k] = __builtin_bit_cast(v4u, __builtin_shufflevector(__builtin_amdgcn_ds_read_tr16_b64_v4i16((LAS s16x4*)(tb + k * 2048)),
                                                                __builtin_amdgcn_ds_read_tr16_b64_v4i16((LAS s16x4*)(tb + k * 2048 + 256)), 0, 1, 2, 3, 4, 5, 6, 7));
        if (k >= 5) acc = mfma(a[k - 5], bq[k - 5], acc);
        SB();
      }
    }
#pragma unroll
    for (int k = 7; k < 12; ++k) acc = mfma(a[k], bq[k], acc);
    if (role != 2) {
      const int blk = role * 576 + (role == 0 ? (3 * u8 + t3) * 24 + 2 * u12 + t2 : (2 * u12 + t2) * 24 + 3 * u8 + t3);
      const unsigned o0 = cvtpk(acc[0], acc[1]), o1 = cvtpk(acc[2], acc[3]);
      auto r0 = __builtin_amdgcn_permlane16_swap(o0, o0, false, false);
      auto r1 = __builtin_amdgcn_permlane16_swap(o1, o1, false, false);
      ST128((v4u{r0[0], r1[0], r0[1], r1[1]}), ((g & 1) ? OOB : 0) + O_MT + blk * 512 + ((g >> 1) * 16 + n16) * 16);
      ST32(scan63(o0 + o1) + tag, l63 + O_RM + blk * 128);
    } else if (g < 2) {
      float v = bfround(g ? acc[1] : acc[0]);
      if (mu >= 12 && mu < 24) v += 1.f;
      ST32(tagged(v, tag & 0xffffu), O_MOD + (g * 1152 + 32 * mu + 16 * t2 + n16) * 4);
    }
  }
  STAMP(4);
  if (role == 3) __builtin_amdgcn_s_sleep(DLY0);
  else if (role == 2) { if (w >= 2) __builtin_amdgcn_s_sleep(DLY4); }
  else if (w >= 6) __builtin_amdgcn_s_sleep(DLY1);
  if (DLY5) __builtin_amdgcn_s_sleep(DLY5);
  const int l16 = opq(lane * 16), l16b = opq(lane * 16 + 12288);
  const int s_n = O_NF + rg * 24576;
  v4u xf[2][12];
#define XF(i) xf[(i) & 1][(i) >> 1]
#define XLOAD(i) XF(i) = LD128((((i) & 1) ? l16b : l16) + (((i) >> 1) & 3) * 1024, s_n + ((i) >> 3) * 4096)
  lp fl = opq(lds + L_MS + lane * 16);
  v4u qf[3]; unsigned qr; int qro, s_ql;
#define HOP2_LOADS() do { _Pragma("unroll") for (int i = 0; i < 3; ++i) qf[i] = LD128(l16 + i * 1024, s_ql); qr = LD32(qro); } while (0)
  {
    int qso, vso, rso, rvo; lp cg, cn;
    unsigned nr, hx = 0;
    v4u a[24];
    f32x4 uu[4], aq[2] = {}, av[2] = {};
    STAMP(5);
#define SETUP(i) do { \
      if ((i) == 2) { const int blk1 = rg * 24 + cb, dead1 = act1 ? 0 : OOB;     \
        qso = opq(dead1 + O_QF + blk1 * 1024 + ((g & 1) * 32 + (g >> 1) * 16 + n16) * 16); \
        vso = opq(dead1 + O_VF + blk1 * 1024 + lane * 16); rso = opq((dead1 | l63) + O_RQ + blk1 * 128); } \
      if ((i) == 3) { rvo = opq(((act1 ? 0 : OOB) | l63) + O_RV + (((b * 4 + kb) * 24 + cb) * 8 + w) * 128); \
        qro = opq(lane < 3 ? O_RQ + (qt * 24 + 3 * w + lane) * 128 : OOB); s_ql = sopq(O_QF + (qt * 24 + 3 * w) * 1024); } \
      if ((i) == 4) { cg = opq(lds + L_CS + g * 16); cn = opq(lds + L_CS + 256 + n16 * 4); } } while (0)
#define QOP(k) do { if ((k) + PD < 12) a[(k) + PD] = LDS128(fl, ((k) + PD) * 1024); \
      if ((k) >= 2 && (k) < 6) uu[(k) - 2] = LDSF4(cg, ((k) - 2) * 64); \
      aq[0] = mfma(a[k], xf[0][k], aq[0]); aq[1] = mfma(a[k], xf[1][k], aq[1]); \
      if ((k) == 9) { uu[0] = uu[0] + uu[1]; uu[2] = uu[2] + uu[3]; } \
      if ((k) == 10) uu[0] = uu[0] + uu[2]; } while (0)
#define QFINISH() do { \
      if (UNLIKELY(wsum(hx - nr) + 8u * tag != 0u)) { unsigned it = 0; \
        do { RETRY(4); __builtin_amdgcn_s_sleep(4); CBAR(); hx = -LD32(nro); \
          _Pragma("unroll") for (int i = 0; i < 24; ++i) XLOAD(i); \
          _Pragma("unroll") for (int i = 0; i < 24; ++i) hx = sum4(hx, XF(i)); \
        } while (wsum(hx) + 8u * tag != 0u && ++it < SPIN_CAP); \
        aq[0] = aq[1] = f32x4{0.f, 0.f, 0.f, 0.f}; \
        _Pragma("unroll") for (int s = 0; s < 12; ++s) { const v4u a0 = LDS128(fl, s * 1024); aq[0] = mfma(a0, xf[0][s], aq[0]); aq[1] = mfma(a0, xf[1][s], aq[1]); } } \
      const f32x4 v0 = aq[0] + uu[0], v1 = aq[1] + uu[0]; \
        \
      auto r0 = __builtin_amdgcn_permlane16_swap(cvtpk(v0[0], v0[1]), cvtpk(v1[0], v1[1]), false, false); \
      auto r1 = __builtin_amdgcn_permlane16_swap(cvtpk(v0[2], v0[3]), cvtpk(v1[2], v1[3]), false, false); \
      const v4u ov = {r0[0], r1[0], r0[1], r1[1]}; \
      ST128(ov, qso); ST32(scan63(sum4(0u, ov)) + tag, rso); } while (0)
    if (w < 4) {
      int mo, ro, s_f; lp csw, bn, bg, msw;
      v4u mf[6], mv4; unsigned mr;
      f32x4 sa[3], sb[3], sc[6]; v4u tf[3]; float oc_, og_;
#define MODOK() __all((tchk(0u, mv4, tag) & 0xffffu) == 0u)
#define HOP1_LOADS(h) do { _Pragma("unroll") for (int i = 0; i < 3; ++i) mf[3 * (h) + i] = LD128(l16 + i * 1024, s_f + (h) * 294912); } while (0)
#define FBAD(h) (wsum(sum4(sum4(sum4((lane >> 3) == (h) ? -mr : 0u, mf[3 * (h)]), mf[3 * (h) + 1]), mf[3 * (h) + 2])) + 6u * tag != 0u)
#define STAGE(h, F, HOOK) do { \
        if (UNLIKELY(FBAD(h))) { unsigned it = 0; \
          do { RETRY(9); __builtin_amdgcn_s_sleep(6); CBAR(); HOP1_LOADS(h); mr = LD32(ro); } while (FBAD(h) && ++it < SPIN_CAP); } \
        f32x4 au = {};     \
        _Pragma("unroll") for (int i = 0; i < 3; ++i) { const v4u f_ = mf[3 * (h) + i]; const f32x4 a_ = sc[2 * i], b_ = sc[2 * i + 1]; \
          au = mfma(tf[i], f_, au); \
          LDS128(msw, (h) * 12288 + i * 1024) = v4u{cvtpk(bflo(f_[0]) * a_[0], bfhi(f_[0]) * a_[1]), cvtpk(bflo(f_[1]) * a_[2], bfhi(f_[1]) * a_[3]), \
                                                    cvtpk(bflo(f_[2]) * b_[0], bfhi(f_[2]) * b_[1]), cvtpk(bflo(f_[3]) * b_[2], bfhi(f_[3]) * b_[3])}; \
          HOOK; SB(); } \
        *(LAS float*)(csw + (h) * 256) = au[0] * (F); } while (0)
#pragma unroll
      for (int i = 0; i < NX1; ++i) {
        XLOAD(i);
        if (i == 0) {
          mo = opq(O_MOD + (b * 1152 + (lane < 24 ? 384 + 96 * w + 4 * lane : (lane < 48 ? 96 * w + 4 * (lane - 24) : ((lane & 4) ? 768 : 384) + 16 * cb + 4 * (lane & 3)))) * 4);
        }
        if (i == 1) {
          const int fb = cb * 24 + 6 * w;
          ro = opq((lane < 16 && (lane & 7) < 6) ? O_RM + ((lane >> 3) * 576 + fb + (lane & 7)) * 128 : OOB);
          s_f = sopq(O_MT + fb * 512);
        }
        SETUP(i);
        if (i == 5) { csw = opq(lds + L_CS + w * 64 + n16 * 4); bn = lds + L_BN + w * 768; bg = opq(bn + g * 32); msw = opq(fl + 3 * w * 1024); }
        if (i >= HL) hx = sum4(hx, XF(i - HL));
        if (i == MP) mv4 = LD128(mo, 0);
        if (i == HP) { HOP1_LOADS(0); mr = LD32(ro); }
        if (i == NX1 - 1) nr = LD32(nro);
        if (i == MC) {
          if (UNLIKELY(!MODOK())) {
            unsigned it = 0;
            do { RETRY(1); __builtin_amdgcn_s_sleep(8); CBAR(); mv4 = LD128(mo, 0); } while (!MODOK() && ++it < SPIN_CAP);
          }
          if (lane >= 24 && lane < 48) *(LAS v2u*)(bn + 512 + (lane - 24) * 8) = v2u{pkhi(mv4[0], mv4[1]), pkhi(mv4[2], mv4[3])};
          else LDS128(bn, (lane < 24 ? lane : (lane & 7) + 24) * 16) = mv4 & 0xffff0000u;
          CBAR();
#pragma unroll
          for (int k = 0; k < 3; ++k) { sa[k] = LDSF4(bg, 128 * k); sb[k] = LDSF4(bg, 128 * k + 16); tf[k] = LDS128(bn, 512 + 64 * k + 16 * g); }
          oc_ = *(LAS float*)(bn + 384 + n16 * 4); og_ = *(LAS float*)(bn + 448 + n16 * 4);
        }
        if (i == MC + 1) oc_ *= 0.05103103630798288f * 1.4426950408889634f;
        if (i > MC && i <= MC + 3) { sc[2 * (i - MC - 1)] = opq(sa[i - MC - 1] * oc_); sc[2 * (i - MC - 1) + 1] = opq(sb[i - MC - 1] * oc_); }
        asm volatile("" : "+v"(hx));
        SB();
      }
      STAMP(6);
      STAGE(0, oc_, { if (NX1 + i < 24) XLOAD(NX1 + i); hx = opq(sum4(hx, XF(NX1 - HL + i))); });
      STAMP(9);
      BAR();
#pragma unroll
      for (int k = 0; k < PD; ++k) a[k] = LDS128(fl, k * 1024);
      SB();
#pragma unroll
      for (int k = 0; k < 12; ++k) {
        if (NX1 + 3 + k < 24) XLOAD(NX1 + 3 + k);
        if (k == 4) HOP1_LOADS(1);
        QOP(k);
        if (2 * k >= NX1 - HL + 3) hx = sum4(hx, XF(2 * k));
        if (2 * k + 1 >= NX1 - HL + 3) hx = sum4(hx, XF(2 * k + 1));
        asm volatile("" : "+v"(hx));
        SB();
      }
      QFINISH();
      STAMP(12);
#pragma unroll
      for (int k = 0; k < 3; ++k) { sc[2 * k] = sa[k] * og_; sc[2 * k + 1] = sb[k] * og_; }
      STAGE(1, og_, {});
    } else {
#pragma unroll
      for (int i = 0; i < BA; ++i) {
        XLOAD(i);
        SETUP(i);
        if (i >= HL) hx = sum4(hx, XF(i - HL));
        asm volatile("" : "+v"(hx));
        SB();
      }
      STAMP(9);
      BAR();
      __builtin_amdgcn_s_setprio(2);
#pragma unroll
      for (int k = 0; k < PD; ++k) a[k] = LDS128(fl, k * 1024);
      SB();
#pragma unroll
      for (int i = BA; i < 24 + HL; ++i) {
        if (i < 24) XLOAD(i);
        if (i == 20) nr = LD32(nro);
        hx = sum4(hx, XF(i - HL));
        if (i == BA) {
#pragma unroll
          for (int k = 0; k < (BA - HL) / 2; ++k) QOP(k);
        }
        if ((i - HL) & 1) QOP((i - HL) >> 1);
        asm volatile("" : "+v"(hx));
        SB();
      }
      QFINISH();
      __builtin_amdgcn_s_setprio(0);
      STAMP(12);
    }
    BAR();
    {
      float zz[4], zs;
#pragma unroll
      for (int k = 12; k < 12 + PD; ++k) a[k] = LDS128(fl, k * 1024);
      SB();
#pragma unroll
      for (int k = 12; k < 24; ++k) {
        if (k + PD < 24) a[k + PD] = LDS128(fl, (k + PD) * 1024);
        if (k >= 14 && k < 18) zz[k - 14] = *(LAS float*)(cn + (k - 14) * 64);
        av[0] = mfma(xf[0][k - 12], a[k], av[0]); av[1] = mfma(xf[1][k - 12], a[k], av[1]);
        if (k == 21) zs = (zz[0] + zz[1]) + (zz[2] + zz[3]);
        SB();
      }
      if (H2E) { HOP2_LOADS(); SB(); }
      const f32x4 w0 = av[0] + zs, w1 = av[1] + zs;
      const v4u o = {cvtpk(w0[0], w0[1]), cvtpk(w0[2], w0[3]), cvtpk(w1[0], w1[1]), cvtpk(w1[2], w1[3])};
      ST128(o, vso);
      ST32(scan63(sum4(0u, o)) + tag, rvo);
    }
    STAMP(10);
    SB();
  }
  STAMP(11);
  if (tid == 448) __hip_atomic_store((gu32*)(ws + O_CL + bid * 128), tag, __ATOMIC_RELAXED, __HIP_MEMORY_SCOPE_AGENT);
  const int vro = opq(lane < 24 ? O_RV + (((b * 4 + kb) * 8 + w) * 24 + lane) * 128 : OOB);
  const int mso = opq((w == 0 && g < 2) ? O_ML + ((qt * 4 + kb) * 32 + lane) * 16 : OOB);
  const int s_vl = sopq(O_VF + ((b * 32 + 8 * kb) * 24 + 3 * w) * 1024);
  const bool own = (w >> 1) == kb;
  const int oso = opq((own ? OOB : 0) + O_OF + ((qt * 24 + 3 * w) * 4 + kb) * 1024 + ((g & 1) * 32 + (g >> 1) * 16 + n16) * 16);
  const int roo = opq(((own ? OOB : 0) | l63) + O_RO + ((qt * 24 + 3 * w) * 4 + kb) * 128);
  lp mxr = opq(lds + L_MX + n16 * 32), mxw = opq(lds + L_MX + n16 * 32 + w * 4);
  lp plw = opq(lds + lane * 16 + w * 2048);
  lp pl = opq(lds + lane * 16);
  const int ooff = opq(((qt * 32 + 8 * (n16 >> 2) + 4 * (lane >> 5) + (n16 & 3)) * 384 + (6 * kb + w) * 16 + 8 * (g & 1)) * 2);
  const v4u xr = __builtin_amdgcn_raw_buffer_load_b128(rx, w < 6 ? ooff : OOB, 0, 0);
  const int mlo = opq(O_ML + (qt * 128 + (lane >> 5) * 16 + n16) * 16);
  const int s_ol = sopq(O_OF + (qt * 24 + 6 * kb + w) * 4096), rlo = opq((lane < 4 && lane != kb) ? O_RO + ((qt * 24 + 6 * kb + w) * 4 + lane) * 128 : OOB);
  v4u vf[8][3];
#define VF(i) vf[(i) & 7][(i) >> 3]
#define VLOAD(i) VF(i) = LD128(l16 + ((i) >> 3) * 1024, s_vl + ((i) & 7) * 24576)
  {
    lp qw = opq(lds + L_QS + (lane >> 5) * 12288 + 3 * w * 512 + (lane & 31) * 16);
    SB();
    if (DLY2) __builtin_amdgcn_s_sleep(DLY2);
    if (!H2E) HOP2_LOADS();
#pragma unroll
    for (int i = 0; i < NV0; ++i) VLOAD(i);
    SB();
#define QBAD() (wsum(sum4(sum4(sum4(-qr, qf[0]), qf[1]), qf[2])) + 3u * tag != 0u)
#pragma unroll
    for (int i = 0; i < 3; ++i) LDS128(qw, i * 512) = qf[i];
    if (UNLIKELY(QBAD())) {
      unsigned it = 0;
      do { RETRY(2); __builtin_amdgcn_s_sleep(3); CBAR(); HOP2_LOADS(); } while (QBAD() && ++it < SPIN_CAP);
#pragma unroll
      for (int i = 0; i < 3; ++i) LDS128(qw, i * 512) = qf[i];
    }
  }
  STAMP(13);
  BAR();
  float m[2]; unsigned vr, hv0 = 0;
  {
    v4u q[24];
#define QREAD(i) q[i] = LDS128(fl, 24576 + (((i) & 1) * 12 + ((i) >> 1)) * 1024)
#pragma unroll
    for (int i = 0; i < PD; ++i) QREAD(i);
    SB();
    f32x4 acc[2][2] = {};
#pragma unroll
    for (int i = 0; i < 24; ++i) {
      if (i + PD < 24) QREAD(i + PD);
      acc[i & 1][0] = mfma(xf[0][i >> 1], q[i], acc[i & 1][0]); acc[i & 1][1] = mfma(xf[1][i >> 1], q[i], acc[i & 1][1]);
      if (i >= VS0 && ((i - VS0) * NVS) / (24 - VS0) != ((i - VS0 + 1) * NVS) / (24 - VS0)) VLOAD(NV0 + ((i - VS0) * NVS) / (24 - VS0));
      if (i >= 24 - NH) hv0 = opq(sum4(hv0, VF(i - (24 - NH))));
      SB();
    }
#pragma unroll
    for (int u = 0; u < 2; ++u) {
      float mx = max3(max3(acc[u][0][0], acc[u][0][1], acc[u][0][2]), acc[u][0][3], acc[u][1][0]);
      mx = max_g(max2(max3(mx, acc[u][1][1], acc[u][1][2]), acc[u][1][3]));
      *(LAS float*)(mxw + u * 512) = mx;
    }
    STAMP(14);
    BAR();
    f32x4 ma[2], mb[2];
#pragma unroll
    for (int u = 0; u < 2; ++u) { ma[u] = LDSF4(mxr, u * 512); mb[u] = LDSF4(mxr, u * 512 + 16); }
    vr = LD32(vro);
    SB();
#pragma unroll
    for (int u = 0; u < 2; ++u) {
      m[u] = max2(max3(max3(ma[u][0], ma[u][1], ma[u][2]), ma[u][3], mb[u][0]), max3(mb[u][1], mb[u][2], mb[u][3]));
      float p[8];
#pragma unroll
      for (int i = 0; i < 8; ++i) p[i] = __builtin_amdgcn_exp2f(acc[u][i >> 2][i & 3] - m[u]);
      LDS128(plw, u * 1024) = v4u{cvtpk(p[0], p[1]), cvtpk(p[2], p[3]), cvtpk(p[4], p[5]), cvtpk(p[6], p[7])};
      if (NV0 + NVS + u < NV2) VLOAD(NV0 + NVS + u);
      SB();
    }
#pragma unroll
    for (int i = NV0 + NVS + 2; i < NV2; ++i) VLOAD(i);
  }
  STAMP(15);
  BAR();
  v4u ml[4];
#define ML_LOADS() do { _Pragma("unroll") for (int k = 0; k < 4; ++k) ml[k] = LD128(mlo, ((kb + k) & 3) * 512); } while (0)
  {
    v4u pf[16];
    f32x4 acc[3][2] = {};
    v4u ov[3];
    unsigned hv[3];
#define OSTORE(et) do { const f32x4 v0 = acc[et][0], v1 = acc[et][1]; \
      auto r0 = __builtin_amdgcn_permlane16_swap(cvtpk(v0[0], v0[1]), cvtpk(v1[0], v1[1]), false, false); \
      auto r1 = __builtin_amdgcn_permlane16_swap(cvtpk(v0[2], v0[3]), cvtpk(v1[2], v1[3]), false, false); \
      ov[et] = v4u{r0[0], r1[0], r0[1], r1[1]}; ST128S(ov[et], oso, (et) * 4096); \
      if (own) LDS128(lds + 16384 + ((w & 1) * 3 + (et)) * 1024, ((g & 1) * 32 + (g >> 1) * 16 + n16) * 16) = ov[et]; } while (0)
#define ORECORD(et) ST32(scan63(sum4(0u, ov[et])) + tag, roo + (et) * 512)
#define VCHECK(et) do { if (UNLIKELY(wsum(hv[et] - ((lane >> 3) == (et) ? vr : 0u)) + 8u * tag != 0u)) { unsigned it = 0; \
        do { RETRY(3); __builtin_amdgcn_s_sleep(2); CBAR(); \
          _Pragma("unroll") for (int i = 0; i < 8; ++i) VLOAD(8 * (et) + i); \
          vr = LD32(vro); hv[et] = 0; \
          _Pragma("unroll") for (int i = 0; i < 8; ++i) hv[et] = sum4(hv[et], VF(8 * (et) + i)); \
        } while (wsum(hv[et] - ((lane >> 3) == (et) ? vr : 0u)) + 8u * tag != 0u && ++it < SPIN_CAP); \
        acc[et][0] = acc[et][1] = f32x4{0.f, 0.f, 0.f, 0.f}; \
        _Pragma("unroll") for (int i = 0; i < 16; ++i) acc[et][i & 1] = mfma(vf[i >> 1][et], pf[i], acc[et][i & 1]); } } while (0)
#pragma unroll
    for (int i = 0; i < 8; ++i) pf[i] = LDS128(pl, i * 1024);
    SB();
    hv[0] = hv0; hv[1] = hv[2] = 0;
#pragma unroll
    for (int i = 0; i < 16; ++i) {
      if (i + 8 < 16) pf[i + 8] = LDS128(pl, (i + 8) * 1024);
      if ((i & 1) && NV2 + (i >> 1) < 24) VLOAD(NV2 + (i >> 1));
      acc[0][i & 1] = mfma(vf[i >> 1][0], pf[i], acc[0][i & 1]);
      if ((i & 1) && (i >> 1) >= NH) hv[0] = opq(sum4(hv[0], vf[i >> 1][0]));
      if (!(i & 1)) hv[1] = opq(sum4(hv[1], vf[i >> 1][1]));
      SB();
    }
    if (w == 0) {
      const v4u ones = {0x3f803f80u, 0x3f803f80u, 0x3f803f80u, 0x3f803f80u};
      f32x4 ls[2] = {};
#pragma unroll
      for (int i = 0; i < 16; ++i) ls[i & 1] = mfma(ones, pf[i], ls[i & 1]);
      ST128((v4u{__float_as_uint(g ? m[1] : m[0]), tag, __float_as_uint(g ? ls[1][0] : ls[0][0]), tag}), mso);
    }
    VCHECK(0);
#pragma unroll
    for (int i = 0; i < 16; ++i) {
      acc[1][i & 1] = mfma(vf[i >> 1][1], pf[i], acc[1][i & 1]);
      if (i == 0) OSTORE(0);
      if (i == 6) ORECORD(0);
      if ((i & 3) == 3) SB();
    }
    VCHECK(1);
#pragma unroll
    for (int i = 0; i < 16; ++i) {
      acc[2][i & 1] = mfma(vf[i >> 1][2], pf[i], acc[2][i & 1]);
      if (i & 1) hv[2] = opq(sum4(hv[2], vf[i >> 1][2]));
      if (i == 0) OSTORE(1);
      if (i == 6) ORECORD(1);
      if (i == 12) ML_LOADS();
      if ((i & 3) == 3) SB();
    }
    VCHECK(2);
    STAMP(16);
    OSTORE(2);
    ORECORD(2);
  }
  STAMP(17);
  BAR();
  if (w < 6) {
    v4u pp[4]; unsigned orr;
#define MLOK() __all(((ml[0][1] ^ tag) | (ml[1][1] ^ tag) | (ml[2][1] ^ tag) | (ml[3][1] ^ tag) | (ml[0][3] ^ tag) | (ml[1][3] ^ tag) | (ml[2][3] ^ tag) | (ml[3][3] ^ tag)) == 0u)
#define HOP3_LOADS() do { _Pragma("unroll") for (int k = 1; k < 4; ++k) pp[k] = LD128(l16, s_ol + ((kb + k) & 3) * 1024); orr = LD32(rlo); } while (0)
#define OBAD() (wsum(sum4(sum4(sum4(-orr, pp[1]), pp[2]), pp[3])) + 3u * tag != 0u)
    pp[0] = LDS128(pl, 16384 + w * 1024);
    SB();
    if (DLY3) __builtin_amdgcn_s_sleep(DLY3);
    HOP3_LOADS();
    SB();
    STAMP(18);
    if (UNLIKELY(!MLOK())) {
      unsigned it = 0;
      do { RETRY(6); __builtin_amdgcn_s_sleep(2); CBAR(); ML_LOADS(); } while (!MLOK() && ++it < SPIN_CAP);
    }
    const float mm = max2(max2(__uint_as_float(ml[0][0]), __uint_as_float(ml[1][0])), max2(__uint_as_float(ml[2][0]), __uint_as_float(ml[3][0])));
    float wk[4], o[8];
#pragma unroll
    for (int k = 0; k < 4; ++k) wk[k] = __builtin_amdgcn_exp2f(__uint_as_float(ml[k][0]) - mm);
    const float inv = __builtin_amdgcn_rcpf((wk[0] * __uint_as_float(ml[0][2]) + wk[1] * __uint_as_float(ml[1][2])) + (wk[2] * __uint_as_float(ml[2][2]) + wk[3] * __uint_as_float(ml[3][2])));
#pragma unroll
    for (int k = 0; k < 4; ++k) wk[k] *= inv;
#pragma unroll
    for (int e = 0; e < 4; ++e) { o[2 * e] = opq(fmaf(wk[0], bflo(pp[0][e]), bflo(xr[e]))); o[2 * e + 1] = opq(fmaf(wk[0], bfhi(pp[0][e]), bfhi(xr[e]))); }
    SB();
    if (UNLIKELY(OBAD())) {
      unsigned it = 0;
      do { RETRY(5); __builtin_amdgcn_s_sleep(3); CBAR(); HOP3_LOADS(); } while (OBAD() && ++it < SPIN_CAP);
    }
    STAMP(1);
    v4u ov;
#pragma unroll
    for (int e = 0; e < 4; ++e) {
#pragma unroll
      for (int k = 1; k < 4; ++k) { o[2 * e] = fmaf(wk[k], bflo(pp[k][e]), o[2 * e]); o[2 * e + 1] = fmaf(wk[k], bfhi(pp[k][e]), o[2 * e + 1]); }
      ov[e] = cvtpk(o[2 * e], o[2 * e + 1]);
    }
    *(v4u*)((unsigned char*)out + ooff) = ov;
  } else if (UNLIKELY(bid == 255 && w == 7)) {
#pragma unroll 1
    for (unsigned it = 0; it < SPIN_CAP; ++it) {
      CBAR();
      unsigned bad = 0;
#pragma unroll
      for (int k = 0; k < 4; ++k) bad |= LD32(O_CL + (k * 64 + lane) * 128) ^ tag;
      if (__all(bad == 0)) break;
    }
    if (lane == 0) __hip_atomic_store((gu32*)(ws + O_EPOCH), tag, __ATOMIC_RELAXED, __HIP_MEMORY_SCOPE_AGENT);
  }
  STAMP(19);
  STFLUSH();
}

extern "C" void kernel_launch(void* const* d_in, const int* in_sizes, int n_in, void* d_out, int out_size, void* d_ws, size_t ws_size,
                              hipStream_t stream) {
  (void)hipFuncSetAttribute((const void*)dit_kernel, hipFuncAttributeMaxDynamicSharedMemorySize, L_TOTAL);
  dit_kernel<<<256, 512, L_TOTAL, stream>>>((const u16*)d_in[0], (const u16*)d_in[1], (const u16*)d_in[2], (const u16*)d_in[3],
                                            (const u16*)d_in[4], (unsigned char*)d_ws + ((size_t)240  __H0__ ), (u16*)d_out);
}
```

```cpp
#include <hip/hip_runtime.h>
#include <hip/hip_bf16.h>

typedef short bf16x8 __attribute__((ext_vector_type(8)));
typedef short s16x4 __attribute__((ext_vector_type(4)));
typedef float f32x4 __attribute__((ext_vector_type(4)));
typedef float f32x2 __attribute__((ext_vector_type(2)));
typedef unsigned v4u __attribute__((ext_vector_type(4)));
typedef unsigned v2u __attribute__((ext_vector_type(2)));
typedef __bf16 bf2 __attribute__((ext_vector_type(2)));
typedef unsigned short u16;
#define DI __device__ __forceinline__
#define LAS __attribute__((address_space(3)))
#define GAS __attribute__((address_space(1)))
typedef GAS unsigned gu32;
typedef LAS unsigned char* lp;
#define STDECL
#define STAMP(i)
#define STFLUSH()
#define RETRY(k)
#define DLY0 30
#define DLY1 8
#define DLY2 3
#define PRE 1
#define HP 4
#define MP 0
#define TOUCH 1
#define CODEPF 16384
#define XS 0
#define DLY3 3
#define PD 4
#define SLP 2
#define ROT 0
#define NX1 16
#define NEARLY 0
#ifndef NV0
#define NV0 0
#endif
#define AUXO 16
#define NH 6
#ifndef NV2
#define NV2 18
#endif
#define HL 8
#define MC 8
#define DLY4 18
#define DLY5 8
#ifndef NVS
#define NVS 13
#endif

constexpr int O_EPOCH = 0;
constexpr int O_MOD = 4096;
constexpr int O_CL = 16384;
constexpr int O_MT = 131072;
constexpr int O_NF = O_MT + 589824;
constexpr int O_QF = O_NF + 1572864;
constexpr int O_VF = O_QF + 1572864;
constexpr int O_OF = O_VF + 1572864;
constexpr int O_ML = O_OF + 6291456;
constexpr int O_RM = O_ML + 131072;
constexpr int O_RN = O_RM + 1152 * 128;
constexpr int O_RQ = O_RN + 512 * 128;
constexpr int O_RV = O_RQ + 1536 * 128;
constexpr int O_RO = O_RV + 1536 * 128;
constexpr int WS_BYTES = O_RO + 6144 * 128;
constexpr int OOB = 0x40000000;

constexpr int L_P0 = 0;
constexpr int L_MS = 61440, L_QS = L_MS + 24576, L_CS = L_QS + 24576, L_MX = L_CS + 512, L_LS = L_MX + 1024, L_BN = L_LS + 1024;
constexpr int L_SCR = L_BN + 6144, L_TAG = L_SCR + 256;
constexpr int L_TOTAL = L_TAG + 32;

static_assert(NV2 >= 16 && NV0 + NVS <= NV2 && NV2 <= 24, "");
static_assert(MC + 3 < NX1 && MP < MC && HP < NX1 && NX1 >= 9 && NX1 <= 21, "");
constexpr int AUX_SC1 = 16;
constexpr unsigned SPIN_CAP = 1u << 16;

DI float bflo(unsigned v) { return __uint_as_float(v << 16); }
DI float bfhi(unsigned v) { return __uint_as_float(v & 0xffff0000u); }
DI unsigned cvtpk(float lo, float hi) {
  f32x2 x = {lo, hi};
  return __builtin_bit_cast(unsigned, __builtin_convertvector(x, bf2));
}
DI float bfround(float x) { return bflo(cvtpk(x, 0.f)); }
DI unsigned pkhi(unsigned lo, unsigned hi) { return __builtin_amdgcn_perm(hi, lo, 0x07060302u); }
DI unsigned tagged(float v, unsigned tag16) { return (cvtpk(0.f, v) & 0xffff0000u) | tag16; }
DI unsigned tchk(unsigned a, v4u v, unsigned tag) { return a | (v[0] ^ tag) | (v[1] ^ tag) | (v[2] ^ tag) | (v[3] ^ tag); }
DI unsigned sum4(unsigned a, v4u v) { return a + v[0] + v[1] + v[2] + v[3]; }
DI unsigned scan63(unsigned v) {
  v += __builtin_amdgcn_update_dpp(0u, v, 0x111, 0xf, 0xf, true);
  v += __builtin_amdgcn_update_dpp(0u, v, 0x112, 0xf, 0xf, true);
  v += __builtin_amdgcn_update_dpp(0u, v, 0x114, 0xf, 0xf, true);
  v += __builtin_amdgcn_update_dpp(0u, v, 0x118, 0xf, 0xf, true);
  v += __builtin_amdgcn_update_dpp(0u, v, 0x142, 0xa, 0xf, true);
  v += __builtin_amdgcn_update_dpp(0u, v, 0x143, 0xc, 0xf, true);
  return v;
}
DI float sum_g(float v);
DI float qsumf(float v) {
#define FSTEP(ctl) v += __uint_as_float(__builtin_amdgcn_update_dpp(0u, __float_as_uint(v), ctl, 0xf, 0xf, true))
  FSTEP(0x128); FSTEP(0x124);
#undef FSTEP
  return sum_g(v);
}
DI unsigned wsum(unsigned v) { return __builtin_amdgcn_readlane(scan63(v), 63); }
DI float max3(float a, float b, float c) { float r; asm("v_max3_f32 %0, %1, %2, %3" : "=v"(r) : "v"(a), "v"(b), "v"(c)); return r; }
DI float max2(float a, float b) { float r; asm("v_max_f32 %0, %1, %2" : "=v"(r) : "v"(a), "v"(b)); return r; }
DI float dot2(unsigned a, unsigned b, float c) { return __builtin_amdgcn_fdot2_f32_bf16(__builtin_bit_cast(bf2, a), __builtin_bit_cast(bf2, b), c, false); }
DI f32x4 mfma(v4u a, v4u b, f32x4 c) {
  return __builtin_amdgcn_mfma_f32_16x16x32_bf16(__builtin_bit_cast(bf16x8, a), __builtin_bit_cast(bf16x8, b), c, 0, 0, 0);
}
DI float sum_g(float v) {
  auto r = __builtin_amdgcn_permlane16_swap(__float_as_uint(v), __float_as_uint(v), false, false);
  v = __uint_as_float(r[0]) + __uint_as_float(r[1]);
  auto q = __builtin_amdgcn_permlane32_swap(__float_as_uint(v), __float_as_uint(v), false, false);
  return __uint_as_float(q[0]) + __uint_as_float(q[1]);
}
DI float max_g(float v) {
  auto r = __builtin_amdgcn_permlane16_swap(__float_as_uint(v), __float_as_uint(v), false, false);
  v = max2(__uint_as_float(r[0]), __uint_as_float(r[1]));
  auto q = __builtin_amdgcn_permlane32_swap(__float_as_uint(v), __float_as_uint(v), false, false);
  return max2(__uint_as_float(q[0]), __uint_as_float(q[1]));
}
DI int sopq(int v) { v = __builtin_amdgcn_readfirstlane(v); asm volatile("" : "+s"(v)); return v; }
template <typename T> DI T opq(T v) { asm volatile("" : "+v"(v)); return v; }
#define BAR() do { asm volatile("s_waitcnt lgkmcnt(0)" ::: "memory"); __builtin_amdgcn_s_barrier(); asm volatile("" ::: "memory"); } while (0)
#define VMWAIT(n) asm volatile("s_waitcnt vmcnt(%0)" :: "i"(n) : "memory")
#define SB() __builtin_amdgcn_sched_barrier(0)
#define CBAR() asm volatile("" ::: "memory")
#define LD128(off, so) __builtin_amdgcn_raw_buffer_load_b128(rws, (off), (so), AUX_SC1)
#define LD64(off) __builtin_amdgcn_raw_buffer_load_b64(rws, (off), 0, AUX_SC1)
#define LD32(off) __builtin_amdgcn_raw_buffer_load_b32(rws, (off), 0, AUX_SC1)
#define ST128(v, off) __builtin_amdgcn_raw_buffer_store_b128((v), rws, (off), 0, AUX_SC1)
#define ST128S(v, off, so) __builtin_amdgcn_raw_buffer_store_b128((v), rws, (off), (so), AUX_SC1)
#define ST64(v, off) __builtin_amdgcn_raw_buffer_store_b64((v), rws, (off), 0, AUX_SC1)
#define ST32(v, off) __builtin_amdgcn_raw_buffer_store_b32((v), rws, (off), 0, AUX_SC1)
#define ST16(v, off) __builtin_amdgcn_raw_buffer_store_b16((v), rws, (off), 0, AUX_SC1)
#define LDS128(p, off) (*(LAS v4u*)((p) + (off)))
#define LDSF4(p, off) (*(LAS f32x4*)((p) + (off)))
DI void dma(unsigned ldsaddr, int voff, __amdgpu_buffer_rsrc_t rs, int soff) {
  asm volatile("s_mov_b32 m0, %0\n\ts_nop 0\n\tbuffer_load_dwordx4 %1, %2, %3 offen lds" :: "s"(ldsaddr), "v"(voff), "s"(rs), "s"(soff) : "memory");
}

DI void touch(unsigned ldsaddr, int voff, __amdgpu_buffer_rsrc_t rs) {
  asm volatile("s_mov_b32 m0, %0\n\ts_nop 0\n\tbuffer_load_dword %1, %2, 0 offen lds" :: "s"(ldsaddr), "v"(voff), "s"(rs) : "memory");
}
#define UNLIKELY(c) __builtin_expect(!!(c), 0)

__global__ __launch_bounds__(512, 2) void dit_kernel(const u16* __restrict__ x, const u16* __restrict__ c, const u16* __restrict__ wqkv,
                                                     const u16* __restrict__ wo, const u16* __restrict__ wmod, unsigned char* ws,
                                                     u16* __restrict__ out) {
  extern __shared__ __attribute__((aligned(16))) unsigned char lds_[];
  lp lds = (lp)lds_;
  const unsigned ldsb = (unsigned)(size_t)lds;
  STDECL;
  STAMP(0);
  const unsigned long long pc0 = __builtin_amdgcn_s_getpc();
  const int tid = threadIdx.x, lane = tid & 63, w = __builtin_amdgcn_readfirstlane(tid >> 6);
  const int n16 = lane & 15, g = lane >> 4;
  const int bid = blockIdx.x, xg = bid & 7, r = bid >> 3;
  const int b = xg >> 2, ql = r & 7, kb = r >> 3;
  const int qt = b * 32 + (xg & 3) * 8 + ql;
  const int qt3 = b * 32 + (((xg & 3) + XS) & 3) * 8 + ql;
  const bool act1 = ql < 6;
  const int cb = (xg & 3) * 6 + (act1 ? ql : ql - 2);
  const __amdgpu_buffer_rsrc_t rws = __builtin_amdgcn_make_buffer_rsrc(ws, 0, WS_BYTES, 0x00020000);
  const __amdgpu_buffer_rsrc_t rx = __builtin_amdgcn_make_buffer_rsrc((void*)x, 0, 2048 * 768, 0x00020000);
  const int dch = 16 * ((lane & 3) ^ ((lane & 32) ? 3 : 0));
  const int rslot = (4 * n16 + (g ^ ((n16 & 8) ? 3 : 0))) * 16;
  const int l63 = lane == 63 ? 0 : OOB;

  const int mu = (((r - 24) >> 1) * 8 + xg) * 2 + (r & 1);
  const int role = r < 12 ? 0 : (r < 24 ? 1 : (mu < 36 ? 2 : 3));
  const int ru = role == 1 ? r - 12 : r;
  const int u12 = 6 * (xg & 1) + ru % 6, u8 = 2 * (xg >> 1) + ru / 6;
  unsigned ep;
  asm volatile("s_nop 4\n\tbuffer_load_dword %0, off, %1, 0 sc1" : "=v"(ep) : "s"(rws) : "memory");
  v4u nv[3];
  const int grp = bid * 2 + (w & 1);
  if (w < 6) {
    unsigned cv = 0;
    if (role == 2) cv = ((const unsigned*)c)[tid];
    if (role < 2) {
      const __amdgpu_buffer_rsrc_t rq = __builtin_amdgcn_make_buffer_rsrc((void*)wqkv, 0, 384 * 2304, 0x00020000);
      const int v = (lane >> 2) * 2304 + dch;
      const int s3 = 48 * u8 * 2304 + (role ? 1536 : 768) + 128 * w;
#pragma unroll
      for (int t = 0; t < 3; ++t) {
        dma(ldsb + L_P0 + (t * 12 + 2 * w) * 1024, v, rq, s3 + 16 * t * 2304);
        dma(ldsb + L_P0 + (t * 12 + 2 * w + 1) * 1024, v, rq, s3 + 16 * t * 2304 + 64);
      }
      if (role == 0) {
        const int s2 = 32 * u12 * 2304 + 128 * w;
#pragma unroll
        for (int t = 0; t < 2; ++t) {
          dma(ldsb + L_P0 + ((3 + t) * 12 + 2 * w) * 1024, v, rq, s2 + 16 * t * 2304);
          dma(ldsb + L_P0 + ((3 + t) * 12 + 2 * w + 1) * 1024, v, rq, s2 + 16 * t * 2304 + 64);
        }
      }
    }
    if (role == 1 || role == 2) {
      const int ld = role == 1 ? 768 : 4608;
      const __amdgpu_buffer_rsrc_t rc = __builtin_amdgcn_make_buffer_rsrc((void*)(role == 1 ? wo : wmod), 0, 384 * ld, 0x00020000);
      const int v = (lane >> 2) * ld + dch;
#pragma unroll
      for (int j = 0; j < 4; ++j) dma(ldsb + L_P0 + (36 + 4 * w + j) * 1024, v, rc, (role == 1 ? u12 : mu) * 64 + (4 * w + j) * 16 * ld);
    }
    if (role == 2) {
      const float f0 = bflo(cv), f1 = bfhi(cv);
      const int bb = tid >= 192, i2 = 2 * (tid - 192 * bb);
      *(LAS unsigned*)(lds + L_P0 + (i2 >> 5) * 1024 + (bb * 4 + ((i2 >> 3) & 3)) * 16 + (i2 & 7) * 2) =
          cvtpk(f0 * __builtin_amdgcn_rcpf(1.f + __expf(-f0)), f1 * __builtin_amdgcn_rcpf(1.f + __expf(-f1)));
    }
    VMWAIT(0);
  } else {
    __builtin_amdgcn_s_setprio(3);
    v4u xs[3];
    asm volatile("buffer_load_dwordx4 %0, %3, %4, 0 offen\n\tbuffer_load_dwordx4 %1, %3, %4, 0 offen offset:256\n\tbuffer_load_dwordx4 %2, %3, %4, 0 offen offset:512"
                 : "=&v"(xs[0]), "=&v"(xs[1]), "=&v"(xs[2]) : "v"((4 * grp + (lane & 3)) * 768 + (lane >> 2) * 16), "s"(rx) : "memory");
    {
      const unsigned scr = ldsb + L_SCR;
      const int dead = ((TOUCH & 1) && lane < 36) ? 0 : OOB;
      const __amdgpu_buffer_rsrc_t rq = __builtin_amdgcn_make_buffer_rsrc((void*)wqkv, 0, 384 * 2304, 0x00020000);
      touch(scr, dead + (w == 6 ? (96 * (xg >> 1) + 3 * r + lane / 12) * 2304 + 768 + (lane % 12) * 128 : (192 * (xg & 1) + 6 * r + lane / 6) * 2304 + (lane % 6) * 128), rq);
      const __amdgpu_buffer_rsrc_t r2 = __builtin_amdgcn_make_buffer_rsrc(w == 6 ? (void*)wo : (void*)(pc0 & ~127ull), 0, w == 6 ? 384 * 768 : CODEPF, 0x00020000);
      touch(scr, w == 6 ? dead + (12 * r + lane / 3) * 768 + 384 * (xg & 1) + (lane % 3) * 128 : (lane < 5 ? (r * 5 + lane) * 128 : OOB), r2);
    }
    if (w == 7) {
      const __amdgpu_buffer_rsrc_t r3 = __builtin_amdgcn_make_buffer_rsrc((void*)wmod, 0, 384 * 4608, 0x00020000);
      touch(ldsb + L_SCR, (lane < (xg < 2 ? 36 : 24) ? 0 : OOB) + (12 * r + lane % 12) * 4608 + ((lane / 12) * 8 + xg) * 128, r3);
    }
    else touch(ldsb + L_SCR, OOB, rx);
    asm volatile("s_waitcnt vmcnt(3)" : "+v"(xs[0]), "+v"(xs[1]), "+v"(xs[2]) :: "memory");
    float f[24], sm = 0.f, q = 0.f;
#pragma unroll
    for (int i = 0; i < 12; ++i) { f[2 * i] = bflo(xs[i >> 2][i & 3]); f[2 * i + 1] = bfhi(xs[i >> 2][i & 3]); sm += f[2 * i] + f[2 * i + 1]; }
    sm = qsumf(sm) * (1.f / 384.f);
#pragma unroll
    for (int i = 0; i < 24; ++i) { f[i] -= sm; q = fmaf(f[i], f[i], q); }
    const float rs = __builtin_amdgcn_rsqf(qsumf(q) * (1.f / 384.f) + 1e-5f);
#pragma unroll
    for (int i = 0; i < 3; ++i)
      nv[i] = opq(v4u{cvtpk(f[8 * i] * rs, f[8 * i + 1] * rs), cvtpk(f[8 * i + 2] * rs, f[8 * i + 3] * rs), cvtpk(f[8 * i + 4] * rs, f[8 * i + 5] * rs), cvtpk(f[8 * i + 6] * rs, f[8 * i + 7] * rs)});
  }
  asm volatile("" : "+v"(ep));
  const unsigned tag = __builtin_amdgcn_readfirstlane(ep) + 1u;
#define NSTORE() do { if (w >= 6) {     \
    const int no = O_NF + (((grp >> 3) * 2 + (grp & 1)) * 12 + g) * 1024 + (((lane >> 2) & 3) * 16 + ((grp >> 1) & 3) * 4 + (lane & 3)) * 16; \
    _Pragma("unroll") for (int i = 0; i < 3; ++i) ST128(nv[i], no + i * 4096); \
    ST64((v2u{scan63(sum4(sum4(sum4(0u, nv[0]), nv[1]), nv[2])) + tag, tag}), l63 + O_RN + grp * 128); \
    __builtin_amdgcn_s_setprio(0); } } while (0)
  if (NEARLY || role >= 2) NSTORE();
  STAMP(2);
  BAR();
  STAMP(3);
  if (!NEARLY && role < 2) NSTORE();
  const int rg = b * 32 + kb * 8 + w;
  const int nro = lane < 8 ? O_RN + (rg * 8 + lane) * 128 : OOB;
  if (w < (role == 2 ? 2 : 6) && role != 3) {
    const int t3 = w >> 1, t2 = w & 1;
    f32x4 acc = {};
    v4u a[12], bq[12];
    if (role == 0) {
      lp ap = opq(lds + L_P0 + (3 + t2) * 12288 + rslot), bp = opq(lds + L_P0 + t3 * 12288 + rslot);
#pragma unroll
      for (int k = 0; k < 12; ++k) {
        a[k] = LDS128(ap, k * 1024); bq[k] = LDS128(bp, k * 1024);
        if (k >= 5) acc = mfma(a[k - 5], bq[k - 5], acc);
        SB();
      }
    } else {
      lp ap = opq(lds + L_P0 + (role == 2 ? ((n16 & 1) * 4 + g) * 16 : t3 * 12288 + rslot));
      lp tb = opq(lds + L_P0 + 36864 + (8 * g + (n16 >> 2)) * 64 + 16 * ((2 * t2 + ((n16 >> 1) & 1)) ^ ((g & 1) ? 3 : 0)) + 8 * (n16 & 1));
#pragma unroll
      for (int k = 0; k < 12; ++k) {
        a[k] = LDS128(ap, k * 1024);
        bq[k] = __builtin_bit_cast(v4u, __builtin_shufflevector(__builtin_amdgcn_ds_read_tr16_b64_v4i16((LAS s16x4*)(tb + k * 2048)),
                                                                __builtin_amdgcn_ds_read_tr16_b64_v4i16((LAS s16x4*)(tb + k * 2048 + 256)), 0, 1, 2, 3, 4, 5, 6, 7));
        if (k >= 5) acc = mfma(a[k - 5], bq[k - 5], acc);
        SB();
      }
    }
#pragma unroll
    for (int k = 7; k < 12; ++k) acc = mfma(a[k], bq[k], acc);
    if (role != 2) {
      const int blk = role * 576 + (role == 0 ? (3 * u8 + t3) * 24 + 2 * u12 + t2 : (2 * u12 + t2) * 24 + 3 * u8 + t3);
      const unsigned o0 = cvtpk(acc[0], acc[1]), o1 = cvtpk(acc[2], acc[3]);
      auto r0 = __builtin_amdgcn_permlane16_swap(o0, o0, false, false);
      auto r1 = __builtin_amdgcn_permlane16_swap(o1, o1, false, false);
      ST128((v4u{r0[0], r1[0], r0[1], r1[1]}), ((g & 1) ? OOB : 0) + O_MT + blk * 512 + ((g >> 1) * 16 + n16) * 16);
      ST32(scan63(o0 + o1) + tag, l63 + O_RM + blk * 128);
    } else if (g < 2) {
      float v = bfround(g ? acc[1] : acc[0]);
      if (mu >= 12 && mu < 24) v += 1.f;
      ST32(tagged(v, tag & 0xffffu), O_MOD + (g * 1152 + 32 * mu + 16 * t2 + n16) * 4);
    }
  }
  STAMP(4);
  if (role == 3) __builtin_amdgcn_s_sleep(DLY0);
  else if (role == 2) { if (w >= 2) __builtin_amdgcn_s_sleep(DLY4); }
  else if (w >= 6) __builtin_amdgcn_s_sleep(DLY1);
  v2u nr = LD64(nro);
  SB();
  const int l16 = opq(lane * 16);
  const int s_n = O_NF + rg * 24576;
  const int rot = ROT ? (ql * 3) >> 1 : 0;
  const int l16b = opq(lane * 16 + 12288);
  v4u xf[2][12];
#define XF(i) xf[(i) & 1][(i) >> 1]
#define XLOAD(i) XF(i) = LD128(((i) & 1) ? l16b : l16, s_n + (((i) >> 1) + rot - (((i) >> 1) + rot >= 12 ? 12 : 0)) * 1024)
  lp fl = opq(lds + L_MS + lane * 16);
  v4u qf[3]; unsigned qr;
  const int qro = opq(lane < 3 ? O_RQ + (qt * 24 + 3 * w + lane) * 128 : OOB);
  const int s_ql = sopq(O_QF + (qt * 24 + 3 * w) * 1024);
#define HOP2_LOADS() do { _Pragma("unroll") for (int i = 0; i < 3; ++i) qf[i] = LD128(l16 + i * 1024, s_ql); qr = LD32(qro); } while (0)
  {
    const int s0 = 3 * (w & 3);
    const int mo = opq(O_MOD + (b * 1152 + (lane < 24 ? 384 + 32 * s0 + 4 * lane : (lane < 48 ? 32 * s0 + 4 * (lane - 24) : (w < 4 ? 384 : 768) + 16 * cb + 4 * (lane & 3)))) * 4);
    const int fb = (w >> 2) * 576 + cb * 24 + 2 * s0;
    const int ro = opq(lane < 6 ? O_RM + (fb + lane) * 128 : OOB);
    const int s_f = sopq(O_MT + fb * 512);
    const int blk1 = rg * 24 + cb, dead1 = act1 ? 0 : OOB;
    const int qso = opq(dead1 + O_QF + blk1 * 1024 + ((g & 1) * 32 + (g >> 1) * 16 + n16) * 16);
    const int vso = opq(dead1 + O_VF + blk1 * 1024 + lane * 16);
    const int rso = opq((dead1 | l63) + O_RQ + blk1 * 128);
    const int rvo = opq((dead1 | l63) + O_RV + (((b * 4 + kb) * 24 + cb) * 8 + w) * 128);
    lp cg = opq(lds + L_CS + g * 16), cn = opq(lds + L_CS + 256 + n16 * 4), csw = opq(lds + L_CS + w * 64 + n16 * 4);
    lp bn = lds + L_BN + w * 768, bg = opq(bn + g * 32), msw[3];
#pragma unroll
    for (int i = 0; i < 3; ++i) { int sl = s0 + i - rot; if (sl < 0) sl += 12; msw[i] = opq(fl + (w >> 2) * 12288 + sl * 1024); }
    SB();
    if (UNLIKELY(!__all(lane >= 8 || nr[1] == tag))) { RETRY(0); __builtin_amdgcn_s_sleep(DLY5); }
    STAMP(5);
    unsigned hx = 0;
    v4u mf[3], mv4; unsigned mr;
    f32x4 sa[3], sb[3]; v4u tf[3]; float oc_;
#define MODOK() __all((tchk(0u, mv4, tag) & 0xffffu) == 0u)
#define HOP1_LOADS() do { _Pragma("unroll") for (int i = 0; i < 3; ++i) mf[i] = LD128(l16 + i * 1024, s_f); mr = LD32(ro); } while (0)
#pragma unroll
    for (int i = 0; i < NX1; ++i) {
      XLOAD(i);
      if (i >= HL) hx = sum4(hx, XF(i - HL));
      if (i == MP) mv4 = LD128(mo, 0);
      if (i == HP) HOP1_LOADS();
      if (i == NX1 - 1) nr = LD64(nro);
      if (i == MC) {
        if (UNLIKELY(!MODOK())) {
          unsigned it = 0;
          do { RETRY(1); __builtin_amdgcn_s_sleep(8); CBAR(); mv4 = LD128(mo, 0); } while (!MODOK() && ++it < SPIN_CAP);
        }
        if (lane >= 24 && lane < 48) *(LAS v2u*)(bn + 512 + (lane - 24) * 8) = v2u{pkhi(mv4[0], mv4[1]), pkhi(mv4[2], mv4[3])};
        else LDS128(bn, (lane < 24 ? lane : (lane & 3) + 24) * 16) = mv4 & 0xffff0000u;
        CBAR();
#pragma unroll
        for (int k = 0; k < 3; ++k) { sa[k] = LDSF4(bg, 128 * k); sb[k] = LDSF4(bg, 128 * k + 16); tf[k] = LDS128(bn, 512 + 64 * k + 16 * g); }
        oc_ = *(LAS float*)(bn + 384 + n16 * 4);
      }
      if (i == MC + 1) { if (w < 4) oc_ *= 0.05103103630798288f * 1.4426950408889634f; sa[0] = opq(sa[0] * oc_); sb[0] = opq(sb[0] * oc_); }
      if (i == MC + 2) { sa[1] = opq(sa[1] * oc_); sb[1] = opq(sb[1] * oc_); }
      if (i == MC + 3) { sa[2] = opq(sa[2] * oc_); sb[2] = opq(sb[2] * oc_); }
      asm volatile("" : "+v"(hx));
      SB();
    }
    STAMP(6);
#define FBAD() (wsum(sum4(sum4(sum4(-mr, mf[0]), mf[1]), mf[2])) + 6u * tag != 0u)
    if (UNLIKELY(FBAD())) {
      unsigned it = 0;
      do { RETRY(9); __builtin_amdgcn_s_sleep(6); CBAR(); HOP1_LOADS(); } while (FBAD() && ++it < SPIN_CAP);
    }
    STAMP(7);
    {
      f32x4 au = {};
#pragma unroll
      for (int i = 0; i < 3; ++i) {
        au = mfma(tf[i], mf[i], au);
        const v4u o = {cvtpk(bflo(mf[i][0]) * sa[i][0], bfhi(mf[i][0]) * sa[i][1]), cvtpk(bflo(mf[i][1]) * sa[i][2], bfhi(mf[i][1]) * sa[i][3]),
                       cvtpk(bflo(mf[i][2]) * sb[i][0], bfhi(mf[i][2]) * sb[i][1]), cvtpk(bflo(mf[i][3]) * sb[i][2], bfhi(mf[i][3]) * sb[i][3])};
        LDS128(msw[i], 0) = o;
        if (NX1 + i < 24) XLOAD(NX1 + i);
        hx = opq(sum4(hx, XF(NX1 - HL + i)));
        SB();
      }
      *(LAS float*)(csw) = au[0] * oc_;
    }
    STAMP(9);
    __builtin_amdgcn_s_setprio(2);
    BAR();
    v4u a[24];
    f32x4 uu[4]; float zz[4], zs;
#define AOP(k) do { if ((k) + PD < 24) a[(k) + PD] = LDS128(fl, ((k) + PD) * 1024); \
      if ((k) < 12) { aq[0] = mfma(a[k], xf[0][(k) % 12], aq[0]); aq[1] = mfma(a[k], xf[1][(k) % 12], aq[1]); } \
      else { av[0] = mfma(xf[0][(k) % 12], a[k], av[0]); av[1] = mfma(xf[1][(k) % 12], a[k], av[1]); } } while (0)
#pragma unroll
    for (int k = 0; k < PD; ++k) a[k] = LDS128(fl, k * 1024);
    SB();
    f32x4 aq[2] = {}, av[2] = {};
#pragma unroll
    for (int k = 0; k < 12; ++k) {
      if (NX1 + 3 + k < 24) XLOAD(NX1 + 3 + k);
      if (k >= 2 && k < 6) uu[k - 2] = LDSF4(cg, (k - 2) * 64);
      AOP(k);
      if (2 * k >= NX1 - HL + 3) hx = sum4(hx, XF(2 * k));
      if (2 * k + 1 >= NX1 - HL + 3) hx = sum4(hx, XF(2 * k + 1));
      if (k == 9) { uu[0] = uu[0] + uu[1]; uu[2] = uu[2] + uu[3]; }
      if (k == 10) uu[0] = uu[0] + uu[2];
      asm volatile("" : "+v"(hx));
      SB();
    }
    if (UNLIKELY(wsum(hx - nr[0]) + 8u * tag != 0u)) {
      unsigned it = 0;
      do { RETRY(4); __builtin_amdgcn_s_sleep(4); CBAR();
        hx = -LD32(nro);
#pragma unroll
        for (int i = 0; i < 24; ++i) XLOAD(i);
#pragma unroll
        for (int i = 0; i < 24; ++i) hx = sum4(hx, XF(i));
      } while (wsum(hx) + 8u * tag != 0u && ++it < SPIN_CAP);
      aq[0] = aq[1] = f32x4{0.f, 0.f, 0.f, 0.f};
#pragma unroll
      for (int s = 0; s < 12; ++s) {
        const v4u a0 = LDS128(fl, s * 1024);
        aq[0] = mfma(a0, xf[0][s], aq[0]); aq[1] = mfma(a0, xf[1][s], aq[1]);
      }
    }
    {
      const f32x4 v0 = aq[0] + uu[0], v1 = aq[1] + uu[0];
      auto r0 = __builtin_amdgcn_permlane16_swap(cvtpk(v0[0], v0[1]), cvtpk(v1[0], v1[1]), false, false);
      auto r1 = __builtin_amdgcn_permlane16_swap(cvtpk(v0[2], v0[3]), cvtpk(v1[2], v1[3]), false, false);
      const v4u ov = {r0[0], r1[0], r0[1], r1[1]};
      ST128(ov, qso);
      ST32(scan63(sum4(0u, ov)) + tag, rso);
      __builtin_amdgcn_s_setprio(0);
      STAMP(12);
      SB();
#pragma unroll
      for (int k = 12; k < 24; ++k) {
        if (k >= 14 && k < 18) zz[k - 14] = *(LAS float*)(cn + (k - 14) * 64);
        AOP(k);
        if (k == 21) zs = (zz[0] + zz[1]) + (zz[2] + zz[3]);
        SB();
      }
      const f32x4 w0 = av[0] + zs, w1 = av[1] + zs;
      const v4u o = {cvtpk(w0[0], w0[1]), cvtpk(w0[2], w0[3]), cvtpk(w1[0], w1[1]), cvtpk(w1[2], w1[3])};
      ST128(o, vso);
      ST32(scan63(sum4(0u, o)) + tag, rvo);
    }
    STAMP(10);
    SB();
  }
  STAMP(11);
  if (tid == 448) __hip_atomic_store((gu32*)(ws + O_CL + bid * 128), tag, __ATOMIC_RELAXED, __HIP_MEMORY_SCOPE_AGENT);
  const int vro = opq(lane < 24 ? O_RV + (((b * 4 + kb) * 8 + w) * 24 + lane) * 128 : OOB);
  const int mso = opq((w == 0 && g < 2) ? O_ML + ((qt * 4 + kb) * 32 + lane) * 16 : OOB);
  const int s_vl = sopq(O_VF + ((b * 32 + 8 * kb) * 24 + 3 * w) * 1024);
  const bool own = (w >> 1) == kb;
  const int oso = opq((own ? OOB : 0) + O_OF + ((qt * 24 + 3 * w) * 4 + kb) * 1024 + ((g & 1) * 32 + (g >> 1) * 16 + n16) * 16);
  const int roo = opq(((own ? OOB : 0) | l63) + O_RO + ((qt * 24 + 3 * w) * 4 + kb) * 128);
  lp mxr = opq(lds + L_MX + n16 * 32), mxw = opq(lds + L_MX + n16 * 32 + w * 4);
  lp plw = opq(lds + lane * 16 + w * 2048);
  lp pl = opq(lds + lane * 16);
  const int ooff = opq(((qt * 32 + 8 * (n16 >> 2) + 4 * (lane >> 5) + (n16 & 3)) * 384 + (6 * kb + w) * 16 + 8 * (g & 1)) * 2);
  const v4u xr = __builtin_amdgcn_raw_buffer_load_b128(rx, w < 6 ? ooff : OOB, 0, 0);
  const int mlo = opq(O_ML + (qt * 128 + (lane >> 5) * 16 + n16) * 16);
  const int s_ol = sopq(O_OF + (qt * 24 + 6 * kb + w) * 4096), rlo = opq((lane < 4 && lane != kb) ? O_RO + ((qt * 24 + 6 * kb + w) * 4 + lane) * 128 : OOB);
  v4u vf[8][3];
#define VF(i) vf[(i) & 7][(i) >> 3]
#define VLOAD(i) VF(i) = LD128(l16 + ((i) >> 3) * 1024, s_vl + ((i) & 7) * 24576)
  {
    lp qw = opq(lds + L_QS + (lane >> 5) * 12288 + 3 * w * 512 + (lane & 31) * 16);
    SB();
    if (DLY2) __builtin_amdgcn_s_sleep(DLY2);
    HOP2_LOADS();
#pragma unroll
    for (int i = 0; i < NV0; ++i) VLOAD(i);
    SB();
#define QBAD() (wsum(sum4(sum4(sum4(-qr, qf[0]), qf[1]), qf[2])) + 3u * tag != 0u)
#pragma unroll
    for (int i = 0; i < 3; ++i) LDS128(qw, i * 512) = qf[i];
    if (UNLIKELY(QBAD())) {
      unsigned it = 0;
      do { RETRY(2); __builtin_amdgcn_s_sleep(3); CBAR(); HOP2_LOADS(); } while (QBAD() && ++it < SPIN_CAP);
#pragma unroll
      for (int i = 0; i < 3; ++i) LDS128(qw, i * 512) = qf[i];
    }
  }
  STAMP(13);
  BAR();
  float m[2]; unsigned vr, hv0 = 0;
  {
    v4u q[24];
#define QREAD(i) q[i] = LDS128(fl, 24576 + (((i) & 1) * 12 + ((i) >> 1)) * 1024)
#pragma unroll
    for (int i = 0; i < PD; ++i) QREAD(i);
    SB();
    f32x4 acc[2][2] = {};
#pragma unroll
    for (int i = 0; i < 24; ++i) {
      if (i + PD < 24) QREAD(i + PD);
      acc[i & 1][0] = mfma(xf[0][i >> 1], q[i], acc[i & 1][0]); acc[i & 1][1] = mfma(xf[1][i >> 1], q[i], acc[i & 1][1]);
      if ((i * NVS) / 24 != ((i + 1) * NVS) / 24) VLOAD(NV0 + (i * NVS) / 24);
      if (i >= 24 - NH) hv0 = opq(sum4(hv0, VF(i - (24 - NH))));
      SB();
    }
#pragma unroll
    for (int u = 0; u < 2; ++u) {
      float mx = max3(max3(acc[u][0][0], acc[u][0][1], acc[u][0][2]), acc[u][0][3], acc[u][1][0]);
      mx = max_g(max2(max3(mx, acc[u][1][1], acc[u][1][2]), acc[u][1][3]));
      *(LAS float*)(mxw + u * 512) = mx;
    }
    STAMP(14);
    BAR();
    f32x4 ma[2], mb[2];
#pragma unroll
    for (int u = 0; u < 2; ++u) { ma[u] = LDSF4(mxr, u * 512); mb[u] = LDSF4(mxr, u * 512 + 16); }
    vr = LD32(vro);
    SB();
#pragma unroll
    for (int u = 0; u < 2; ++u) {
      m[u] = max2(max3(max3(ma[u][0], ma[u][1], ma[u][2]), ma[u][3], mb[u][0]), max3(mb[u][1], mb[u][2], mb[u][3]));
      float p[8];
#pragma unroll
      for (int i = 0; i < 8; ++i) p[i] = __builtin_amdgcn_exp2f(acc[u][i >> 2][i & 3] - m[u]);
      LDS128(plw, u * 1024) = v4u{cvtpk(p[0], p[1]), cvtpk(p[2], p[3]), cvtpk(p[4], p[5]), cvtpk(p[6], p[7])};
      if (NV0 + NVS + u < NV2) VLOAD(NV0 + NVS + u);
      SB();
    }
#pragma unroll
    for (int i = NV0 + NVS + 2; i < NV2; ++i) VLOAD(i);
  }
  STAMP(15);
  BAR();
  v4u ml[4];
#define ML_LOADS() do { _Pragma("unroll") for (int k = 0; k < 4; ++k) ml[k] = LD128(mlo, ((kb + k) & 3) * 512); } while (0)
  {
    v4u pf[16];
    f32x4 acc[3][2] = {};
    v4u ov[3];
    unsigned hv[3];
#define OSTORE(et) do { const f32x4 v0 = acc[et][0], v1 = acc[et][1]; \
      auto r0 = __builtin_amdgcn_permlane16_swap(cvtpk(v0[0], v0[1]), cvtpk(v1[0], v1[1]), false, false); \
      auto r1 = __builtin_amdgcn_permlane16_swap(cvtpk(v0[2], v0[3]), cvtpk(v1[2], v1[3]), false, false); \
      ov[et] = v4u{r0[0], r1[0], r0[1], r1[1]}; ST128S(ov[et], oso, (et) * 4096); \
      if (own) LDS128(lds + 16384 + ((w & 1) * 3 + (et)) * 1024, ((g & 1) * 32 + (g >> 1) * 16 + n16) * 16) = ov[et]; } while (0)
#define ORECORD(et) ST32(scan63(sum4(0u, ov[et])) + tag, roo + (et) * 512)
#define VCHECK(et) do { if (UNLIKELY(wsum(hv[et] - ((lane >> 3) == (et) ? vr : 0u)) + 8u * tag != 0u)) { unsigned it = 0; \
        do { RETRY(3); __builtin_amdgcn_s_sleep(2); CBAR(); \
          _Pragma("unroll") for (int i = 0; i < 8; ++i) VLOAD(8 * (et) + i); \
          vr = LD32(vro); hv[et] = 0; \
          _Pragma("unroll") for (int i = 0; i < 8; ++i) hv[et] = sum4(hv[et], VF(8 * (et) + i)); \
        } while (wsum(hv[et] - ((lane >> 3) == (et) ? vr : 0u)) + 8u * tag != 0u && ++it < SPIN_CAP); \
        acc[et][0] = acc[et][1] = f32x4{0.f, 0.f, 0.f, 0.f}; \
        _Pragma("unroll") for (int i = 0; i < 16; ++i) acc[et][i & 1] = mfma(vf[i >> 1][et], pf[i], acc[et][i & 1]); } } while (0)
#pragma unroll
    for (int i = 0; i < 8; ++i) pf[i] = LDS128(pl, i * 1024);
    SB();
    hv[0] = hv0; hv[1] = hv[2] = 0;
#pragma unroll
    for (int i = 0; i < 16; ++i) {
      if (i + 8 < 16) pf[i + 8] = LDS128(pl, (i + 8) * 1024);
      if ((i & 1) && NV2 + (i >> 1) < 24) VLOAD(NV2 + (i >> 1));
      acc[0][i & 1] = mfma(vf[i >> 1][0], pf[i], acc[0][i & 1]);
      if ((i & 1) && (i >> 1) >= NH) hv[0] = opq(sum4(hv[0], vf[i >> 1][0]));
      if (!(i & 1)) hv[1] = opq(sum4(hv[1], vf[i >> 1][1]));
      SB();
    }
    if (w == 0) {
      const v4u ones = {0x3f803f80u, 0x3f803f80u, 0x3f803f80u, 0x3f803f80u};
      f32x4 ls[2] = {};
#pragma unroll
      for (int i = 0; i < 16; ++i) ls[i & 1] = mfma(ones, pf[i], ls[i & 1]);
      ST128((v4u{__float_as_uint(g ? m[1] : m[0]), tag, __float_as_uint(g ? ls[1][0] : ls[0][0]), tag}), mso);
    }
    VCHECK(0);
#pragma unroll
    for (int i = 0; i < 16; ++i) {
      acc[1][i & 1] = mfma(vf[i >> 1][1], pf[i], acc[1][i & 1]);
      if (i == 0) OSTORE(0);
      if (i == 6) ORECORD(0);
      if ((i & 3) == 3) SB();
    }
    VCHECK(1);
#pragma unroll
    for (int i = 0; i < 16; ++i) {
      acc[2][i & 1] = mfma(vf[i >> 1][2], pf[i], acc[2][i & 1]);
      if (i & 1) hv[2] = opq(sum4(hv[2], vf[i >> 1][2]));
      if (i == 0) OSTORE(1);
      if (i == 6) ORECORD(1);
      if (i == 12) ML_LOADS();
      if ((i & 3) == 3) SB();
    }
    VCHECK(2);
    STAMP(16);
    OSTORE(2);
    ORECORD(2);
  }
  STAMP(17);
  BAR();
  if (w < 6) {
    v4u pp[4]; unsigned orr;
#define MLOK() __all(((ml[0][1] ^ tag) | (ml[1][1] ^ tag) | (ml[2][1] ^ tag) | (ml[3][1] ^ tag) | (ml[0][3] ^ tag) | (ml[1][3] ^ tag) | (ml[2][3] ^ tag) | (ml[3][3] ^ tag)) == 0u)
#define HOP3_LOADS() do { _Pragma("unroll") for (int k = 1; k < 4; ++k) pp[k] = LD128(l16, s_ol + ((kb + k) & 3) * 1024); orr = LD32(rlo); } while (0)
#define OBAD() (wsum(sum4(sum4(sum4(-orr, pp[1]), pp[2]), pp[3])) + 3u * tag != 0u)
    pp[0] = LDS128(pl, 16384 + w * 1024);
    SB();
    if (DLY3) __builtin_amdgcn_s_sleep(DLY3);
    HOP3_LOADS();
    SB();
    STAMP(18);
    if (UNLIKELY(!MLOK())) {
      unsigned it = 0;
      do { RETRY(6); __builtin_amdgcn_s_sleep(2); CBAR(); ML_LOADS(); } while (!MLOK() && ++it < SPIN_CAP);
    }
    const float mm = max2(max2(__uint_as_float(ml[0][0]), __uint_as_float(ml[1][0])), max2(__uint_as_float(ml[2][0]), __uint_as_float(ml[3][0])));
    float wk[4], o[8];
#pragma unroll
    for (int k = 0; k < 4; ++k) wk[k] = __builtin_amdgcn_exp2f(__uint_as_float(ml[k][0]) - mm);
    const float inv = __builtin_amdgcn_rcpf((wk[0] * __uint_as_float(ml[0][2]) + wk[1] * __uint_as_float(ml[1][2])) + (wk[2] * __uint_as_float(ml[2][2]) + wk[3] * __uint_as_float(ml[3][2])));
#pragma unroll
    for (int k = 0; k < 4; ++k) wk[k] *= inv;
#pragma unroll
    for (int e = 0; e < 4; ++e) { o[2 * e] = opq(fmaf(wk[0], bflo(pp[0][e]), bflo(xr[e]))); o[2 * e + 1] = opq(fmaf(wk[0], bfhi(pp[0][e]), bfhi(xr[e]))); }
    SB();
    if (UNLIKELY(OBAD())) {
      unsigned it = 0;
      do { RETRY(5); __builtin_amdgcn_s_sleep(3); CBAR(); HOP3_LOADS(); } while (OBAD() && ++it < SPIN_CAP);
    }
    STAMP(1);
    v4u ov;
#pragma unroll
    for (int e = 0; e < 4; ++e) {
#pragma unroll
      for (int k = 1; k < 4; ++k) { o[2 * e] = fmaf(wk[k], bflo(pp[k][e]), o[2 * e]); o[2 * e + 1] = fmaf(wk[k], bfhi(pp[k][e]), o[2 * e + 1]); }
      ov[e] = cvtpk(o[2 * e], o[2 * e + 1]);
    }
    *(v4u*)((unsigned char*)out + ooff) = ov;
  } else if (UNLIKELY(bid == 255 && w == 7)) {
#pragma unroll 1
    for (unsigned it = 0; it < SPIN_CAP; ++it) {
      CBAR();
      unsigned bad = 0;
#pragma unroll
      for (int k = 0; k < 4; ++k) bad |= LD32(O_CL + (k * 64 + lane) * 128) ^ tag;
      if (__all(bad == 0)) break;
    }
    if (lane == 0) __hip_atomic_store((gu32*)(ws + O_EPOCH), tag, __ATOMIC_RELAXED, __HIP_MEMORY_SCOPE_AGENT);
  }
  STAMP(19);
  STFLUSH();
}

extern "C" void kernel_launch(void* const* d_in, const int* in_sizes, int n_in, void* d_out, int out_size, void* d_ws, size_t ws_size,
                              hipStream_t stream) {
  (void)hipFuncSetAttribute((const void*)dit_kernel, hipFuncAttributeMaxDynamicSharedMemorySize, L_TOTAL);
  dit_kernel<<<256, 512, L_TOTAL, stream>>>((const u16*)d_in[0], (const u16*)d_in[1], (const u16*)d_in[2], (const u16*)d_in[3],
                                            (const u16*)d_in[4], (unsigned char*)d_ws, (u16*)d_out);
}
```

```cpp
#include <hip/hip_runtime.h>
#include <hip/hip_bf16.h>

typedef short bf16x8 __attribute__((ext_vector_type(8)));
typedef short s16x4 __attribute__((ext_vector_type(4)));
typedef float f32x4 __attribute__((ext_vector_type(4)));
typedef float f32x2 __attribute__((ext_vector_type(2)));
typedef unsigned v4u __attribute__((ext_vector_type(4)));
typedef unsigned v2u __attribute__((ext_vector_type(2)));
typedef __bf16 bf2 __attribute__((ext_vector_type(2)));
typedef unsigned short u16;
#define DI __device__ __forceinline__
#define LAS __attribute__((address_space(3)))
#define GAS __attribute__((address_space(1)))
typedef GAS unsigned gu32;
typedef LAS unsigned char* lp;
#define STDECL
#define STAMP(i)
#define STFLUSH()
#define RETRY(k)
#define DLY0 30
#define DLY1 8
#define DLY2 0
#define PRE 1
#define HP 6
#define MP 2
#define TOUCH 1
#define CODEPF 18000
#define XS 0
#define DLY3 3
#define PD 4
#define SLP 2
#define ROT 0
#define NX1 16
#define NEARLY 0
#ifndef NV0
#define NV0 0
#endif
#define AUXO 16
#define NH 6
#ifndef NV2
#define NV2 18
#endif
#define HL 8
#define MC 9
#define DLY4 18
#define DLY5 8
#define BA 4
#ifndef NVS
#define NVS 13
#endif

constexpr int O_EPOCH = 0;
constexpr int O_MOD = 4096;
constexpr int O_CL = 16384;
constexpr int O_MT = 131072;
constexpr int O_NF = O_MT + 589824;
constexpr int O_QF = O_NF + 1572864;
constexpr int O_VF = O_QF + 1572864;
constexpr int O_OF = O_VF + 1572864;
constexpr int O_ML = O_OF + 6291456;
constexpr int O_RM = O_ML + 131072;
constexpr int O_RN = O_RM + 1152 * 128;
constexpr int O_RQ = O_RN + 512 * 128;
constexpr int O_RV = O_RQ + 1536 * 128;
constexpr int O_RO = O_RV + 1536 * 128;
constexpr int WS_BYTES = O_RO + 6144 * 128;
constexpr int OOB = 0x40000000;

constexpr int L_P0 = 0;
constexpr int L_MS = 61440, L_QS = L_MS + 24576, L_CS = L_QS + 24576, L_MX = L_CS + 512, L_LS = L_MX + 1024, L_BN = L_LS + 1024;
constexpr int L_SCR = L_BN + 6144, L_TAG = L_SCR + 256;
constexpr int L_TOTAL = L_TAG + 32;

static_assert(NV2 >= 16 && NV0 + NVS <= NV2 && NV2 <= 24, "");
static_assert(BA < NX1 && MC + 3 < NX1 && MP < MC && HP < NX1 && NX1 >= 9 && NX1 <= 21, "");
constexpr int AUX_SC1 = 16;
constexpr unsigned SPIN_CAP = 1u << 16;

DI float bflo(unsigned v) { return __uint_as_float(v << 16); }
DI float bfhi(unsigned v) { return __uint_as_float(v & 0xffff0000u); }
DI unsigned cvtpk(float lo, float hi) {
  f32x2 x = {lo, hi};
  return __builtin_bit_cast(unsigned, __builtin_convertvector(x, bf2));
}
DI float bfround(float x) { return bflo(cvtpk(x, 0.f)); }
DI unsigned pkhi(unsigned lo, unsigned hi) { return __builtin_amdgcn_perm(hi, lo, 0x07060302u); }
DI unsigned tagged(float v, unsigned tag16) { return (cvtpk(0.f, v) & 0xffff0000u) | tag16; }
DI unsigned tchk(unsigned a, v4u v, unsigned tag) { return a | (v[0] ^ tag) | (v[1] ^ tag) | (v[2] ^ tag) | (v[3] ^ tag); }
DI unsigned sum4(unsigned a, v4u v) { return a + v[0] + v[1] + v[2] + v[3]; }
DI unsigned scan63(unsigned v) {
  v += __builtin_amdgcn_update_dpp(0u, v, 0x111, 0xf, 0xf, true);
  v += __builtin_amdgcn_update_dpp(0u, v, 0x112, 0xf, 0xf, true);
  v += __builtin_amdgcn_update_dpp(0u, v, 0x114, 0xf, 0xf, true);
  v += __builtin_amdgcn_update_dpp(0u, v, 0x118, 0xf, 0xf, true);
  v += __builtin_amdgcn_update_dpp(0u, v, 0x142, 0xa, 0xf, true);
  v += __builtin_amdgcn_update_dpp(0u, v, 0x143, 0xc, 0xf, true);
  return v;
}
DI float sum_g(float v);
DI float qsumf(float v) {
#define FSTEP(ctl) v += __uint_as_float(__builtin_amdgcn_update_dpp(0u, __float_as_uint(v), ctl, 0xf, 0xf, true))
  FSTEP(0x128); FSTEP(0x124);
#undef FSTEP
  return sum_g(v);
}
DI unsigned wsum(unsigned v) { return __builtin_amdgcn_readlane(scan63(v), 63); }
DI float max3(float a, float b, float c) { float r; asm("v_max3_f32 %0, %1, %2, %3" : "=v"(r) : "v"(a), "v"(b), "v"(c)); return r; }
DI float max2(float a, float b) { float r; asm("v_max_f32 %0, %1, %2" : "=v"(r) : "v"(a), "v"(b)); return r; }
DI float dot2(unsigned a, unsigned b, float c) { return __builtin_amdgcn_fdot2_f32_bf16(__builtin_bit_cast(bf2, a), __builtin_bit_cast(bf2, b), c, false); }
DI f32x4 mfma(v4u a, v4u b, f32x4 c) {
  return __builtin_amdgcn_mfma_f32_16x16x32_bf16(__builtin_bit_cast(bf16x8, a), __builtin_bit_cast(bf16x8, b), c, 0, 0, 0);
}
DI float sum_g(float v) {
  auto r = __builtin_amdgcn_permlane16_swap(__float_as_uint(v), __float_as_uint(v), false, false);
  v = __uint_as_float(r[0]) + __uint_as_float(r[1]);
  auto q = __builtin_amdgcn_permlane32_swap(__float_as_uint(v), __float_as_uint(v), false, false);
  return __uint_as_float(q[0]) + __uint_as_float(q[1]);
}
DI float max_g(float v) {
  auto r = __builtin_amdgcn_permlane16_swap(__float_as_uint(v), __float_as_uint(v), false, false);
  v = max2(__uint_as_float(r[0]), __uint_as_float(r[1]));
  auto q = __builtin_amdgcn_permlane32_swap(__float_as_uint(v), __float_as_uint(v), false, false);
  return max2(__uint_as_float(q[0]), __uint_as_float(q[1]));
}
DI int sopq(int v) { v = __builtin_amdgcn_readfirstlane(v); asm volatile("" : "+s"(v)); return v; }
template <typename T> DI T opq(T v) { asm volatile("" : "+v"(v)); return v; }
#define BAR() do { asm volatile("s_waitcnt lgkmcnt(0)" ::: "memory"); __builtin_amdgcn_s_barrier(); asm volatile("" ::: "memory"); } while (0)
#define VMWAIT(n) asm volatile("s_waitcnt vmcnt(%0)" :: "i"(n) : "memory")
#define SB() __builtin_amdgcn_sched_barrier(0)
#define CBAR() asm volatile("" ::: "memory")
#define LD128(off, so) __builtin_amdgcn_raw_buffer_load_b128(rws, (off), (so), AUX_SC1)
#define LD64(off) __builtin_amdgcn_raw_buffer_load_b64(rws, (off), 0, AUX_SC1)
#define LD32(off) __builtin_amdgcn_raw_buffer_load_b32(rws, (off), 0, AUX_SC1)
#define ST128(v, off) __builtin_amdgcn_raw_buffer_store_b128((v), rws, (off), 0, AUX_SC1)
#define ST128S(v, off, so) __builtin_amdgcn_raw_buffer_store_b128((v), rws, (off), (so), AUX_SC1)
#define ST64(v, off) __builtin_amdgcn_raw_buffer_store_b64((v), rws, (off), 0, AUX_SC1)
#define ST32(v, off) __builtin_amdgcn_raw_buffer_store_b32((v), rws, (off), 0, AUX_SC1)
#define ST16(v, off) __builtin_amdgcn_raw_buffer_store_b16((v), rws, (off), 0, AUX_SC1)
#define LDS128(p, off) (*(LAS v4u*)((p) + (off)))
#define LDSF4(p, off) (*(LAS f32x4*)((p) + (off)))
DI void dma(unsigned ldsaddr, int voff, __amdgpu_buffer_rsrc_t rs, int soff) {
  asm volatile("s_mov_b32 m0, %0\n\ts_nop 0\n\tbuffer_load_dwordx4 %1, %2, %3 offen lds" :: "s"(ldsaddr), "v"(voff), "s"(rs), "s"(soff) : "memory");
}

DI void touch(unsigned ldsaddr, int voff, __amdgpu_buffer_rsrc_t rs) {
  asm volatile("s_mov_b32 m0, %0\n\ts_nop 0\n\tbuffer_load_dword %1, %2, 0 offen lds" :: "s"(ldsaddr), "v"(voff), "s"(rs) : "memory");
}
#define UNLIKELY(c) __builtin_expect(!!(c), 0)

__global__ __launch_bounds__(512, 2) void dit_kernel(const u16* __restrict__ x, const u16* __restrict__ c, const u16* __restrict__ wqkv,
                                                     const u16* __restrict__ wo, const u16* __restrict__ wmod, unsigned char* ws,
                                                     u16* __restrict__ out) {
  extern __shared__ __attribute__((aligned(16))) unsigned char lds_[];
  lp lds = (lp)lds_;
  const unsigned ldsb = (unsigned)(size_t)lds;
  STDECL;
  STAMP(0);
  const unsigned long long pc0 = __builtin_amdgcn_s_getpc();
  const int tid = threadIdx.x, lane = tid & 63, w = __builtin_amdgcn_readfirstlane(tid >> 6);
  const int n16 = lane & 15, g = lane >> 4;
  const int bid = blockIdx.x, xg = bid & 7, r = bid >> 3;
  const int b = xg >> 2, ql = r & 7, kb = r >> 3;
  const int qt = b * 32 + (xg & 3) * 8 + ql;
  const int qt3 = b * 32 + (((xg & 3) + XS) & 3) * 8 + ql;
  const bool act1 = ql < 6;
  const int cb = (xg & 3) * 6 + (act1 ? ql : ql - 2);
  const __amdgpu_buffer_rsrc_t rws = __builtin_amdgcn_make_buffer_rsrc(ws, 0, WS_BYTES, 0x00020000);
  const __amdgpu_buffer_rsrc_t rx = __builtin_amdgcn_make_buffer_rsrc((void*)x, 0, 2048 * 768, 0x00020000);
  const int dch = 16 * ((lane & 3) ^ ((lane & 32) ? 3 : 0));
  const int rslot = (4 * n16 + (g ^ ((n16 & 8) ? 3 : 0))) * 16;
  const int l63 = lane == 63 ? 0 : OOB;

  const int mu = (((r - 24) >> 1) * 8 + xg) * 2 + (r & 1);
  const int role = r < 12 ? 0 : (r < 24 ? 1 : (mu < 36 ? 2 : 3));
  const int ru = role == 1 ? r - 12 : r;
  const int u12 = 6 * (xg & 1) + ru % 6, u8 = 2 * (xg >> 1) + ru / 6;
  unsigned ep;
  asm volatile("s_nop 4\n\tbuffer_load_dword %0, off, %1, 0 sc1" : "=v"(ep) : "s"(rws) : "memory");
  v4u nv[3];
  const int grp = bid * 2 + (w & 1);
  if (w < 6) {
    unsigned cv = 0;
    if (role == 2) cv = ((const unsigned*)c)[tid];
    if (role < 2) {
      const __amdgpu_buffer_rsrc_t rq = __builtin_amdgcn_make_buffer_rsrc((void*)wqkv, 0, 384 * 2304, 0x00020000);
      const int v = (lane >> 2) * 2304 + dch;
      const int s3 = 48 * u8 * 2304 + (role ? 1536 : 768) + 128 * w;
#pragma unroll
      for (int t = 0; t < 3; ++t) {
        dma(ldsb + L_P0 + (t * 12 + 2 * w) * 1024, v, rq, s3 + 16 * t * 2304);
        dma(ldsb + L_P0 + (t * 12 + 2 * w + 1) * 1024, v, rq, s3 + 16 * t * 2304 + 64);
      }
      if (role == 0) {
        const int s2 = 32 * u12 * 2304 + 128 * w;
#pragma unroll
        for (int t = 0; t < 2; ++t) {
          dma(ldsb + L_P0 + ((3 + t) * 12 + 2 * w) * 1024, v, rq, s2 + 16 * t * 2304);
          dma(ldsb + L_P0 + ((3 + t) * 12 + 2 * w + 1) * 1024, v, rq, s2 + 16 * t * 2304 + 64);
        }
      }
    }
    if (role == 1 || role == 2) {
      const int ld = role == 1 ? 768 : 4608;
      const __amdgpu_buffer_rsrc_t rc = __builtin_amdgcn_make_buffer_rsrc((void*)(role == 1 ? wo : wmod), 0, 384 * ld, 0x00020000);
      const int v = (lane >> 2) * ld + dch;
#pragma unroll
      for (int j = 0; j < 4; ++j) dma(ldsb + L_P0 + (36 + 4 * w + j) * 1024, v, rc, (role == 1 ? u12 : mu) * 64 + (4 * w + j) * 16 * ld);
    }
    if (role == 2) {
      const float f0 = bflo(cv), f1 = bfhi(cv);
      const int bb = tid >= 192, i2 = 2 * (tid - 192 * bb);
      *(LAS unsigned*)(lds + L_P0 + (i2 >> 5) * 1024 + (bb * 4 + ((i2 >> 3) & 3)) * 16 + (i2 & 7) * 2) =
          cvtpk(f0 * __builtin_amdgcn_rcpf(1.f + __expf(-f0)), f1 * __builtin_amdgcn_rcpf(1.f + __expf(-f1)));
    }
    VMWAIT(0);
  } else {
    __builtin_amdgcn_s_setprio(3);
    v4u xs[3];
    asm volatile("buffer_load_dwordx4 %0, %3, %4, 0 offen\n\tbuffer_load_dwordx4 %1, %3, %4, 0 offen offset:256\n\tbuffer_load_dwordx4 %2, %3, %4, 0 offen offset:512"
                 : "=&v"(xs[0]), "=&v"(xs[1]), "=&v"(xs[2]) : "v"((4 * grp + (lane & 3)) * 768 + (lane >> 2) * 16), "s"(rx) : "memory");
    {
      const unsigned scr = ldsb + L_SCR;
      const int dead = ((TOUCH & 1) && lane < 36) ? 0 : OOB;
      const __amdgpu_buffer_rsrc_t rq = __builtin_amdgcn_make_buffer_rsrc((void*)wqkv, 0, 384 * 2304, 0x00020000);
      touch(scr, dead + (w == 6 ? (96 * (xg >> 1) + 3 * r + lane / 12) * 2304 + 768 + (lane % 12) * 128 : (192 * (xg & 1) + 6 * r + lane / 6) * 2304 + (lane % 6) * 128), rq);
      const __amdgpu_buffer_rsrc_t r2 = __builtin_amdgcn_make_buffer_rsrc(w == 6 ? (void*)wo : (void*)(pc0 & ~127ull), 0, w == 6 ? 384 * 768 : CODEPF, 0x00020000);
      touch(scr, w == 6 ? dead + (12 * r + lane / 3) * 768 + 384 * (xg & 1) + (lane % 3) * 128 : (lane < 5 ? (r * 5 + lane) * 128 : OOB), r2);
    }
    if (w == 7) {
      const __amdgpu_buffer_rsrc_t r3 = __builtin_amdgcn_make_buffer_rsrc((void*)wmod, 0, 384 * 4608, 0x00020000);
      touch(ldsb + L_SCR, (lane < (xg < 2 ? 36 : 24) ? 0 : OOB) + (12 * r + lane % 12) * 4608 + ((lane / 12) * 8 + xg) * 128, r3);
    }
    else touch(ldsb + L_SCR, OOB, rx);
    asm volatile("s_waitcnt vmcnt(3)" : "+v"(xs[0]), "+v"(xs[1]), "+v"(xs[2]) :: "memory");
    float f[24], sm = 0.f, q = 0.f;
#pragma unroll
    for (int i = 0; i < 12; ++i) { f[2 * i] = bflo(xs[i >> 2][i & 3]); f[2 * i + 1] = bfhi(xs[i >> 2][i & 3]); sm += f[2 * i] + f[2 * i + 1]; }
    sm = qsumf(sm) * (1.f / 384.f);
#pragma unroll
    for (int i = 0; i < 24; ++i) { f[i] -= sm; q = fmaf(f[i], f[i], q); }
    const float rs = __builtin_amdgcn_rsqf(qsumf(q) * (1.f / 384.f) + 1e-5f);
#pragma unroll
    for (int i = 0; i < 3; ++i)
      nv[i] = opq(v4u{cvtpk(f[8 * i] * rs, f[8 * i + 1] * rs), cvtpk(f[8 * i + 2] * rs, f[8 * i + 3] * rs), cvtpk(f[8 * i + 4] * rs, f[8 * i + 5] * rs), cvtpk(f[8 * i + 6] * rs, f[8 * i + 7] * rs)});
  }
  asm volatile("" : "+v"(ep));
  const unsigned tag = __builtin_amdgcn_readfirstlane(ep) + 1u;
#define NSTORE() do { if (w >= 6) {     \
    const int no = O_NF + (((grp >> 3) * 2 + (grp & 1)) * 12 + g) * 1024 + (((lane >> 2) & 3) * 16 + ((grp >> 1) & 3) * 4 + (lane & 3)) * 16; \
    _Pragma("unroll") for (int i = 0; i < 3; ++i) ST128(nv[i], no + i * 4096); \
    ST64((v2u{scan63(sum4(sum4(sum4(0u, nv[0]), nv[1]), nv[2])) + tag, tag}), l63 + O_RN + grp * 128); \
    __builtin_amdgcn_s_setprio(0); } } while (0)
  if (NEARLY || role >= 2) NSTORE();
  STAMP(2);
  BAR();
  STAMP(3);
  if (!NEARLY && role < 2) NSTORE();
  const int rg = b * 32 + kb * 8 + w;
  const int nro = lane < 8 ? O_RN + (rg * 8 + lane) * 128 : OOB;
  if (w < (role == 2 ? 2 : 6) && role != 3) {
    const int t3 = w >> 1, t2 = w & 1;
    f32x4 acc = {};
    v4u a[12], bq[12];
    if (role == 0) {
      lp ap = opq(lds + L_P0 + (3 + t2) * 12288 + rslot), bp = opq(lds + L_P0 + t3 * 12288 + rslot);
#pragma unroll
      for (int k = 0; k < 12; ++k) {
        a[k] = LDS128(ap, k * 1024); bq[k] = LDS128(bp, k * 1024);
        if (k >= 5) acc = mfma(a[k - 5], bq[k - 5], acc);
        SB();
      }
    } else {
      lp ap = opq(lds + L_P0 + (role == 2 ? ((n16 & 1) * 4 + g) * 16 : t3 * 12288 + rslot));
      lp tb = opq(lds + L_P0 + 36864 + (8 * g + (n16 >> 2)) * 64 + 16 * ((2 * t2 + ((n16 >> 1) & 1)) ^ ((g & 1) ? 3 : 0)) + 8 * (n16 & 1));
#pragma unroll
      for (int k = 0; k < 12; ++k) {
        a[k] = LDS128(ap, k * 1024);
        bq[k] = __builtin_bit_cast(v4u, __builtin_shufflevector(__builtin_amdgcn_ds_read_tr16_b64_v4i16((LAS s16x4*)(tb + k * 2048)),
                                                                __builtin_amdgcn_ds_read_tr16_b64_v4i16((LAS s16x4*)(tb + k * 2048 + 256)), 0, 1, 2, 3, 4, 5, 6, 7));
        if (k >= 5) acc = mfma(a[k - 5], bq[k - 5], acc);
        SB();
      }
    }
#pragma unroll
    for (int k = 7; k < 12; ++k) acc = mfma(a[k], bq[k], acc);
    if (role != 2) {
      const int blk = role * 576 + (role == 0 ? (3 * u8 + t3) * 24 + 2 * u12 + t2 : (2 * u12 + t2) * 24 + 3 * u8 + t3);
      const unsigned o0 = cvtpk(acc[0], acc[1]), o1 = cvtpk(acc[2], acc[3]);
      auto r0 = __builtin_amdgcn_permlane16_swap(o0, o0, false, false);
      auto r1 = __builtin_amdgcn_permlane16_swap(o1, o1, false, false);
      ST128((v4u{r0[0], r1[0], r0[1], r1[1]}), ((g & 1) ? OOB : 0) + O_MT + blk * 512 + ((g >> 1) * 16 + n16) * 16);
      ST32(scan63(o0 + o1) + tag, l63 + O_RM + blk * 128);
    } else if (g < 2) {
      float v = bfround(g ? acc[1] : acc[0]);
      if (mu >= 12 && mu < 24) v += 1.f;
      ST32(tagged(v, tag & 0xffffu), O_MOD + (g * 1152 + 32 * mu + 16 * t2 + n16) * 4);
    }
  }
  STAMP(4);
  if (role == 3) __builtin_amdgcn_s_sleep(DLY0);
  else if (role == 2) { if (w >= 2) __builtin_amdgcn_s_sleep(DLY4); }
  else if (w >= 6) __builtin_amdgcn_s_sleep(DLY1);
  v2u nr = LD64(nro);
  SB();
  const int l16 = opq(lane * 16);
  const int s_n = O_NF + rg * 24576;
  const int rot = ROT ? (ql * 3) >> 1 : 0;
  const int l16b = opq(lane * 16 + 12288);
  v4u xf[2][12];
#define XF(i) xf[(i) & 1][(i) >> 1]
#define XLOAD(i) XF(i) = LD128(((i) & 1) ? l16b : l16, s_n + (((i) >> 1) + rot - (((i) >> 1) + rot >= 12 ? 12 : 0)) * 1024)
  lp fl = opq(lds + L_MS + lane * 16);
  v4u qf[3]; unsigned qr;
  const int qro = opq(lane < 3 ? O_RQ + (qt * 24 + 3 * w + lane) * 128 : OOB);
  const int s_ql = sopq(O_QF + (qt * 24 + 3 * w) * 1024);
#define HOP2_LOADS() do { _Pragma("unroll") for (int i = 0; i < 3; ++i) qf[i] = LD128(l16 + i * 1024, s_ql); qr = LD32(qro); } while (0)
  {
    const int s0 = 3 * (w & 3);
    const int mo = opq(O_MOD + (b * 1152 + (lane < 24 ? 384 + 32 * s0 + 4 * lane : (lane < 48 ? 32 * s0 + 4 * (lane - 24) : (w < 4 ? 384 : 768) + 16 * cb + 4 * (lane & 3)))) * 4);
    const int fb = (w >> 2) * 576 + cb * 24 + 2 * s0;
    const int ro = opq(lane < 6 ? O_RM + (fb + lane) * 128 : OOB);
    const int s_f = sopq(O_MT + fb * 512);
    const int blk1 = rg * 24 + cb, dead1 = act1 ? 0 : OOB;
    const int qso = opq(dead1 + O_QF + blk1 * 1024 + ((g & 1) * 32 + (g >> 1) * 16 + n16) * 16);
    const int vso = opq(dead1 + O_VF + blk1 * 1024 + lane * 16);
    const int rso = opq((dead1 | l63) + O_RQ + blk1 * 128);
    const int rvo = opq((dead1 | l63) + O_RV + (((b * 4 + kb) * 24 + cb) * 8 + w) * 128);
    lp cg = opq(lds + L_CS + g * 16), cn = opq(lds + L_CS + 256 + n16 * 4), csw = opq(lds + L_CS + w * 64 + n16 * 4);
    lp bn = lds + L_BN + w * 768, bg = opq(bn + g * 32), msw[3];
#pragma unroll
    for (int i = 0; i < 3; ++i) { int sl = s0 + i - rot; if (sl < 0) sl += 12; msw[i] = opq(fl + (w >> 2) * 12288 + sl * 1024); }
    SB();
    if (UNLIKELY(!__all(lane >= 8 || nr[1] == tag))) { RETRY(0); __builtin_amdgcn_s_sleep(DLY5); }
    STAMP(5);
    unsigned hx = 0;
    v4u mf[3], mv4; unsigned mr;
    f32x4 sa[3], sb[3]; v4u tf[3]; float oc_;
#define MODOK() __all((tchk(0u, mv4, tag) & 0xffffu) == 0u)
#define HOP1_LOADS() do { _Pragma("unroll") for (int i = 0; i < 3; ++i) mf[i] = LD128(l16 + i * 1024, s_f); mr = LD32(ro); } while (0)
#pragma unroll
    for (int i = 0; i < NX1; ++i) {
      XLOAD(i);
      if (i >= HL) hx = sum4(hx, XF(i - HL));
      if (i == MP) mv4 = LD128(mo, 0);
      if (i == HP) HOP1_LOADS();
      if (i == NX1 - 1) nr = LD64(nro);
      if (i == BA && w >= 4) __builtin_amdgcn_s_barrier();
      if (i == MC) {
        if (UNLIKELY(!MODOK())) {
          unsigned it = 0;
          do { RETRY(1); __builtin_amdgcn_s_sleep(8); CBAR(); mv4 = LD128(mo, 0); } while (!MODOK() && ++it < SPIN_CAP);
        }
        if (lane >= 24 && lane < 48) *(LAS v2u*)(bn + 512 + (lane - 24) * 8) = v2u{pkhi(mv4[0], mv4[1]), pkhi(mv4[2], mv4[3])};
        else LDS128(bn, (lane < 24 ? lane : (lane & 3) + 24) * 16) = mv4 & 0xffff0000u;
        CBAR();
#pragma unroll
        for (int k = 0; k < 3; ++k) { sa[k] = LDSF4(bg, 128 * k); sb[k] = LDSF4(bg, 128 * k + 16); tf[k] = LDS128(bn, 512 + 64 * k + 16 * g); }
        oc_ = *(LAS float*)(bn + 384 + n16 * 4);
      }
      if (i == MC + 1) { if (w < 4) oc_ *= 0.05103103630798288f * 1.4426950408889634f; sa[0] = opq(sa[0] * oc_); sb[0] = opq(sb[0] * oc_); }
      if (i == MC + 2) { sa[1] = opq(sa[1] * oc_); sb[1] = opq(sb[1] * oc_); }
      if (i == MC + 3) { sa[2] = opq(sa[2] * oc_); sb[2] = opq(sb[2] * oc_); }
      asm volatile("" : "+v"(hx));
      SB();
    }
    STAMP(6);
#define FBAD() (wsum(sum4(sum4(sum4(-mr, mf[0]), mf[1]), mf[2])) + 6u * tag != 0u)
    if (UNLIKELY(FBAD())) {
      unsigned it = 0;
      do { RETRY(9); __builtin_amdgcn_s_sleep(6); CBAR(); HOP1_LOADS(); } while (FBAD() && ++it < SPIN_CAP);
    }
    STAMP(7);
    {
      f32x4 au = {};
#pragma unroll
      for (int i = 0; i < 3; ++i) {
        au = mfma(tf[i], mf[i], au);
        const v4u o = {cvtpk(bflo(mf[i][0]) * sa[i][0], bfhi(mf[i][0]) * sa[i][1]), cvtpk(bflo(mf[i][1]) * sa[i][2], bfhi(mf[i][1]) * sa[i][3]),
                       cvtpk(bflo(mf[i][2]) * sb[i][0], bfhi(mf[i][2]) * sb[i][1]), cvtpk(bflo(mf[i][3]) * sb[i][2], bfhi(mf[i][3]) * sb[i][3])};
        LDS128(msw[i], 0) = o;
        if (NX1 + i < 24) XLOAD(NX1 + i);
        hx = opq(sum4(hx, XF(NX1 - HL + i)));
        SB();
      }
      *(LAS float*)(csw) = au[0] * oc_;
    }
    STAMP(9);
    __builtin_amdgcn_s_setprio(2);
    BAR();
    v4u a[24];
    f32x4 uu[4]; float zz[4], zs;
#define AOP(k) do { if ((k) + PD < ((k) < 12 ? 12 : 24)) a[(k) + PD] = LDS128(fl, ((k) + PD) * 1024); \
      if ((k) < 12) { aq[0] = mfma(a[k], xf[0][(k) % 12], aq[0]); aq[1] = mfma(a[k], xf[1][(k) % 12], aq[1]); } \
      else { av[0] = mfma(xf[0][(k) % 12], a[k], av[0]); av[1] = mfma(xf[1][(k) % 12], a[k], av[1]); } } while (0)
#pragma unroll
    for (int k = 0; k < PD; ++k) a[k] = LDS128(fl, k * 1024);
    SB();
    f32x4 aq[2] = {}, av[2] = {};
#pragma unroll
    for (int k = 0; k < 12; ++k) {
      if (NX1 + 3 + k < 24) XLOAD(NX1 + 3 + k);
      if (k >= 2 && k < 6) uu[k - 2] = LDSF4(cg, (k - 2) * 64);
      AOP(k);
      if (2 * k >= NX1 - HL + 3) hx = sum4(hx, XF(2 * k));
      if (2 * k + 1 >= NX1 - HL + 3) hx = sum4(hx, XF(2 * k + 1));
      if (k == 9) { uu[0] = uu[0] + uu[1]; uu[2] = uu[2] + uu[3]; }
      if (k == 10) uu[0] = uu[0] + uu[2];
      asm volatile("" : "+v"(hx));
      SB();
    }
    if (UNLIKELY(wsum(hx - nr[0]) + 8u * tag != 0u)) {
      unsigned it = 0;
      do { RETRY(4); __builtin_amdgcn_s_sleep(4); CBAR();
        hx = -LD32(nro);
#pragma unroll
        for (int i = 0; i < 24; ++i) XLOAD(i);
#pragma unroll
        for (int i = 0; i < 24; ++i) hx = sum4(hx, XF(i));
      } while (wsum(hx) + 8u * tag != 0u && ++it < SPIN_CAP);
      aq[0] = aq[1] = f32x4{0.f, 0.f, 0.f, 0.f};
#pragma unroll
      for (int s = 0; s < 12; ++s) {
        const v4u a0 = LDS128(fl, s * 1024);
        aq[0] = mfma(a0, xf[0][s], aq[0]); aq[1] = mfma(a0, xf[1][s], aq[1]);
      }
    }
    {
      const f32x4 v0 = aq[0] + uu[0], v1 = aq[1] + uu[0];
      auto r0 = __builtin_amdgcn_permlane16_swap(cvtpk(v0[0], v0[1]), cvtpk(v1[0], v1[1]), false, false);
      auto r1 = __builtin_amdgcn_permlane16_swap(cvtpk(v0[2], v0[3]), cvtpk(v1[2], v1[3]), false, false);
      const v4u ov = {r0[0], r1[0], r0[1], r1[1]};
      ST128(ov, qso);
      ST32(scan63(sum4(0u, ov)) + tag, rso);
      __builtin_amdgcn_s_setprio(0);
      STAMP(12);
      if (w < 4) BAR();
      CBAR();
#pragma unroll
      for (int k = 12; k < 12 + PD; ++k) a[k] = LDS128(fl, k * 1024);
      SB();
#pragma unroll
      for (int k = 12; k < 24; ++k) {
        if (k >= 14 && k < 18) zz[k - 14] = *(LAS float*)(cn + (k - 14) * 64);
        AOP(k);
        if (k == 21) zs = (zz[0] + zz[1]) + (zz[2] + zz[3]);
        SB();
      }
      const f32x4 w0 = av[0] + zs, w1 = av[1] + zs;
      const v4u o = {cvtpk(w0[0], w0[1]), cvtpk(w0[2], w0[3]), cvtpk(w1[0], w1[1]), cvtpk(w1[2], w1[3])};
      ST128(o, vso);
      ST32(scan63(sum4(0u, o)) + tag, rvo);
    }
    STAMP(10);
    SB();
  }
  STAMP(11);
  if (tid == 448) __hip_atomic_store((gu32*)(ws + O_CL + bid * 128), tag, __ATOMIC_RELAXED, __HIP_MEMORY_SCOPE_AGENT);
  const int vro = opq(lane < 24 ? O_RV + (((b * 4 + kb) * 8 + w) * 24 + lane) * 128 : OOB);
  const int mso = opq((w == 0 && g < 2) ? O_ML + ((qt * 4 + kb) * 32 + lane) * 16 : OOB);
  const int s_vl = sopq(O_VF + ((b * 32 + 8 * kb) * 24 + 3 * w) * 1024);
  const bool own = (w >> 1) == kb;
  const int oso = opq((own ? OOB : 0) + O_OF + ((qt * 24 + 3 * w) * 4 + kb) * 1024 + ((g & 1) * 32 + (g >> 1) * 16 + n16) * 16);
  const int roo = opq(((own ? OOB : 0) | l63) + O_RO + ((qt * 24 + 3 * w) * 4 + kb) * 128);
  lp mxr = opq(lds + L_MX + n16 * 32), mxw = opq(lds + L_MX + n16 * 32 + w * 4);
  lp plw = opq(lds + lane * 16 + w * 2048);
  lp pl = opq(lds + lane * 16);
  const int ooff = opq(((qt * 32 + 8 * (n16 >> 2) + 4 * (lane >> 5) + (n16 & 3)) * 384 + (6 * kb + w) * 16 + 8 * (g & 1)) * 2);
  const v4u xr = __builtin_amdgcn_raw_buffer_load_b128(rx, w < 6 ? ooff : OOB, 0, 0);
  const int mlo = opq(O_ML + (qt * 128 + (lane >> 5) * 16 + n16) * 16);
  const int s_ol = sopq(O_OF + (qt * 24 + 6 * kb + w) * 4096), rlo = opq((lane < 4 && lane != kb) ? O_RO + ((qt * 24 + 6 * kb + w) * 4 + lane) * 128 : OOB);
  v4u vf[8][3];
#define VF(i) vf[(i) & 7][(i) >> 3]
#define VLOAD(i) VF(i) = LD128(l16 + ((i) >> 3) * 1024, s_vl + ((i) & 7) * 24576)
  {
    lp qw = opq(lds + L_QS + (lane >> 5) * 12288 + 3 * w * 512 + (lane & 31) * 16);
    SB();
    __builtin_amdgcn_s_barrier();
    if (DLY2) __builtin_amdgcn_s_sleep(DLY2);
    HOP2_LOADS();
#pragma unroll
    for (int i = 0; i < NV0; ++i) VLOAD(i);
    SB();
#define QBAD() (wsum(sum4(sum4(sum4(-qr, qf[0]), qf[1]), qf[2])) + 3u * tag != 0u)
#pragma unroll
    for (int i = 0; i < 3; ++i) LDS128(qw, i * 512) = qf[i];
    if (UNLIKELY(QBAD())) {
      unsigned it = 0;
      do { RETRY(2); __builtin_amdgcn_s_sleep(3); CBAR(); HOP2_LOADS(); } while (QBAD() && ++it < SPIN_CAP);
#pragma unroll
      for (int i = 0; i < 3; ++i) LDS128(qw, i * 512) = qf[i];
    }
  }
  STAMP(13);
  BAR();
  float m[2]; unsigned vr, hv0 = 0;
  {
    v4u q[24];
#define QREAD(i) q[i] = LDS128(fl, 24576 + (((i) & 1) * 12 + ((i) >> 1)) * 1024)
#pragma unroll
    for (int i = 0; i < PD; ++i) QREAD(i);
    SB();
    f32x4 acc[2][2] = {};
#pragma unroll
    for (int i = 0; i < 24; ++i) {
      if (i + PD < 24) QREAD(i + PD);
      acc[i & 1][0] = mfma(xf[0][i >> 1], q[i], acc[i & 1][0]); acc[i & 1][1] = mfma(xf[1][i >> 1], q[i], acc[i & 1][1]);
      if ((i * NVS) / 24 != ((i + 1) * NVS) / 24) VLOAD(NV0 + (i * NVS) / 24);
      if (i >= 24 - NH) hv0 = opq(sum4(hv0, VF(i - (24 - NH))));
      SB();
    }
#pragma unroll
    for (int u = 0; u < 2; ++u) {
      float mx = max3(max3(acc[u][0][0], acc[u][0][1], acc[u][0][2]), acc[u][0][3], acc[u][1][0]);
      mx = max_g(max2(max3(mx, acc[u][1][1], acc[u][1][2]), acc[u][1][3]));
      *(LAS float*)(mxw + u * 512) = mx;
    }
    STAMP(14);
    BAR();
    f32x4 ma[2], mb[2];
#pragma unroll
    for (int u = 0; u < 2; ++u) { ma[u] = LDSF4(mxr, u * 512); mb[u] = LDSF4(mxr, u * 512 + 16); }
    vr = LD32(vro);
    SB();
#pragma unroll
    for (int u = 0; u < 2; ++u) {
      m[u] = max2(max3(max3(ma[u][0], ma[u][1], ma[u][2]), ma[u][3], mb[u][0]), max3(mb[u][1], mb[u][2], mb[u][3]));
      float p[8];
#pragma unroll
      for (int i = 0; i < 8; ++i) p[i] = __builtin_amdgcn_exp2f(acc[u][i >> 2][i & 3] - m[u]);
      LDS128(plw, u * 1024) = v4u{cvtpk(p[0], p[1]), cvtpk(p[2], p[3]), cvtpk(p[4], p[5]), cvtpk(p[6], p[7])};
      if (NV0 + NVS + u < NV2) VLOAD(NV0 + NVS + u);
      SB();
    }
#pragma unroll
    for (int i = NV0 + NVS + 2; i < NV2; ++i) VLOAD(i);
  }
  STAMP(15);
  BAR();
  v4u ml[4];
#define ML_LOADS() do { _Pragma("unroll") for (int k = 0; k < 4; ++k) ml[k] = LD128(mlo, ((kb + k) & 3) * 512); } while (0)
  {
    v4u pf[16];
    f32x4 acc[3][2] = {};
    v4u ov[3];
    unsigned hv[3];
#define OSTORE(et) do { const f32x4 v0 = acc[et][0], v1 = acc[et][1]; \
      auto r0 = __builtin_amdgcn_permlane16_swap(cvtpk(v0[0], v0[1]), cvtpk(v1[0], v1[1]), false, false); \
      auto r1 = __builtin_amdgcn_permlane16_swap(cvtpk(v0[2], v0[3]), cvtpk(v1[2], v1[3]), false, false); \
      ov[et] = v4u{r0[0], r1[0], r0[1], r1[1]}; ST128S(ov[et], oso, (et) * 4096); \
      if (own) LDS128(lds + 16384 + ((w & 1) * 3 + (et)) * 1024, ((g & 1) * 32 + (g >> 1) * 16 + n16) * 16) = ov[et]; } while (0)
#define ORECORD(et) ST32(scan63(sum4(0u, ov[et])) + tag, roo + (et) * 512)
#define VCHECK(et) do { if (UNLIKELY(wsum(hv[et] - ((lane >> 3) == (et) ? vr : 0u)) + 8u * tag != 0u)) { unsigned it = 0; \
        do { RETRY(3); __builtin_amdgcn_s_sleep(2); CBAR(); \
          _Pragma("unroll") for (int i = 0; i < 8; ++i) VLOAD(8 * (et) + i); \
          vr = LD32(vro); hv[et] = 0; \
          _Pragma("unroll") for (int i = 0; i < 8; ++i) hv[et] = sum4(hv[et], VF(8 * (et) + i)); \
        } while (wsum(hv[et] - ((lane >> 3) == (et) ? vr : 0u)) + 8u * tag != 0u && ++it < SPIN_CAP); \
        acc[et][0] = acc[et][1] = f32x4{0.f, 0.f, 0.f, 0.f}; \
        _Pragma("unroll") for (int i = 0; i < 16; ++i) acc[et][i & 1] = mfma(vf[i >> 1][et], pf[i], acc[et][i & 1]); } } while (0)
#pragma unroll
    for (int i = 0; i < 8; ++i) pf[i] = LDS128(pl, i * 1024);
    SB();
    hv[0] = hv0; hv[1] = hv[2] = 0;
#pragma unroll
    for (int i = 0; i < 16; ++i) {
      if (i + 8 < 16) pf[i + 8] = LDS128(pl, (i + 8) * 1024);
      if ((i & 1) && NV2 + (i >> 1) < 24) VLOAD(NV2 + (i >> 1));
      acc[0][i & 1] = mfma(vf[i >> 1][0], pf[i], acc[0][i & 1]);
      if ((i & 1) && (i >> 1) >= NH) hv[0] = opq(sum4(hv[0], vf[i >> 1][0]));
      if (!(i & 1)) hv[1] = opq(sum4(hv[1], vf[i >> 1][1]));
      SB();
    }
    if (w == 0) {
      const v4u ones = {0x3f803f80u, 0x3f803f80u, 0x3f803f80u, 0x3f803f80u};
      f32x4 ls[2] = {};
#pragma unroll
      for (int i = 0; i < 16; ++i) ls[i & 1] = mfma(ones, pf[i], ls[i & 1]);
      ST128((v4u{__float_as_uint(g ? m[1] : m[0]), tag, __float_as_uint(g ? ls[1][0] : ls[0][0]), tag}), mso);
    }
    VCHECK(0);
#pragma unroll
    for (int i = 0; i < 16; ++i) {
      acc[1][i & 1] = mfma(vf[i >> 1][1], pf[i], acc[1][i & 1]);
      if (i == 0) OSTORE(0);
      if (i == 6) ORECORD(0);
      if ((i & 3) == 3) SB();
    }
    VCHECK(1);
#pragma unroll
    for (int i = 0; i < 16; ++i) {
      acc[2][i & 1] = mfma(vf[i >> 1][2], pf[i], acc[2][i & 1]);
      if (i & 1) hv[2] = opq(sum4(hv[2], vf[i >> 1][2]));
      if (i == 0) OSTORE(1);
      if (i == 6) ORECORD(1);
      if (i == 12) ML_LOADS();
      if ((i & 3) == 3) SB();
    }
    VCHECK(2);
    STAMP(16);
    OSTORE(2);
    ORECORD(2);
  }
  STAMP(17);
  BAR();
  if (w < 6) {
    v4u pp[4]; unsigned orr;
#define MLOK() __all(((ml[0][1] ^ tag) | (ml[1][1] ^ tag) | (ml[2][1] ^ tag) | (ml[3][1] ^ tag) | (ml[0][3] ^ tag) | (ml[1][3] ^ tag) | (ml[2][3] ^ tag) | (ml[3][3] ^ tag)) == 0u)
#define HOP3_LOADS() do { _Pragma("unroll") for (int k = 1; k < 4; ++k) pp[k] = LD128(l16, s_ol + ((kb + k) & 3) * 1024); orr = LD32(rlo); } while (0)
#define OBAD() (wsum(sum4(sum4(sum4(-orr, pp[1]), pp[2]), pp[3])) + 3u * tag != 0u)
    pp[0] = LDS128(pl, 16384 + w * 1024);
    SB();
    if (DLY3) __builtin_amdgcn_s_sleep(DLY3);
    HOP3_LOADS();
    SB();
    STAMP(18);
    if (UNLIKELY(!MLOK())) {
      unsigned it = 0;
      do { RETRY(6); __builtin_amdgcn_s_sleep(2); CBAR(); ML_LOADS(); } while (!MLOK() && ++it < SPIN_CAP);
    }
    const float mm = max2(max2(__uint_as_float(ml[0][0]), __uint_as_float(ml[1][0])), max2(__uint_as_float(ml[2][0]), __uint_as_float(ml[3][0])));
    float wk[4], o[8];
#pragma unroll
    for (int k = 0; k < 4; ++k) wk[k] = __builtin_amdgcn_exp2f(__uint_as_float(ml[k][0]) - mm);
    const float inv = __builtin_amdgcn_rcpf((wk[0] * __uint_as_float(ml[0][2]) + wk[1] * __uint_as_float(ml[1][2])) + (wk[2] * __uint_as_float(ml[2][2]) + wk[3] * __uint_as_float(ml[3][2])));
#pragma unroll
    for (int k = 0; k < 4; ++k) wk[k] *= inv;
#pragma unroll
    for (int e = 0; e < 4; ++e) { o[2 * e] = opq(fmaf(wk[0], bflo(pp[0][e]), bflo(xr[e]))); o[2 * e + 1] = opq(fmaf(wk[0], bfhi(pp[0][e]), bfhi(xr[e]))); }
    SB();
    if (UNLIKELY(OBAD())) {
      unsigned it = 0;
      do { RETRY(5); __builtin_amdgcn_s_sleep(3); CBAR(); HOP3_LOADS(); } while (OBAD() && ++it < SPIN_CAP);
    }
    STAMP(1);
    v4u ov;
#pragma unroll
    for (int e = 0; e < 4; ++e) {
#pragma unroll
      for (int k = 1; k < 4; ++k) { o[2 * e] = fmaf(wk[k], bflo(pp[k][e]), o[2 * e]); o[2 * e + 1] = fmaf(wk[k], bfhi(pp[k][e]), o[2 * e + 1]); }
      ov[e] = cvtpk(o[2 * e], o[2 * e + 1]);
    }
    *(v4u*)((unsigned char*)out + ooff) = ov;
  } else if (UNLIKELY(bid == 255 && w == 7)) {
#pragma unroll 1
    for (unsigned it = 0; it < SPIN_CAP; ++it) {
      CBAR();
      unsigned bad = 0;
#pragma unroll
      for (int k = 0; k < 4; ++k) bad |= LD32(O_CL + (k * 64 + lane) * 128) ^ tag;
      if (__all(bad == 0)) break;
    }
    if (lane == 0) __hip_atomic_store((gu32*)(ws + O_EPOCH), tag, __ATOMIC_RELAXED, __HIP_MEMORY_SCOPE_AGENT);
  }
  STAMP(19);
  STFLUSH();
}

extern "C" void kernel_launch(void* const* d_in, const int* in_sizes, int n_in, void* d_out, int out_size, void* d_ws, size_t ws_size,
                              hipStream_t stream) {
  (void)hipFuncSetAttribute((const void*)dit_kernel, hipFuncAttributeMaxDynamicSharedMemorySize, L_TOTAL);
  dit_kernel<<<256, 512, L_TOTAL, stream>>>((const u16*)d_in[0], (const u16*)d_in[1], (const u16*)d_in[2], (const u16*)d_in[3],
                                            (const u16*)d_in[4], (unsigned char*)d_ws, (u16*)d_out);
}
```

```cpp
#include <hip/hip_runtime.h>
#include <hip/hip_bf16.h>

typedef short bf16x8 __attribute__((ext_vector_type(8)));
typedef short s16x4 __attribute__((ext_vector_type(4)));
typedef float f32x4 __attribute__((ext_vector_type(4)));
typedef float f32x2 __attribute__((ext_vector_type(2)));
typedef unsigned v4u __attribute__((ext_vector_type(4)));
typedef unsigned v2u __attribute__((ext_vector_type(2)));
typedef __bf16 bf2 __attribute__((ext_vector_type(2)));
typedef unsigned short u16;
#define DI __device__ __forceinline__
#define LAS __attribute__((address_space(3)))
#define GAS __attribute__((address_space(1)))
typedef GAS unsigned gu32;
typedef LAS unsigned char* lp;
#define STDECL
#define STAMP(i)
#define STFLUSH()
#define RETRY(k)
#define DLY0 30
#define DLY1 8
#define DLY2 0
#define PRE 1
#define HP 6
#define MP 2
#define TOUCH 1
#define CODEPF 18000
#define XS 0
#define DLY3 3
#define PD 4
#define SLP 2
#define ROT 0
#define NX1 16
#define NEARLY 0
#ifndef NV0
#define NV0 0
#endif
#define AUXO 16
#define NH 6
#ifndef NV2
#define NV2 18
#endif
#define HL 8
#define MC 9
#define DLY4 18
#define DLY5 8
#define BA 4
#define TLBT 0
#define DLYT 8
#ifndef NVS
#define NVS 13
#endif

constexpr int O_EPOCH = 0;
constexpr int O_MOD = 4096;
constexpr int O_CL = 16384;
constexpr int O_MT = 131072;
constexpr int O_NF = O_MT + 589824;
constexpr int O_QF = O_NF + 1572864;
constexpr int O_VF = O_QF + 1572864;
constexpr int O_OF = O_VF + 1572864;
constexpr int O_ML = O_OF + 6291456;
constexpr int O_RM = O_ML + 131072;
constexpr int O_RN = O_RM + 1152 * 128;
constexpr int O_RQ = O_RN + 512 * 128;
constexpr int O_RV = O_RQ + 1536 * 128;
constexpr int O_RO = O_RV + 1536 * 128;
constexpr int WS_BYTES = O_RO + 6144 * 128;
constexpr int OOB = 0x40000000;

constexpr int L_P0 = 0;
constexpr int L_MS = 61440, L_QS = L_MS + 24576, L_CS = L_QS + 24576, L_MX = L_CS + 512, L_LS = L_MX + 1024, L_BN = L_LS + 1024;
constexpr int L_SCR = L_BN + 6144, L_TAG = L_SCR + 256;
constexpr int L_TOTAL = L_TAG + 32;

static_assert(NV2 >= 16 && NV0 + NVS <= NV2 && NV2 <= 24, "");
static_assert(BA < NX1 && MC + 3 < NX1 && MP < MC && HP < NX1 && NX1 >= 9 && NX1 <= 21, "");
constexpr int AUX_SC1 = 16;
constexpr unsigned SPIN_CAP = 1u << 16;

DI float bflo(unsigned v) { return __uint_as_float(v << 16); }
DI float bfhi(unsigned v) { return __uint_as_float(v & 0xffff0000u); }
DI unsigned cvtpk(float lo, float hi) {
  f32x2 x = {lo, hi};
  return __builtin_bit_cast(unsigned, __builtin_convertvector(x, bf2));
}
DI float bfround(float x) { return bflo(cvtpk(x, 0.f)); }
DI unsigned pkhi(unsigned lo, unsigned hi) { return __builtin_amdgcn_perm(hi, lo, 0x07060302u); }
DI unsigned tagged(float v, unsigned tag16) { return (cvtpk(0.f, v) & 0xffff0000u) | tag16; }
DI unsigned tchk(unsigned a, v4u v, unsigned tag) { return a | (v[0] ^ tag) | (v[1] ^ tag) | (v[2] ^ tag) | (v[3] ^ tag); }
DI unsigned sum4(unsigned a, v4u v) { return a + v[0] + v[1] + v[2] + v[3]; }
DI unsigned scan63(unsigned v) {
  v += __builtin_amdgcn_update_dpp(0u, v, 0x111, 0xf, 0xf, true);
  v += __builtin_amdgcn_update_dpp(0u, v, 0x112, 0xf, 0xf, true);
  v += __builtin_amdgcn_update_dpp(0u, v, 0x114, 0xf, 0xf, true);
  v += __builtin_amdgcn_update_dpp(0u, v, 0x118, 0xf, 0xf, true);
  v += __builtin_amdgcn_update_dpp(0u, v, 0x142, 0xa, 0xf, true);
  v += __builtin_amdgcn_update_dpp(0u, v, 0x143, 0xc, 0xf, true);
  return v;
}
DI float sum_g(float v);
DI float qsumf(float v) {
#define FSTEP(ctl) v += __uint_as_float(__builtin_amdgcn_update_dpp(0u, __float_as_uint(v), ctl, 0xf, 0xf, true))
  FSTEP(0x128); FSTEP(0x124);
#undef FSTEP
  return sum_g(v);
}
DI unsigned wsum(unsigned v) { return __builtin_amdgcn_readlane(scan63(v), 63); }
DI float max3(float a, float b, float c) { float r; asm("v_max3_f32 %0, %1, %2, %3" : "=v"(r) : "v"(a), "v"(b), "v"(c)); return r; }
DI float max2(float a, float b) { float r; asm("v_max_f32 %0, %1, %2" : "=v"(r) : "v"(a), "v"(b)); return r; }
DI float dot2(unsigned a, unsigned b, float c) { return __builtin_amdgcn_fdot2_f32_bf16(__builtin_bit_cast(bf2, a), __builtin_bit_cast(bf2, b), c, false); }
DI f32x4 mfma(v4u a, v4u b, f32x4 c) {
  return __builtin_amdgcn_mfma_f32_16x16x32_bf16(__builtin_bit_cast(bf16x8, a), __builtin_bit_cast(bf16x8, b), c, 0, 0, 0);
}
DI float sum_g(float v) {
  auto r = __builtin_amdgcn_permlane16_swap(__float_as_uint(v), __float_as_uint(v), false, false);
  v = __uint_as_float(r[0]) + __uint_as_float(r[1]);
  auto q = __builtin_amdgcn_permlane32_swap(__float_as_uint(v), __float_as_uint(v), false, false);
  return __uint_as_float(q[0]) + __uint_as_float(q[1]);
}
DI float max_g(float v) {
  auto r = __builtin_amdgcn_permlane16_swap(__float_as_uint(v), __float_as_uint(v), false, false);
  v = max2(__uint_as_float(r[0]), __uint_as_float(r[1]));
  auto q = __builtin_amdgcn_permlane32_swap(__float_as_uint(v), __float_as_uint(v), false, false);
  return max2(__uint_as_float(q[0]), __uint_as_float(q[1]));
}
DI int sopq(int v) { v = __builtin_amdgcn_readfirstlane(v); asm volatile("" : "+s"(v)); return v; }
template <typename T> DI T opq(T v) { asm volatile("" : "+v"(v)); return v; }
#define BAR() do { asm volatile("s_waitcnt lgkmcnt(0)" ::: "memory"); __builtin_amdgcn_s_barrier(); asm volatile("" ::: "memory"); } while (0)
#define VMWAIT(n) asm volatile("s_waitcnt vmcnt(%0)" :: "i"(n) : "memory")
#define SB() __builtin_amdgcn_sched_barrier(0)
#define CBAR() asm volatile("" ::: "memory")
#define LD128(off, so) __builtin_amdgcn_raw_buffer_load_b128(rws, (off), (so), AUX_SC1)
#define LD64(off) __builtin_amdgcn_raw_buffer_load_b64(rws, (off), 0, AUX_SC1)
#define LD32(off) __builtin_amdgcn_raw_buffer_load_b32(rws, (off), 0, AUX_SC1)
#define ST128(v, off) __builtin_amdgcn_raw_buffer_store_b128((v), rws, (off), 0, AUX_SC1)
#define ST128S(v, off, so) __builtin_amdgcn_raw_buffer_store_b128((v), rws, (off), (so), AUX_SC1)
#define ST64(v, off) __builtin_amdgcn_raw_buffer_store_b64((v), rws, (off), 0, AUX_SC1)
#define ST32(v, off) __builtin_amdgcn_raw_buffer_store_b32((v), rws, (off), 0, AUX_SC1)
#define ST16(v, off) __builtin_amdgcn_raw_buffer_store_b16((v), rws, (off), 0, AUX_SC1)
#define LDS128(p, off) (*(LAS v4u*)((p) + (off)))
#define LDSF4(p, off) (*(LAS f32x4*)((p) + (off)))
DI void dma(unsigned ldsaddr, int voff, __amdgpu_buffer_rsrc_t rs, int soff) {
  asm volatile("s_mov_b32 m0, %0\n\ts_nop 0\n\tbuffer_load_dwordx4 %1, %2, %3 offen lds" :: "s"(ldsaddr), "v"(voff), "s"(rs), "s"(soff) : "memory");
}

DI void touch(unsigned ldsaddr, int voff, __amdgpu_buffer_rsrc_t rs) {
  asm volatile("s_mov_b32 m0, %0\n\ts_nop 0\n\tbuffer_load_dword %1, %2, 0 offen lds" :: "s"(ldsaddr), "v"(voff), "s"(rs) : "memory");
}
#define UNLIKELY(c) __builtin_expect(!!(c), 0)

__global__ __launch_bounds__(512, 2) void dit_kernel(const u16* __restrict__ x, const u16* __restrict__ c, const u16* __restrict__ wqkv,
                                                     const u16* __restrict__ wo, const u16* __restrict__ wmod, unsigned char* ws,
                                                     u16* __restrict__ out) {
  extern __shared__ __attribute__((aligned(16))) unsigned char lds_[];
  lp lds = (lp)lds_;
  const unsigned ldsb = (unsigned)(size_t)lds;
  STDECL;
  STAMP(0);
  const unsigned long long pc0 = __builtin_amdgcn_s_getpc();
  const int tid = threadIdx.x, lane = tid & 63, w = __builtin_amdgcn_readfirstlane(tid >> 6);
  const int n16 = lane & 15, g = lane >> 4;
  const int bid = blockIdx.x, xg = bid & 7, r = bid >> 3;
  const int b = xg >> 2, ql = r & 7, kb = r >> 3;
  const int qt = b * 32 + (xg & 3) * 8 + ql;
  const int qt3 = b * 32 + (((xg & 3) + XS) & 3) * 8 + ql;
  const bool act1 = ql < 6;
  const int cb = (xg & 3) * 6 + (act1 ? ql : ql - 2);
  const __amdgpu_buffer_rsrc_t rws = __builtin_amdgcn_make_buffer_rsrc(ws, 0, WS_BYTES, 0x00020000);
  const __amdgpu_buffer_rsrc_t rx = __builtin_amdgcn_make_buffer_rsrc((void*)x, 0, 2048 * 768, 0x00020000);
  const int dch = 16 * ((lane & 3) ^ ((lane & 32) ? 3 : 0));
  const int rslot = (4 * n16 + (g ^ ((n16 & 8) ? 3 : 0))) * 16;
  const int l63 = lane == 63 ? 0 : OOB;

  const int mu = (((r - 24) >> 1) * 8 + xg) * 2 + (r & 1);
  const int role = r < 12 ? 0 : (r < 24 ? 1 : (mu < 36 ? 2 : 3));
  const int ru = role == 1 ? r - 12 : r;
  const int u12 = 6 * (xg & 1) + ru % 6, u8 = 2 * (xg >> 1) + ru / 6;
  unsigned ep;
  asm volatile("s_nop 4\n\tbuffer_load_dword %0, off, %1, 0 sc1" : "=v"(ep) : "s"(rws) : "memory");
  v4u nv[3];
  const int grp = bid * 2 + (w & 1);
  if (w < 6) {
    {
      const void* tp = w < 2 ? (const void*)wqkv : (w == 2 ? (const void*)wo : (w == 3 ? (const void*)wmod : (w == 4 ? (const void*)(pc0 & ~127ull) : (const void*)ws)));
      const int tn = w < 2 ? 384 * 2304 : (w == 2 ? 384 * 768 : (w == 3 ? 384 * 4608 : (w == 4 ? CODEPF : WS_BYTES)));
      int to;
      if (w == 0) to = (96 * (xg >> 1) + 3 * r + lane / 12) * 2304 + 768 + (lane % 12) * 128;
      else if (w == 1) to = (192 * (xg & 1) + 6 * r + lane / 6) * 2304 + (lane % 6) * 128;
      else if (w == 2) to = (12 * r + lane / 3) * 768 + 384 * (xg & 1) + (lane % 3) * 128;
      else if (w == 3) to = (lane < (xg < 2 ? 36 : 24) ? 0 : OOB) + (12 * r + lane % 12) * 4608 + ((lane / 12) * 8 + xg) * 128;
      else if (w == 4) to = lane < 5 ? (r * 5 + lane) * 128 : OOB;
      else to = TLBT ? lane * 393216 + 64 : OOB;
      touch(ldsb + L_SCR, (lane < 36 ? 0 : OOB) | to, __builtin_amdgcn_make_buffer_rsrc((void*)tp, 0, tn, 0x00020000));
      if (TLBT && w == 4) touch(ldsb + L_SCR, lane * 65536, __builtin_amdgcn_make_buffer_rsrc((void*)out, 0, 2048 * 768, 0x00020000));
      if (DLYT) __builtin_amdgcn_s_sleep(DLYT);
    }
    unsigned cv = 0;
    if (role == 2) cv = ((const unsigned*)c)[tid];
    if (role < 2) {
      const __amdgpu_buffer_rsrc_t rq = __builtin_amdgcn_make_buffer_rsrc((void*)wqkv, 0, 384 * 2304, 0x00020000);
      const int v = (lane >> 2) * 2304 + dch;
      const int s3 = 48 * u8 * 2304 + (role ? 1536 : 768) + 128 * w;
#pragma unroll
      for (int t = 0; t < 3; ++t) {
        dma(ldsb + L_P0 + (t * 12 + 2 * w) * 1024, v, rq, s3 + 16 * t * 2304);
        dma(ldsb + L_P0 + (t * 12 + 2 * w + 1) * 1024, v, rq, s3 + 16 * t * 2304 + 64);
      }
      if (role == 0) {
        const int s2 = 32 * u12 * 2304 + 128 * w;
#pragma unroll
        for (int t = 0; t < 2; ++t) {
          dma(ldsb + L_P0 + ((3 + t) * 12 + 2 * w) * 1024, v, rq, s2 + 16 * t * 2304);
          dma(ldsb + L_P0 + ((3 + t) * 12 + 2 * w + 1) * 1024, v, rq, s2 + 16 * t * 2304 + 64);
        }
      }
    }
    if (role == 1 || role == 2) {
      const int ld = role == 1 ? 768 : 4608;
      const __amdgpu_buffer_rsrc_t rc = __builtin_amdgcn_make_buffer_rsrc((void*)(role == 1 ? wo : wmod), 0, 384 * ld, 0x00020000);
      const int v = (lane >> 2) * ld + dch;
#pragma unroll
      for (int j = 0; j < 4; ++j) dma(ldsb + L_P0 + (36 + 4 * w + j) * 1024, v, rc, (role == 1 ? u12 : mu) * 64 + (4 * w + j) * 16 * ld);
    }
    if (role == 2) {
      const float f0 = bflo(cv), f1 = bfhi(cv);
      const int bb = tid >= 192, i2 = 2 * (tid - 192 * bb);
      *(LAS unsigned*)(lds + L_P0 + (i2 >> 5) * 1024 + (bb * 4 + ((i2 >> 3) & 3)) * 16 + (i2 & 7) * 2) =
          cvtpk(f0 * __builtin_amdgcn_rcpf(1.f + __expf(-f0)), f1 * __builtin_amdgcn_rcpf(1.f + __expf(-f1)));
    }
    VMWAIT(0);
  } else {
    __builtin_amdgcn_s_setprio(3);
    v4u xs[3];
    asm volatile("buffer_load_dwordx4 %0, %3, %4, 0 offen\n\tbuffer_load_dwordx4 %1, %3, %4, 0 offen offset:256\n\tbuffer_load_dwordx4 %2, %3, %4, 0 offen offset:512"
                 : "=&v"(xs[0]), "=&v"(xs[1]), "=&v"(xs[2]) : "v"((4 * grp + (lane & 3)) * 768 + (lane >> 2) * 16), "s"(rx) : "memory");
    asm volatile("s_waitcnt vmcnt(0)" : "+v"(xs[0]), "+v"(xs[1]), "+v"(xs[2]) :: "memory");
    float f[24], sm = 0.f, q = 0.f;
#pragma unroll
    for (int i = 0; i < 12; ++i) { f[2 * i] = bflo(xs[i >> 2][i & 3]); f[2 * i + 1] = bfhi(xs[i >> 2][i & 3]); sm += f[2 * i] + f[2 * i + 1]; }
    sm = qsumf(sm) * (1.f / 384.f);
#pragma unroll
    for (int i = 0; i < 24; ++i) { f[i] -= sm; q = fmaf(f[i], f[i], q); }
    const float rs = __builtin_amdgcn_rsqf(qsumf(q) * (1.f / 384.f) + 1e-5f);
#pragma unroll
    for (int i = 0; i < 3; ++i)
      nv[i] = opq(v4u{cvtpk(f[8 * i] * rs, f[8 * i + 1] * rs), cvtpk(f[8 * i + 2] * rs, f[8 * i + 3] * rs), cvtpk(f[8 * i + 4] * rs, f[8 * i + 5] * rs), cvtpk(f[8 * i + 6] * rs, f[8 * i + 7] * rs)});
  }
  asm volatile("" : "+v"(ep));
  const unsigned tag = __builtin_amdgcn_readfirstlane(ep) + 1u;
#define NSTORE() do { if (w >= 6) {     \
    const int no = O_NF + (((grp >> 3) * 2 + (grp & 1)) * 12 + g) * 1024 + (((lane >> 2) & 3) * 16 + ((grp >> 1) & 3) * 4 + (lane & 3)) * 16; \
    _Pragma("unroll") for (int i = 0; i < 3; ++i) ST128(nv[i], no + i * 4096); \
    ST64((v2u{scan63(sum4(sum4(sum4(0u, nv[0]), nv[1]), nv[2])) + tag, tag}), l63 + O_RN + grp * 128); \
    __builtin_amdgcn_s_setprio(0); } } while (0)
  if (NEARLY || role >= 2) NSTORE();
  STAMP(2);
  BAR();
  STAMP(3);
  if (!NEARLY && role < 2) NSTORE();
  const int rg = b * 32 + kb * 8 + w;
  const int nro = lane < 8 ? O_RN + (rg * 8 + lane) * 128 : OOB;
  if (w < (role == 2 ? 2 : 6) && role != 3) {
    const int t3 = w >> 1, t2 = w & 1;
    f32x4 acc = {};
    v4u a[12], bq[12];
    if (role == 0) {
      lp ap = opq(lds + L_P0 + (3 + t2) * 12288 + rslot), bp = opq(lds + L_P0 + t3 * 12288 + rslot);
#pragma unroll
      for (int k = 0; k < 12; ++k) {
        a[k] = LDS128(ap, k * 1024); bq[k] = LDS128(bp, k * 1024);
        if (k >= 5) acc = mfma(a[k - 5], bq[k - 5], acc);
        SB();
      }
    } else {
      lp ap = opq(lds + L_P0 + (role == 2 ? ((n16 & 1) * 4 + g) * 16 : t3 * 12288 + rslot));
      lp tb = opq(lds + L_P0 + 36864 + (8 * g + (n16 >> 2)) * 64 + 16 * ((2 * t2 + ((n16 >> 1) & 1)) ^ ((g & 1) ? 3 : 0)) + 8 * (n16 & 1));
#pragma unroll
      for (int k = 0; k < 12; ++k) {
        a[k] = LDS128(ap, k * 1024);
        bq[k] = __builtin_bit_cast(v4u, __builtin_shufflevector(__builtin_amdgcn_ds_read_tr16_b64_v4i16((LAS s16x4*)(tb + k * 2048)),
                                                                __builtin_amdgcn_ds_read_tr16_b64_v4i16((LAS s16x4*)(tb + k * 2048 + 256)), 0, 1, 2, 3, 4, 5, 6, 7));
        if (k >= 5) acc = mfma(a[k - 5], bq[k - 5], acc);
        SB();
      }
    }
#pragma unroll
    for (int k = 7; k < 12; ++k) acc = mfma(a[k], bq[k], acc);
    if (role != 2) {
      const int blk = role * 576 + (role == 0 ? (3 * u8 + t3) * 24 + 2 * u12 + t2 : (2 * u12 + t2) * 24 + 3 * u8 + t3);
      const unsigned o0 = cvtpk(acc[0], acc[1]), o1 = cvtpk(acc[2], acc[3]);
      auto r0 = __builtin_amdgcn_permlane16_swap(o0, o0, false, false);
      auto r1 = __builtin_amdgcn_permlane16_swap(o1, o1, false, false);
      ST128((v4u{r0[0], r1[0], r0[1], r1[1]}), ((g & 1) ? OOB : 0) + O_MT + blk * 512 + ((g >> 1) * 16 + n16) * 16);
      ST32(scan63(o0 + o1) + tag, l63 + O_RM + blk * 128);
    } else if (g < 2) {
      float v = bfround(g ? acc[1] : acc[0]);
      if (mu >= 12 && mu < 24) v += 1.f;
      ST32(tagged(v, tag & 0xffffu), O_MOD + (g * 1152 + 32 * mu + 16 * t2 + n16) * 4);
    }
  }
  STAMP(4);
  if (role == 3) __builtin_amdgcn_s_sleep(DLY0);
  else if (role == 2) { if (w >= 2) __builtin_amdgcn_s_sleep(DLY4); }
  else if (w >= 6) __builtin_amdgcn_s_sleep(DLY1);
  v2u nr = LD64(nro);
  SB();
  const int l16 = opq(lane * 16);
  const int s_n = O_NF + rg * 24576;
  const int rot = ROT ? (ql * 3) >> 1 : 0;
  const int l16b = opq(lane * 16 + 12288);
  v4u xf[2][12];
#define XF(i) xf[(i) & 1][(i) >> 1]
#define XLOAD(i) XF(i) = LD128(((i) & 1) ? l16b : l16, s_n + (((i) >> 1) + rot - (((i) >> 1) + rot >= 12 ? 12 : 0)) * 1024)
  lp fl = opq(lds + L_MS + lane * 16);
  v4u qf[3]; unsigned qr;
  const int qro = opq(lane < 3 ? O_RQ + (qt * 24 + 3 * w + lane) * 128 : OOB);
  const int s_ql = sopq(O_QF + (qt * 24 + 3 * w) * 1024);
#define HOP2_LOADS() do { _Pragma("unroll") for (int i = 0; i < 3; ++i) qf[i] = LD128(l16 + i * 1024, s_ql); qr = LD32(qro); } while (0)
  {
    const int s0 = 3 * (w & 3);
    const int mo = opq(O_MOD + (b * 1152 + (lane < 24 ? 384 + 32 * s0 + 4 * lane : (lane < 48 ? 32 * s0 + 4 * (lane - 24) : (w < 4 ? 384 : 768) + 16 * cb + 4 * (lane & 3)))) * 4);
    const int fb = (w >> 2) * 576 + cb * 24 + 2 * s0;
    const int ro = opq(lane < 6 ? O_RM + (fb + lane) * 128 : OOB);
    const int s_f = sopq(O_MT + fb * 512);
    const int blk1 = rg * 24 + cb, dead1 = act1 ? 0 : OOB;
    const int qso = opq(dead1 + O_QF + blk1 * 1024 + ((g & 1) * 32 + (g >> 1) * 16 + n16) * 16);
    const int vso = opq(dead1 + O_VF + blk1 * 1024 + lane * 16);
    const int rso = opq((dead1 | l63) + O_RQ + blk1 * 128);
    const int rvo = opq((dead1 | l63) + O_RV + (((b * 4 + kb) * 24 + cb) * 8 + w) * 128);
    lp cg = opq(lds + L_CS + g * 16), cn = opq(lds + L_CS + 256 + n16 * 4), csw = opq(lds + L_CS + w * 64 + n16 * 4);
    lp bn = lds + L_BN + w * 768, bg = opq(bn + g * 32), msw[3];
#pragma unroll
    for (int i = 0; i < 3; ++i) { int sl = s0 + i - rot; if (sl < 0) sl += 12; msw[i] = opq(fl + (w >> 2) * 12288 + sl * 1024); }
    SB();
    if (UNLIKELY(!__all(lane >= 8 || nr[1] == tag))) { RETRY(0); __builtin_amdgcn_s_sleep(DLY5); }
    STAMP(5);
    unsigned hx = 0;
    v4u mf[3], mv4; unsigned mr;
    f32x4 sa[3], sb[3]; v4u tf[3]; float oc_;
#define MODOK() __all((tchk(0u, mv4, tag) & 0xffffu) == 0u)
#define HOP1_LOADS() do { _Pragma("unroll") for (int i = 0; i < 3; ++i) mf[i] = LD128(l16 + i * 1024, s_f); mr = LD32(ro); } while (0)
#pragma unroll
    for (int i = 0; i < NX1; ++i) {
      XLOAD(i);
      if (i >= HL) hx = sum4(hx, XF(i - HL));
      if (i == MP) mv4 = LD128(mo, 0);
      if (i == HP) HOP1_LOADS();
      if (i == NX1 - 1) nr = LD64(nro);
      if (i == BA && w >= 4) __builtin_amdgcn_s_barrier();
      if (i == MC) {
        if (UNLIKELY(!MODOK())) {
          unsigned it = 0;
          do { RETRY(1); __builtin_amdgcn_s_sleep(8); CBAR(); mv4 = LD128(mo, 0); } while (!MODOK() && ++it < SPIN_CAP);
        }
        if (lane >= 24 && lane < 48) *(LAS v2u*)(bn + 512 + (lane - 24) * 8) = v2u{pkhi(mv4[0], mv4[1]), pkhi(mv4[2], mv4[3])};
        else LDS128(bn, (lane < 24 ? lane : (lane & 3) + 24) * 16) = mv4 & 0xffff0000u;
        CBAR();
#pragma unroll
        for (int k = 0; k < 3; ++k) { sa[k] = LDSF4(bg, 128 * k); sb[k] = LDSF4(bg, 128 * k + 16); tf[k] = LDS128(bn, 512 + 64 * k + 16 * g); }
        oc_ = *(LAS float*)(bn + 384 + n16 * 4);
      }
      if (i == MC + 1) { if (w < 4) oc_ *= 0.05103103630798288f * 1.4426950408889634f; sa[0] = opq(sa[0] * oc_); sb[0] = opq(sb[0] * oc_); }
      if (i == MC + 2) { sa[1] = opq(sa[1] * oc_); sb[1] = opq(sb[1] * oc_); }
      if (i == MC + 3) { sa[2] = opq(sa[2] * oc_); sb[2] = opq(sb[2] * oc_); }
      asm volatile("" : "+v"(hx));
      SB();
    }
    STAMP(6);
#define FBAD() (wsum(sum4(sum4(sum4(-mr, mf[0]), mf[1]), mf[2])) + 6u * tag != 0u)
    if (UNLIKELY(FBAD())) {
      unsigned it = 0;
      do { RETRY(9); __builtin_amdgcn_s_sleep(6); CBAR(); HOP1_LOADS(); } while (FBAD() && ++it < SPIN_CAP);
    }
    STAMP(7);
    {
      f32x4 au = {};
#pragma unroll
      for (int i = 0; i < 3; ++i) {
        au = mfma(tf[i], mf[i], au);
        const v4u o = {cvtpk(bflo(mf[i][0]) * sa[i][0], bfhi(mf[i][0]) * sa[i][1]), cvtpk(bflo(mf[i][1]) * sa[i][2], bfhi(mf[i][1]) * sa[i][3]),
                       cvtpk(bflo(mf[i][2]) * sb[i][0], bfhi(mf[i][2]) * sb[i][1]), cvtpk(bflo(mf[i][3]) * sb[i][2], bfhi(mf[i][3]) * sb[i][3])};
        LDS128(msw[i], 0) = o;
        if (NX1 + i < 24) XLOAD(NX1 + i);
        hx = opq(sum4(hx, XF(NX1 - HL + i)));
        SB();
      }
      *(LAS float*)(csw) = au[0] * oc_;
    }
    STAMP(9);
    __builtin_amdgcn_s_setprio(2);
    BAR();
    v4u a[24];
    f32x4 uu[4]; float zz[4], zs;
#define AOP(k) do { if ((k) + PD < ((k) < 12 ? 12 : 24)) a[(k) + PD] = LDS128(fl, ((k) + PD) * 1024); \
      if ((k) < 12) { aq[0] = mfma(a[k], xf[0][(k) % 12], aq[0]); aq[1] = mfma(a[k], xf[1][(k) % 12], aq[1]); } \
      else { av[0] = mfma(xf[0][(k) % 12], a[k], av[0]); av[1] = mfma(xf[1][(k) % 12], a[k], av[1]); } } while (0)
#pragma unroll
    for (int k = 0; k < PD; ++k) a[k] = LDS128(fl, k * 1024);
    SB();
    f32x4 aq[2] = {}, av[2] = {};
#pragma unroll
    for (int k = 0; k < 12; ++k) {
      if (NX1 + 3 + k < 24) XLOAD(NX1 + 3 + k);
      if (k >= 2 && k < 6) uu[k - 2] = LDSF4(cg, (k - 2) * 64);
      AOP(k);
      if (2 * k >= NX1 - HL + 3) hx = sum4(hx, XF(2 * k));
      if (2 * k + 1 >= NX1 - HL + 3) hx = sum4(hx, XF(2 * k + 1));
      if (k == 9) { uu[0] = uu[0] + uu[1]; uu[2] = uu[2] + uu[3]; }
      if (k == 10) uu[0] = uu[0] + uu[2];
      asm volatile("" : "+v"(hx));
      SB();
    }
    if (UNLIKELY(wsum(hx - nr[0]) + 8u * tag != 0u)) {
      unsigned it = 0;
      do { RETRY(4); __builtin_amdgcn_s_sleep(4); CBAR();
        hx = -LD32(nro);
#pragma unroll
        for (int i = 0; i < 24; ++i) XLOAD(i);
#pragma unroll
        for (int i = 0; i < 24; ++i) hx = sum4(hx, XF(i));
      } while (wsum(hx) + 8u * tag != 0u && ++it < SPIN_CAP);
      aq[0] = aq[1] = f32x4{0.f, 0.f, 0.f, 0.f};
#pragma unroll
      for (int s = 0; s < 12; ++s) {
        const v4u a0 = LDS128(fl, s * 1024);
        aq[0] = mfma(a0, xf[0][s], aq[0]); aq[1] = mfma(a0, xf[1][s], aq[1]);
      }
    }
    {
      const f32x4 v0 = aq[0] + uu[0], v1 = aq[1] + uu[0];
      auto r0 = __builtin_amdgcn_permlane16_swap(cvtpk(v0[0], v0[1]), cvtpk(v1[0], v1[1]), false, false);
      auto r1 = __builtin_amdgcn_permlane16_swap(cvtpk(v0[2], v0[3]), cvtpk(v1[2], v1[3]), false, false);
      const v4u ov = {r0[0], r1[0], r0[1], r1[1]};
      ST128(ov, qso);
      ST32(scan63(sum4(0u, ov)) + tag, rso);
      __builtin_amdgcn_s_setprio(0);
      STAMP(12);
      if (w < 4) BAR();
      CBAR();
#pragma unroll
      for (int k = 12; k < 12 + PD; ++k) a[k] = LDS128(fl, k * 1024);
      SB();
#pragma unroll
      for (int k = 12; k < 24; ++k) {
        if (k >= 14 && k < 18) zz[k - 14] = *(LAS float*)(cn + (k - 14) * 64);
        AOP(k);
        if (k == 21) zs = (zz[0] + zz[1]) + (zz[2] + zz[3]);
        SB();
      }
      const f32x4 w0 = av[0] + zs, w1 = av[1] + zs;
      const v4u o = {cvtpk(w0[0], w0[1]), cvtpk(w0[2], w0[3]), cvtpk(w1[0], w1[1]), cvtpk(w1[2], w1[3])};
      ST128(o, vso);
      ST32(scan63(sum4(0u, o)) + tag, rvo);
    }
    STAMP(10);
    SB();
  }
  STAMP(11);
  if (tid == 448) __hip_atomic_store((gu32*)(ws + O_CL + bid * 128), tag, __ATOMIC_RELAXED, __HIP_MEMORY_SCOPE_AGENT);
  const int vro = opq(lane < 24 ? O_RV + (((b * 4 + kb) * 8 + w) * 24 + lane) * 128 : OOB);
  const int mso = opq((w == 0 && g < 2) ? O_ML + ((qt * 4 + kb) * 32 + lane) * 16 : OOB);
  const int s_vl = sopq(O_VF + ((b * 32 + 8 * kb) * 24 + 3 * w) * 1024);
  const bool own = (w >> 1) == kb;
  const int oso = opq((own ? OOB : 0) + O_OF + ((qt * 24 + 3 * w) * 4 + kb) * 1024 + ((g & 1) * 32 + (g >> 1) * 16 + n16) * 16);
  const int roo = opq(((own ? OOB : 0) | l63) + O_RO + ((qt * 24 + 3 * w) * 4 + kb) * 128);
  lp mxr = opq(lds + L_MX + n16 * 32), mxw = opq(lds + L_MX + n16 * 32 + w * 4);
  lp plw = opq(lds + lane * 16 + w * 2048);
  lp pl = opq(lds + lane * 16);
  const int ooff = opq(((qt * 32 + 8 * (n16 >> 2) + 4 * (lane >> 5) + (n16 & 3)) * 384 + (6 * kb + w) * 16 + 8 * (g & 1)) * 2);
  const v4u xr = __builtin_amdgcn_raw_buffer_load_b128(rx, w < 6 ? ooff : OOB, 0, 0);
  const int mlo = opq(O_ML + (qt * 128 + (lane >> 5) * 16 + n16) * 16);
  const int s_ol = sopq(O_OF + (qt * 24 + 6 * kb + w) * 4096), rlo = opq((lane < 4 && lane != kb) ? O_RO + ((qt * 24 + 6 * kb + w) * 4 + lane) * 128 : OOB);
  v4u vf[8][3];
#define VF(i) vf[(i) & 7][(i) >> 3]
#define VLOAD(i) VF(i) = LD128(l16 + ((i) >> 3) * 1024, s_vl + ((i) & 7) * 24576)
  {
    lp qw = opq(lds + L_QS + (lane >> 5) * 12288 + 3 * w * 512 + (lane & 31) * 16);
    SB();
    __builtin_amdgcn_s_barrier();
    if (DLY2) __builtin_amdgcn_s_sleep(DLY2);
    HOP2_LOADS();
#pragma unroll
    for (int i = 0; i < NV0; ++i) VLOAD(i);
    SB();
#define QBAD() (wsum(sum4(sum4(sum4(-qr, qf[0]), qf[1]), qf[2])) + 3u * tag != 0u)
#pragma unroll
    for (int i = 0; i < 3; ++i) LDS128(qw, i * 512) = qf[i];
    if (UNLIKELY(QBAD())) {
      unsigned it = 0;
      do { RETRY(2); __builtin_amdgcn_s_sleep(3); CBAR(); HOP2_LOADS(); } while (QBAD() && ++it < SPIN_CAP);
#pragma unroll
      for (int i = 0; i < 3; ++i) LDS128(qw, i * 512) = qf[i];
    }
  }
  STAMP(13);
  BAR();
  float m[2]; unsigned vr, hv0 = 0;
  {
    v4u q[24];
#define QREAD(i) q[i] = LDS128(fl, 24576 + (((i) & 1) * 12 + ((i) >> 1)) * 1024)
#pragma unroll
    for (int i = 0; i < PD; ++i) QREAD(i);
    SB();
    f32x4 acc[2][2] = {};
#pragma unroll
    for (int i = 0; i < 24; ++i) {
      if (i + PD < 24) QREAD(i + PD);
      acc[i & 1][0] = mfma(xf[0][i >> 1], q[i], acc[i & 1][0]); acc[i & 1][1] = mfma(xf[1][i >> 1], q[i], acc[i & 1][1]);
      if ((i * NVS) / 24 != ((i + 1) * NVS) / 24) VLOAD(NV0 + (i * NVS) / 24);
      if (i >= 24 - NH) hv0 = opq(sum4(hv0, VF(i - (24 - NH))));
      SB();
    }
#pragma unroll
    for (int u = 0; u < 2; ++u) {
      float mx = max3(max3(acc[u][0][0], acc[u][0][1], acc[u][0][2]), acc[u][0][3], acc[u][1][0]);
      mx = max_g(max2(max3(mx, acc[u][1][1], acc[u][1][2]), acc[u][1][3]));
      *(LAS float*)(mxw + u * 512) = mx;
    }
    STAMP(14);
    BAR();
    f32x4 ma[2], mb[2];
#pragma unroll
    for (int u = 0; u < 2; ++u) { ma[u] = LDSF4(mxr, u * 512); mb[u] = LDSF4(mxr, u * 512 + 16); }
    vr = LD32(vro);
    SB();
#pragma unroll
    for (int u = 0; u < 2; ++u) {
      m[u] = max2(max3(max3(ma[u][0], ma[u][1], ma[u][2]), ma[u][3], mb[u][0]), max3(mb[u][1], mb[u][2], mb[u][3]));
      float p[8];
#pragma unroll
      for (int i = 0; i < 8; ++i) p[i] = __builtin_amdgcn_exp2f(acc[u][i >> 2][i & 3] - m[u]);
      LDS128(plw, u * 1024) = v4u{cvtpk(p[0], p[1]), cvtpk(p[2], p[3]), cvtpk(p[4], p[5]), cvtpk(p[6], p[7])};
      if (NV0 + NVS + u < NV2) VLOAD(NV0 + NVS + u);
      SB();
    }
#pragma unroll
    for (int i = NV0 + NVS + 2; i < NV2; ++i) VLOAD(i);
  }
  STAMP(15);
  BAR();
  v4u ml[4];
#define ML_LOADS() do { _Pragma("unroll") for (int k = 0; k < 4; ++k) ml[k] = LD128(mlo, ((kb + k) & 3) * 512); } while (0)
  {
    v4u pf[16];
    f32x4 acc[3][2] = {};
    v4u ov[3];
    unsigned hv[3];
#define OSTORE(et) do { const f32x4 v0 = acc[et][0], v1 = acc[et][1]; \
      auto r0 = __builtin_amdgcn_permlane16_swap(cvtpk(v0[0], v0[1]), cvtpk(v1[0], v1[1]), false, false); \
      auto r1 = __builtin_amdgcn_permlane16_swap(cvtpk(v0[2], v0[3]), cvtpk(v1[2], v1[3]), false, false); \
      ov[et] = v4u{r0[0], r1[0], r0[1], r1[1]}; ST128S(ov[et], oso, (et) * 4096); \
      if (own) LDS128(lds + 16384 + ((w & 1) * 3 + (et)) * 1024, ((g & 1) * 32 + (g >> 1) * 16 + n16) * 16) = ov[et]; } while (0)
#define ORECORD(et) ST32(scan63(sum4(0u, ov[et])) + tag, roo + (et) * 512)
#define VCHECK(et) do { if (UNLIKELY(wsum(hv[et] - ((lane >> 3) == (et) ? vr : 0u)) + 8u * tag != 0u)) { unsigned it = 0; \
        do { RETRY(3); __builtin_amdgcn_s_sleep(2); CBAR(); \
          _Pragma("unroll") for (int i = 0; i < 8; ++i) VLOAD(8 * (et) + i); \
          vr = LD32(vro); hv[et] = 0; \
          _Pragma("unroll") for (int i = 0; i < 8; ++i) hv[et] = sum4(hv[et], VF(8 * (et) + i)); \
        } while (wsum(hv[et] - ((lane >> 3) == (et) ? vr : 0u)) + 8u * tag != 0u && ++it < SPIN_CAP); \
        acc[et][0] = acc[et][1] = f32x4{0.f, 0.f, 0.f, 0.f}; \
        _Pragma("unroll") for (int i = 0; i < 16; ++i) acc[et][i & 1] = mfma(vf[i >> 1][et], pf[i], acc[et][i & 1]); } } while (0)
#pragma unroll
    for (int i = 0; i < 8; ++i) pf[i] = LDS128(pl, i * 1024);
    SB();
    hv[0] = hv0; hv[1] = hv[2] = 0;
#pragma unroll
    for (int i = 0; i < 16; ++i) {
      if (i + 8 < 16) pf[i + 8] = LDS128(pl, (i + 8) * 1024);
      if ((i & 1) && NV2 + (i >> 1) < 24) VLOAD(NV2 + (i >> 1));
      acc[0][i & 1] = mfma(vf[i >> 1][0], pf[i], acc[0][i & 1]);
      if ((i & 1) && (i >> 1) >= NH) hv[0] = opq(sum4(hv[0], vf[i >> 1][0]));
      if (!(i & 1)) hv[1] = opq(sum4(hv[1], vf[i >> 1][1]));
      SB();
    }
    if (w == 0) {
      const v4u ones = {0x3f803f80u, 0x3f803f80u, 0x3f803f80u, 0x3f803f80u};
      f32x4 ls[2] = {};
#pragma unroll
      for (int i = 0; i < 16; ++i) ls[i & 1] = mfma(ones, pf[i], ls[i & 1]);
      ST128((v4u{__float_as_uint(g ? m[1] : m[0]), tag, __float_as_uint(g ? ls[1][0] : ls[0][0]), tag}), mso);
    }
    VCHECK(0);
#pragma unroll
    for (int i = 0; i < 16; ++i) {
      acc[1][i & 1] = mfma(vf[i >> 1][1], pf[i], acc[1][i & 1]);
      if (i == 0) OSTORE(0);
      if (i == 6) ORECORD(0);
      if ((i & 3) == 3) SB();
    }
    VCHECK(1);
#pragma unroll
    for (int i = 0; i < 16; ++i) {
      acc[2][i & 1] = mfma(vf[i >> 1][2], pf[i], acc[2][i & 1]);
      if (i & 1) hv[2] = opq(sum4(hv[2], vf[i >> 1][2]));
      if (i == 0) OSTORE(1);
      if (i == 6) ORECORD(1);
      if (i == 12) ML_LOADS();
      if ((i & 3) == 3) SB();
    }
    VCHECK(2);
    STAMP(16);
    OSTORE(2);
    ORECORD(2);
  }
  STAMP(17);
  BAR();
  if (w < 6) {
    v4u pp[4]; unsigned orr;
#define MLOK() __all(((ml[0][1] ^ tag) | (ml[1][1] ^ tag) | (ml[2][1] ^ tag) | (ml[3][1] ^ tag) | (ml[0][3] ^ tag) | (ml[1][3] ^ tag) | (ml[2][3] ^ tag) | (ml[3][3] ^ tag)) == 0u)
#define HOP3_LOADS() do { _Pragma("unroll") for (int k = 1; k < 4; ++k) pp[k] = LD128(l16, s_ol + ((kb + k) & 3) * 1024); orr = LD32(rlo); } while (0)
#define OBAD() (wsum(sum4(sum4(sum4(-orr, pp[1]), pp[2]), pp[3])) + 3u * tag != 0u)
    pp[0] = LDS128(pl, 16384 + w * 1024);
    SB();
    if (DLY3) __builtin_amdgcn_s_sleep(DLY3);
    HOP3_LOADS();
    SB();
    STAMP(18);
    if (UNLIKELY(!MLOK())) {
      unsigned it = 0;
      do { RETRY(6); __builtin_amdgcn_s_sleep(2); CBAR(); ML_LOADS(); } while (!MLOK() && ++it < SPIN_CAP);
    }
    const float mm = max2(max2(__uint_as_float(ml[0][0]), __uint_as_float(ml[1][0])), max2(__uint_as_float(ml[2][0]), __uint_as_float(ml[3][0])));
    float wk[4], o[8];
#pragma unroll
    for (int k = 0; k < 4; ++k) wk[k] = __builtin_amdgcn_exp2f(__uint_as_float(ml[k][0]) - mm);
    const float inv = __builtin_amdgcn_rcpf((wk[0] * __uint_as_float(ml[0][2]) + wk[1] * __uint_as_float(ml[1][2])) + (wk[2] * __uint_as_float(ml[2][2]) + wk[3] * __uint_as_float(ml[3][2])));
#pragma unroll
    for (int k = 0; k < 4; ++k) wk[k] *= inv;
#pragma unroll
    for (int e = 0; e < 4; ++e) { o[2 * e] = opq(fmaf(wk[0], bflo(pp[0][e]), bflo(xr[e]))); o[2 * e + 1] = opq(fmaf(wk[0], bfhi(pp[0][e]), bfhi(xr[e]))); }
    SB();
    if (UNLIKELY(OBAD())) {
      unsigned it = 0;
      do { RETRY(5); __builtin_amdgcn_s_sleep(3); CBAR(); HOP3_LOADS(); } while (OBAD() && ++it < SPIN_CAP);
    }
    STAMP(1);
    v4u ov;
#pragma unroll
    for (int e = 0; e < 4; ++e) {
#pragma unroll
      for (int k = 1; k < 4; ++k) { o[2 * e] = fmaf(wk[k], bflo(pp[k][e]), o[2 * e]); o[2 * e + 1] = fmaf(wk[k], bfhi(pp[k][e]), o[2 * e + 1]); }
      ov[e] = cvtpk(o[2 * e], o[2 * e + 1]);
    }
    *(v4u*)((unsigned char*)out + ooff) = ov;
  } else if (UNLIKELY(bid == 255 && w == 7)) {
#pragma unroll 1
    for (unsigned it = 0; it < SPIN_CAP; ++it) {
      CBAR();
      unsigned bad = 0;
#pragma unroll
      for (int k = 0; k < 4; ++k) bad |= LD32(O_CL + (k * 64 + lane) * 128) ^ tag;
      if (__all(bad == 0)) break;
    }
    if (lane == 0) __hip_atomic_store((gu32*)(ws + O_EPOCH), tag, __ATOMIC_RELAXED, __HIP_MEMORY_SCOPE_AGENT);
  }
  STAMP(19);
  STFLUSH();
}

extern "C" void kernel_launch(void* const* d_in, const int* in_sizes, int n_in, void* d_out, int out_size, void* d_ws, size_t ws_size,
                              hipStream_t stream) {
  (void)hipFuncSetAttribute((const void*)dit_kernel, hipFuncAttributeMaxDynamicSharedMemorySize, L_TOTAL);
  dit_kernel<<<256, 512, L_TOTAL, stream>>>((const u16*)d_in[0], (const u16*)d_in[1], (const u16*)d_in[2], (const u16*)d_in[3],
                                            (const u16*)d_in[4], (unsigned char*)d_ws, (u16*)d_out);
}
```

```cpp
#include <hip/hip_runtime.h>
#include <hip/hip_bf16.h>

typedef short bf16x8 __attribute__((ext_vector_type(8)));
typedef short s16x4 __attribute__((ext_vector_type(4)));
typedef float f32x4 __attribute__((ext_vector_type(4)));
typedef float f32x2 __attribute__((ext_vector_type(2)));
typedef unsigned v4u __attribute__((ext_vector_type(4)));
typedef unsigned v2u __attribute__((ext_vector_type(2)));
typedef __bf16 bf2 __attribute__((ext_vector_type(2)));
typedef unsigned short u16;
#define DI __device__ __forceinline__
#define LAS __attribute__((address_space(3)))
#define GAS __attribute__((address_space(1)))
typedef GAS unsigned gu32;
typedef LAS unsigned char* lp;
#define STDECL
#define STAMP(i)
#define STFLUSH()
#define RETRY(k)
#define DLY0 30
#define DLY1 8
#define DLY2 0
#define PRE 1
#define HP 9
#define MP 5
#define TOUCH 1
#define CODEPF 18000
#define XS 0
#define DLY3 3
#define PD 4
#define SLP 2
#define ROT 0
#define NX1 18
#define NEARLY 0
#ifndef NV0
#define NV0 0
#endif
#define AUXO 16
#define NH 6
#ifndef NV2
#define NV2 18
#endif
#define HL 8
#define MC 12
#define DLY4 18
#define DLY5 8
#define BA 4
#define GATE 2
#ifndef NVS
#define NVS 13
#endif

constexpr int O_EPOCH = 0;
constexpr int O_MOD = 4096;
constexpr int O_CL = 16384;
constexpr int O_MT = 131072;
constexpr int O_NF = O_MT + 589824;
constexpr int O_QF = O_NF + 1572864;
constexpr int O_VF = O_QF + 1572864;
constexpr int O_OF = O_VF + 1572864;
constexpr int O_ML = O_OF + 6291456;
constexpr int O_RM = O_ML + 131072;
constexpr int O_RN = O_RM + 1152 * 128;
constexpr int O_RQ = O_RN + 512 * 128;
constexpr int O_RV = O_RQ + 1536 * 128;
constexpr int O_RO = O_RV + 1536 * 128;
constexpr int WS_BYTES = O_RO + 6144 * 128;
constexpr int OOB = 0x40000000;

constexpr int L_P0 = 0;
constexpr int L_MS = 61440, L_QS = L_MS + 24576, L_CS = L_QS + 24576, L_MX = L_CS + 512, L_LS = L_MX + 1024, L_BN = L_LS + 1024;
constexpr int L_SCR = L_BN + 6144, L_TAG = L_SCR + 256;
constexpr int L_TOTAL = L_TAG + 32;

static_assert(NV2 >= 16 && NV0 + NVS <= NV2 && NV2 <= 24, "");
static_assert(BA < NX1 && MC + 3 < NX1 && MP < MC && HP < NX1 && NX1 >= 9 && NX1 <= 21, "");
constexpr int AUX_SC1 = 16;
constexpr unsigned SPIN_CAP = 1u << 16;

DI float bflo(unsigned v) { return __uint_as_float(v << 16); }
DI float bfhi(unsigned v) { return __uint_as_float(v & 0xffff0000u); }
DI unsigned cvtpk(float lo, float hi) {
  f32x2 x = {lo, hi};
  return __builtin_bit_cast(unsigned, __builtin_convertvector(x, bf2));
}
DI float bfround(float x) { return bflo(cvtpk(x, 0.f)); }
DI unsigned pkhi(unsigned lo, unsigned hi) { return __builtin_amdgcn_perm(hi, lo, 0x07060302u); }
DI unsigned tagged(float v, unsigned tag16) { return (cvtpk(0.f, v) & 0xffff0000u) | tag16; }
DI unsigned tchk(unsigned a, v4u v, unsigned tag) { return a | (v[0] ^ tag) | (v[1] ^ tag) | (v[2] ^ tag) | (v[3] ^ tag); }
DI unsigned sum4(unsigned a, v4u v) { return a + v[0] + v[1] + v[2] + v[3]; }
DI unsigned scan63(unsigned v) {
  v += __builtin_amdgcn_update_dpp(0u, v, 0x111, 0xf, 0xf, true);
  v += __builtin_amdgcn_update_dpp(0u, v, 0x112, 0xf, 0xf, true);
  v += __builtin_amdgcn_update_dpp(0u, v, 0x114, 0xf, 0xf, true);
  v += __builtin_amdgcn_update_dpp(0u, v, 0x118, 0xf, 0xf, true);
  v += __builtin_amdgcn_update_dpp(0u, v, 0x142, 0xa, 0xf, true);
  v += __builtin_amdgcn_update_dpp(0u, v, 0x143, 0xc, 0xf, true);
  return v;
}
DI float sum_g(float v);
DI float qsumf(float v) {
#define FSTEP(ctl) v += __uint_as_float(__builtin_amdgcn_update_dpp(0u, __float_as_uint(v), ctl, 0xf, 0xf, true))
  FSTEP(0x128); FSTEP(0x124);
#undef FSTEP
  return sum_g(v);
}
DI unsigned wsum(unsigned v) { return __builtin_amdgcn_readlane(scan63(v), 63); }
DI float max3(float a, float b, float c) { float r; asm("v_max3_f32 %0, %1, %2, %3" : "=v"(r) : "v"(a), "v"(b), "v"(c)); return r; }
DI float max2(float a, float b) { float r; asm("v_max_f32 %0, %1, %2" : "=v"(r) : "v"(a), "v"(b)); return r; }
DI float dot2(unsigned a, unsigned b, float c) { return __builtin_amdgcn_fdot2_f32_bf16(__builtin_bit_cast(bf2, a), __builtin_bit_cast(bf2, b), c, false); }
DI f32x4 mfma(v4u a, v4u b, f32x4 c) {
  return __builtin_amdgcn_mfma_f32_16x16x32_bf16(__builtin_bit_cast(bf16x8, a), __builtin_bit_cast(bf16x8, b), c, 0, 0, 0);
}
DI float sum_g(float v) {
  auto r = __builtin_amdgcn_permlane16_swap(__float_as_uint(v), __float_as_uint(v), false, false);
  v = __uint_as_float(r[0]) + __uint_as_float(r[1]);
  auto q = __builtin_amdgcn_permlane32_swap(__float_as_uint(v), __float_as_uint(v), false, false);
  return __uint_as_float(q[0]) + __uint_as_float(q[1]);
}
DI float max_g(float v) {
  auto r = __builtin_amdgcn_permlane16_swap(__float_as_uint(v), __float_as_uint(v), false, false);
  v = max2(__uint_as_float(r[0]), __uint_as_float(r[1]));
  auto q = __builtin_amdgcn_permlane32_swap(__float_as_uint(v), __float_as_uint(v), false, false);
  return max2(__uint_as_float(q[0]), __uint_as_float(q[1]));
}
DI int sopq(int v) { v = __builtin_amdgcn_readfirstlane(v); asm volatile("" : "+s"(v)); return v; }
template <typename T> DI T opq(T v) { asm volatile("" : "+v"(v)); return v; }
#define BAR() do { asm volatile("s_waitcnt lgkmcnt(0)" ::: "memory"); __builtin_amdgcn_s_barrier(); asm volatile("" ::: "memory"); } while (0)
#define VMWAIT(n) asm volatile("s_waitcnt vmcnt(%0)" :: "i"(n) : "memory")
#define SB() __builtin_amdgcn_sched_barrier(0)
#define CBAR() asm volatile("" ::: "memory")
#define LD128(off, so) __builtin_amdgcn_raw_buffer_load_b128(rws, (off), (so), AUX_SC1)
#define LD64(off) __builtin_amdgcn_raw_buffer_load_b64(rws, (off), 0, AUX_SC1)
#define LD32(off) __builtin_amdgcn_raw_buffer_load_b32(rws, (off), 0, AUX_SC1)
#define ST128(v, off) __builtin_amdgcn_raw_buffer_store_b128((v), rws, (off), 0, AUX_SC1)
#define ST128S(v, off, so) __builtin_amdgcn_raw_buffer_store_b128((v), rws, (off), (so), AUX_SC1)
#define ST64(v, off) __builtin_amdgcn_raw_buffer_store_b64((v), rws, (off), 0, AUX_SC1)
#define ST32(v, off) __builtin_amdgcn_raw_buffer_store_b32((v), rws, (off), 0, AUX_SC1)
#define ST16(v, off) __builtin_amdgcn_raw_buffer_store_b16((v), rws, (off), 0, AUX_SC1)
#define LDS128(p, off) (*(LAS v4u*)((p) + (off)))
#define LDSF4(p, off) (*(LAS f32x4*)((p) + (off)))
DI void dma(unsigned ldsaddr, int voff, __amdgpu_buffer_rsrc_t rs, int soff) {
  asm volatile("s_mov_b32 m0, %0\n\ts_nop 0\n\tbuffer_load_dwordx4 %1, %2, %3 offen lds" :: "s"(ldsaddr), "v"(voff), "s"(rs), "s"(soff) : "memory");
}

DI void touch(unsigned ldsaddr, int voff, __amdgpu_buffer_rsrc_t rs) {
  asm volatile("s_mov_b32 m0, %0\n\ts_nop 0\n\tbuffer_load_dword %1, %2, 0 offen lds" :: "s"(ldsaddr), "v"(voff), "s"(rs) : "memory");
}
#define UNLIKELY(c) __builtin_expect(!!(c), 0)

__global__ __launch_bounds__(512, 2) void dit_kernel(const u16* __restrict__ x, const u16* __restrict__ c, const u16* __restrict__ wqkv,
                                                     const u16* __restrict__ wo, const u16* __restrict__ wmod, unsigned char* ws,
                                                     u16* __restrict__ out) {
  extern __shared__ __attribute__((aligned(16))) unsigned char lds_[];
  lp lds = (lp)lds_;
  const unsigned ldsb = (unsigned)(size_t)lds;
  STDECL;
  STAMP(0);
  const unsigned long long pc0 = __builtin_amdgcn_s_getpc();
  const int tid = threadIdx.x, lane = tid & 63, w = __builtin_amdgcn_readfirstlane(tid >> 6);
  const int n16 = lane & 15, g = lane >> 4;
  const int bid = blockIdx.x, xg = bid & 7, r = bid >> 3;
  const int b = xg >> 2, ql = r & 7, kb = r >> 3;
  const int qt = b * 32 + (xg & 3) * 8 + ql;
  const int qt3 = b * 32 + (((xg & 3) + XS) & 3) * 8 + ql;
  const bool act1 = ql < 6;
  const int cb = (xg & 3) * 6 + (act1 ? ql : ql - 2);
  const __amdgpu_buffer_rsrc_t rws = __builtin_amdgcn_make_buffer_rsrc(ws, 0, WS_BYTES, 0x00020000);
  const __amdgpu_buffer_rsrc_t rx = __builtin_amdgcn_make_buffer_rsrc((void*)x, 0, 2048 * 768, 0x00020000);
  const int dch = 16 * ((lane & 3) ^ ((lane & 32) ? 3 : 0));
  const int rslot = (4 * n16 + (g ^ ((n16 & 8) ? 3 : 0))) * 16;
  const int l63 = lane == 63 ? 0 : OOB;

  const int mu = (((r - 24) >> 1) * 8 + xg) * 2 + (r & 1);
  const int role = r < 12 ? 0 : (r < 24 ? 1 : (mu < 36 ? 2 : 3));
  const int ru = role == 1 ? r - 12 : r;
  const int u12 = 6 * (xg & 1) + ru % 6, u8 = 2 * (xg >> 1) + ru / 6;
  unsigned ep;
  asm volatile("s_nop 4\n\tbuffer_load_dword %0, off, %1, 0 sc1" : "=v"(ep) : "s"(rws) : "memory");
  v4u nv[3];
  const int grp = bid * 2 + (w & 1);
  if (w < 6) {
    unsigned cv = 0;
    if (role == 2) cv = ((const unsigned*)c)[tid];
    if (role < 2) {
      const __amdgpu_buffer_rsrc_t rq = __builtin_amdgcn_make_buffer_rsrc((void*)wqkv, 0, 384 * 2304, 0x00020000);
      const int v = (lane >> 2) * 2304 + dch;
      const int s3 = 48 * u8 * 2304 + (role ? 1536 : 768) + 128 * w;
#pragma unroll
      for (int t = 0; t < 3; ++t) {
        dma(ldsb + L_P0 + (t * 12 + 2 * w) * 1024, v, rq, s3 + 16 * t * 2304);
        dma(ldsb + L_P0 + (t * 12 + 2 * w + 1) * 1024, v, rq, s3 + 16 * t * 2304 + 64);
      }
      if (role == 0) {
        const int s2 = 32 * u12 * 2304 + 128 * w;
#pragma unroll
        for (int t = 0; t < 2; ++t) {
          dma(ldsb + L_P0 + ((3 + t) * 12 + 2 * w) * 1024, v, rq, s2 + 16 * t * 2304);
          dma(ldsb + L_P0 + ((3 + t) * 12 + 2 * w + 1) * 1024, v, rq, s2 + 16 * t * 2304 + 64);
        }
      }
    }
    if (role == 1 || role == 2) {
      const int ld = role == 1 ? 768 : 4608;
      const __amdgpu_buffer_rsrc_t rc = __builtin_amdgcn_make_buffer_rsrc((void*)(role == 1 ? wo : wmod), 0, 384 * ld, 0x00020000);
      const int v = (lane >> 2) * ld + dch;
#pragma unroll
      for (int j = 0; j < 4; ++j) dma(ldsb + L_P0 + (36 + 4 * w + j) * 1024, v, rc, (role == 1 ? u12 : mu) * 64 + (4 * w + j) * 16 * ld);
    }
    if (role == 2) {
      const float f0 = bflo(cv), f1 = bfhi(cv);
      const int bb = tid >= 192, i2 = 2 * (tid - 192 * bb);
      *(LAS unsigned*)(lds + L_P0 + (i2 >> 5) * 1024 + (bb * 4 + ((i2 >> 3) & 3)) * 16 + (i2 & 7) * 2) =
          cvtpk(f0 * __builtin_amdgcn_rcpf(1.f + __expf(-f0)), f1 * __builtin_amdgcn_rcpf(1.f + __expf(-f1)));
    }
    VMWAIT(0);
  } else {
    __builtin_amdgcn_s_setprio(3);
    v4u xs[3];
    asm volatile("buffer_load_dwordx4 %0, %3, %4, 0 offen\n\tbuffer_load_dwordx4 %1, %3, %4, 0 offen offset:256\n\tbuffer_load_dwordx4 %2, %3, %4, 0 offen offset:512"
                 : "=&v"(xs[0]), "=&v"(xs[1]), "=&v"(xs[2]) : "v"((4 * grp + (lane & 3)) * 768 + (lane >> 2) * 16), "s"(rx) : "memory");
    {
      const unsigned scr = ldsb + L_SCR;
      const int dead = ((TOUCH & 1) && lane < 36) ? 0 : OOB;
      const __amdgpu_buffer_rsrc_t rq = __builtin_amdgcn_make_buffer_rsrc((void*)wqkv, 0, 384 * 2304, 0x00020000);
      touch(scr, dead + (w == 6 ? (96 * (xg >> 1) + 3 * r + lane / 12) * 2304 + 768 + (lane % 12) * 128 : (192 * (xg & 1) + 6 * r + lane / 6) * 2304 + (lane % 6) * 128), rq);
      const __amdgpu_buffer_rsrc_t r2 = __builtin_amdgcn_make_buffer_rsrc(w == 6 ? (void*)wo : (void*)(pc0 & ~127ull), 0, w == 6 ? 384 * 768 : CODEPF, 0x00020000);
      touch(scr, w == 6 ? dead + (12 * r + lane / 3) * 768 + 384 * (xg & 1) + (lane % 3) * 128 : (lane < 5 ? (r * 5 + lane) * 128 : OOB), r2);
    }
    if (w == 7) {
      const __amdgpu_buffer_rsrc_t r3 = __builtin_amdgcn_make_buffer_rsrc((void*)wmod, 0, 384 * 4608, 0x00020000);
      touch(ldsb + L_SCR, (lane < (xg < 2 ? 36 : 24) ? 0 : OOB) + (12 * r + lane % 12) * 4608 + ((lane / 12) * 8 + xg) * 128, r3);
    }
    else touch(ldsb + L_SCR, OOB, rx);
    asm volatile("s_waitcnt vmcnt(3)" : "+v"(xs[0]), "+v"(xs[1]), "+v"(xs[2]) :: "memory");
    float f[24], sm = 0.f, q = 0.f;
#pragma unroll
    for (int i = 0; i < 12; ++i) { f[2 * i] = bflo(xs[i >> 2][i & 3]); f[2 * i + 1] = bfhi(xs[i >> 2][i & 3]); sm += f[2 * i] + f[2 * i + 1]; }
    sm = qsumf(sm) * (1.f / 384.f);
#pragma unroll
    for (int i = 0; i < 24; ++i) { f[i] -= sm; q = fmaf(f[i], f[i], q); }
    const float rs = __builtin_amdgcn_rsqf(qsumf(q) * (1.f / 384.f) + 1e-5f);
#pragma unroll
    for (int i = 0; i < 3; ++i)
      nv[i] = opq(v4u{cvtpk(f[8 * i] * rs, f[8 * i + 1] * rs), cvtpk(f[8 * i + 2] * rs, f[8 * i + 3] * rs), cvtpk(f[8 * i + 4] * rs, f[8 * i + 5] * rs), cvtpk(f[8 * i + 6] * rs, f[8 * i + 7] * rs)});
  }
  asm volatile("" : "+v"(ep));
  const unsigned tag = __builtin_amdgcn_readfirstlane(ep) + 1u;
#define NSTORE() do { if (w >= 6) {     \
    const int no = O_NF + (((grp >> 3) * 2 + (grp & 1)) * 12 + g) * 1024 + (((lane >> 2) & 3) * 16 + ((grp >> 1) & 3) * 4 + (lane & 3)) * 16; \
    _Pragma("unroll") for (int i = 0; i < 3; ++i) ST128(nv[i], no + i * 4096); \
    ST64((v2u{scan63(sum4(sum4(sum4(0u, nv[0]), nv[1]), nv[2])) + tag, tag}), l63 + O_RN + grp * 128); \
    __builtin_amdgcn_s_setprio(0); } } while (0)
  if (NEARLY || role >= 2) NSTORE();
  STAMP(2);
  BAR();
  STAMP(3);
  if (!NEARLY && role < 2) NSTORE();
  const int rg = b * 32 + kb * 8 + w;
  const int nro = lane < 8 ? O_RN + (rg * 8 + lane) * 128 : OOB;
  if (w < (role == 2 ? 2 : 6) && role != 3) {
    const int t3 = w >> 1, t2 = w & 1;
    f32x4 acc = {};
    v4u a[12], bq[12];
    if (role == 0) {
      lp ap = opq(lds + L_P0 + (3 + t2) * 12288 + rslot), bp = opq(lds + L_P0 + t3 * 12288 + rslot);
#pragma unroll
      for (int k = 0; k < 12; ++k) {
        a[k] = LDS128(ap, k * 1024); bq[k] = LDS128(bp, k * 1024);
        if (k >= 5) acc = mfma(a[k - 5], bq[k - 5], acc);
        SB();
      }
    } else {
      lp ap = opq(lds + L_P0 + (role == 2 ? ((n16 & 1) * 4 + g) * 16 : t3 * 12288 + rslot));
      lp tb = opq(lds + L_P0 + 36864 + (8 * g + (n16 >> 2)) * 64 + 16 * ((2 * t2 + ((n16 >> 1) & 1)) ^ ((g & 1) ? 3 : 0)) + 8 * (n16 & 1));
#pragma unroll
      for (int k = 0; k < 12; ++k) {
        a[k] = LDS128(ap, k * 1024);
        bq[k] = __builtin_bit_cast(v4u, __builtin_shufflevector(__builtin_amdgcn_ds_read_tr16_b64_v4i16((LAS s16x4*)(tb + k * 2048)),
                                                                __builtin_amdgcn_ds_read_tr16_b64_v4i16((LAS s16x4*)(tb + k * 2048 + 256)), 0, 1, 2, 3, 4, 5, 6, 7));
        if (k >= 5) acc = mfma(a[k - 5], bq[k - 5], acc);
        SB();
      }
    }
#pragma unroll
    for (int k = 7; k < 12; ++k) acc = mfma(a[k], bq[k], acc);
    if (role != 2) {
      const int blk = role * 576 + (role == 0 ? (3 * u8 + t3) * 24 + 2 * u12 + t2 : (2 * u12 + t2) * 24 + 3 * u8 + t3);
      const unsigned o0 = cvtpk(acc[0], acc[1]), o1 = cvtpk(acc[2], acc[3]);
      auto r0 = __builtin_amdgcn_permlane16_swap(o0, o0, false, false);
      auto r1 = __builtin_amdgcn_permlane16_swap(o1, o1, false, false);
      ST128((v4u{r0[0], r1[0], r0[1], r1[1]}), ((g & 1) ? OOB : 0) + O_MT + blk * 512 + ((g >> 1) * 16 + n16) * 16);
      ST32(scan63(o0 + o1) + tag, l63 + O_RM + blk * 128);
    } else if (g < 2) {
      float v = bfround(g ? acc[1] : acc[0]);
      if (mu >= 12 && mu < 24) v += 1.f;
      ST32(tagged(v, tag & 0xffffu), O_MOD + (g * 1152 + 32 * mu + 16 * t2 + n16) * 4);
    }
  }
  STAMP(4);
  if (role == 3) __builtin_amdgcn_s_sleep(DLY0);
  else if (role == 2) { if (w >= 2) __builtin_amdgcn_s_sleep(DLY4); }
  else if (w >= 6) __builtin_amdgcn_s_sleep(DLY1);
  v2u nr = LD64(nro);
  SB();
  const int l16 = opq(lane * 16);
  const int s_n = O_NF + rg * 24576;
  const int rot = ROT ? (ql * 3) >> 1 : 0;
  const int l16b = opq(lane * 16 + 12288);
  v4u xf[2][12];
#define XF(i) xf[(i) & 1][(i) >> 1]
#define XLOAD(i) XF(i) = LD128(((i) & 1) ? l16b : l16, s_n + (((i) >> 1) + rot - (((i) >> 1) + rot >= 12 ? 12 : 0)) * 1024)
  lp fl = opq(lds + L_MS + lane * 16);
  v4u qf[3]; unsigned qr;
  const int qro = opq(lane < 3 ? O_RQ + (qt * 24 + 3 * w + lane) * 128 : OOB);
  const int s_ql = sopq(O_QF + (qt * 24 + 3 * w) * 1024);
#define HOP2_LOADS() do { _Pragma("unroll") for (int i = 0; i < 3; ++i) qf[i] = LD128(l16 + i * 1024, s_ql); qr = LD32(qro); } while (0)
  {
    const int s0 = 3 * (w & 3);
    const int mo = opq(O_MOD + (b * 1152 + (lane < 24 ? 384 + 32 * s0 + 4 * lane : (lane < 48 ? 32 * s0 + 4 * (lane - 24) : (w < 4 ? 384 : 768) + 16 * cb + 4 * (lane & 3)))) * 4);
    const int fb = (w >> 2) * 576 + cb * 24 + 2 * s0;
    const int ro = opq(lane < 6 ? O_RM + (fb + lane) * 128 : OOB);
    const int s_f = sopq(O_MT + fb * 512);
    const int blk1 = rg * 24 + cb, dead1 = act1 ? 0 : OOB;
    const int qso = opq(dead1 + O_QF + blk1 * 1024 + ((g & 1) * 32 + (g >> 1) * 16 + n16) * 16);
    const int vso = opq(dead1 + O_VF + blk1 * 1024 + lane * 16);
    const int rso = opq((dead1 | l63) + O_RQ + blk1 * 128);
    const int rvo = opq((dead1 | l63) + O_RV + (((b * 4 + kb) * 24 + cb) * 8 + w) * 128);
    lp cg = opq(lds + L_CS + g * 16), cn = opq(lds + L_CS + 256 + n16 * 4), csw = opq(lds + L_CS + w * 64 + n16 * 4);
    lp bn = lds + L_BN + w * 768, bg = opq(bn + g * 32), msw[3];
#pragma unroll
    for (int i = 0; i < 3; ++i) { int sl = s0 + i - rot; if (sl < 0) sl += 12; msw[i] = opq(fl + (w >> 2) * 12288 + sl * 1024); }
    SB();
    if (UNLIKELY(!__all(lane >= 8 || nr[1] == tag))) {
      unsigned it = 0;
      do { RETRY(0); __builtin_amdgcn_s_sleep(2); CBAR(); nr = LD64(nro); } while (!__all(lane >= 8 || nr[1] == tag) && ++it < SPIN_CAP);
    }
    if (GATE == 2) __builtin_amdgcn_s_barrier();
    STAMP(5);
    unsigned hx = 0;
    v4u mf[3], mv4; unsigned mr;
    f32x4 sa[3], sb[3]; v4u tf[3]; float oc_;
#define MODOK() __all((tchk(0u, mv4, tag) & 0xffffu) == 0u)
#define HOP1_LOADS() do { _Pragma("unroll") for (int i = 0; i < 3; ++i) mf[i] = LD128(l16 + i * 1024, s_f); mr = LD32(ro); } while (0)
#pragma unroll
    for (int i = 0; i < NX1; ++i) {
      XLOAD(i);
      if (i >= HL) hx = sum4(hx, XF(i - HL));
      if (i == MP) mv4 = LD128(mo, 0);
      if (i == HP) HOP1_LOADS();
      if (i == NX1 - 1) nr = LD64(nro);
      if (i == BA && w >= 4) __builtin_amdgcn_s_barrier();
      if (i == MC) {
        if (UNLIKELY(!MODOK())) {
          unsigned it = 0;
          do { RETRY(1); __builtin_amdgcn_s_sleep(8); CBAR(); mv4 = LD128(mo, 0); } while (!MODOK() && ++it < SPIN_CAP);
        }
        if (lane >= 24 && lane < 48) *(LAS v2u*)(bn + 512 + (lane - 24) * 8) = v2u{pkhi(mv4[0], mv4[1]), pkhi(mv4[2], mv4[3])};
        else LDS128(bn, (lane < 24 ? lane : (lane & 3) + 24) * 16) = mv4 & 0xffff0000u;
        CBAR();
#pragma unroll
        for (int k = 0; k < 3; ++k) { sa[k] = LDSF4(bg, 128 * k); sb[k] = LDSF4(bg, 128 * k + 16); tf[k] = LDS128(bn, 512 + 64 * k + 16 * g); }
        oc_ = *(LAS float*)(bn + 384 + n16 * 4);
      }
      if (i == MC + 1) { if (w < 4) oc_ *= 0.05103103630798288f * 1.4426950408889634f; sa[0] = opq(sa[0] * oc_); sb[0] = opq(sb[0] * oc_); }
      if (i == MC + 2) { sa[1] = opq(sa[1] * oc_); sb[1] = opq(sb[1] * oc_); }
      if (i == MC + 3) { sa[2] = opq(sa[2] * oc_); sb[2] = opq(sb[2] * oc_); }
      asm volatile("" : "+v"(hx));
      SB();
    }
    STAMP(6);
#define FBAD() (wsum(sum4(sum4(sum4(-mr, mf[0]), mf[1]), mf[2])) + 6u * tag != 0u)
    if (UNLIKELY(FBAD())) {
      unsigned it = 0;
      do { RETRY(9); __builtin_amdgcn_s_sleep(6); CBAR(); HOP1_LOADS(); } while (FBAD() && ++it < SPIN_CAP);
    }
    STAMP(7);
    {
      f32x4 au = {};
#pragma unroll
      for (int i = 0; i < 3; ++i) {
        au = mfma(tf[i], mf[i], au);
        const v4u o = {cvtpk(bflo(mf[i][0]) * sa[i][0], bfhi(mf[i][0]) * sa[i][1]), cvtpk(bflo(mf[i][1]) * sa[i][2], bfhi(mf[i][1]) * sa[i][3]),
                       cvtpk(bflo(mf[i][2]) * sb[i][0], bfhi(mf[i][2]) * sb[i][1]), cvtpk(bflo(mf[i][3]) * sb[i][2], bfhi(mf[i][3]) * sb[i][3])};
        LDS128(msw[i], 0) = o;
        if (NX1 + i < 24) XLOAD(NX1 + i);
        hx = opq(sum4(hx, XF(NX1 - HL + i)));
        SB();
      }
      *(LAS float*)(csw) = au[0] * oc_;
    }
    STAMP(9);
    __builtin_amdgcn_s_setprio(2);
    BAR();
    v4u a[24];
    f32x4 uu[4]; float zz[4], zs;
#define AOP(k) do { if ((k) + PD < ((k) < 12 ? 12 : 24)) a[(k) + PD] = LDS128(fl, ((k) + PD) * 1024); \
      if ((k) < 12) { aq[0] = mfma(a[k], xf[0][(k) % 12], aq[0]); aq[1] = mfma(a[k], xf[1][(k) % 12], aq[1]); } \
      else { av[0] = mfma(xf[0][(k) % 12], a[k], av[0]); av[1] = mfma(xf[1][(k) % 12], a[k], av[1]); } } while (0)
#pragma unroll
    for (int k = 0; k < PD; ++k) a[k] = LDS128(fl, k * 1024);
    SB();
    f32x4 aq[2] = {}, av[2] = {};
#pragma unroll
    for (int k = 0; k < 12; ++k) {
      if (NX1 + 3 + k < 24) XLOAD(NX1 + 3 + k);
      if (k >= 2 && k < 6) uu[k - 2] = LDSF4(cg, (k - 2) * 64);
      AOP(k);
      if (2 * k >= NX1 - HL + 3) hx = sum4(hx, XF(2 * k));
      if (2 * k + 1 >= NX1 - HL + 3) hx = sum4(hx, XF(2 * k + 1));
      if (k == 9) { uu[0] = uu[0] + uu[1]; uu[2] = uu[2] + uu[3]; }
      if (k == 10) uu[0] = uu[0] + uu[2];
      asm volatile("" : "+v"(hx));
      SB();
    }
    if (UNLIKELY(wsum(hx - nr[0]) + 8u * tag != 0u)) {
      unsigned it = 0;
      do { RETRY(4); __builtin_amdgcn_s_sleep(4); CBAR();
        hx = -LD32(nro);
#pragma unroll
        for (int i = 0; i < 24; ++i) XLOAD(i);
#pragma unroll
        for (int i = 0; i < 24; ++i) hx = sum4(hx, XF(i));
      } while (wsum(hx) + 8u * tag != 0u && ++it < SPIN_CAP);
      aq[0] = aq[1] = f32x4{0.f, 0.f, 0.f, 0.f};
#pragma unroll
      for (int s = 0; s < 12; ++s) {
        const v4u a0 = LDS128(fl, s * 1024);
        aq[0] = mfma(a0, xf[0][s], aq[0]); aq[1] = mfma(a0, xf[1][s], aq[1]);
      }
    }
    {
      const f32x4 v0 = aq[0] + uu[0], v1 = aq[1] + uu[0];
      auto r0 = __builtin_amdgcn_permlane16_swap(cvtpk(v0[0], v0[1]), cvtpk(v1[0], v1[1]), false, false);
      auto r1 = __builtin_amdgcn_permlane16_swap(cvtpk(v0[2], v0[3]), cvtpk(v1[2], v1[3]), false, false);
      const v4u ov = {r0[0], r1[0], r0[1], r1[1]};
      ST128(ov, qso);
      ST32(scan63(sum4(0u, ov)) + tag, rso);
      __builtin_amdgcn_s_setprio(0);
      STAMP(12);
      if (w < 4) BAR();
      CBAR();
#pragma unroll
      for (int k = 12; k < 12 + PD; ++k) a[k] = LDS128(fl, k * 1024);
      SB();
#pragma unroll
      for (int k = 12; k < 24; ++k) {
        if (k >= 14 && k < 18) zz[k - 14] = *(LAS float*)(cn + (k - 14) * 64);
        AOP(k);
        if (k == 21) zs = (zz[0] + zz[1]) + (zz[2] + zz[3]);
        SB();
      }
      const f32x4 w0 = av[0] + zs, w1 = av[1] + zs;
      const v4u o = {cvtpk(w0[0], w0[1]), cvtpk(w0[2], w0[3]), cvtpk(w1[0], w1[1]), cvtpk(w1[2], w1[3])};
      ST128(o, vso);
      ST32(scan63(sum4(0u, o)) + tag, rvo);
    }
    STAMP(10);
    SB();
  }
  STAMP(11);
  if (tid == 448) __hip_atomic_store((gu32*)(ws + O_CL + bid * 128), tag, __ATOMIC_RELAXED, __HIP_MEMORY_SCOPE_AGENT);
  const int vro = opq(lane < 24 ? O_RV + (((b * 4 + kb) * 8 + w) * 24 + lane) * 128 : OOB);
  const int mso = opq((w == 0 && g < 2) ? O_ML + ((qt * 4 + kb) * 32 + lane) * 16 : OOB);
  const int s_vl = sopq(O_VF + ((b * 32 + 8 * kb) * 24 + 3 * w) * 1024);
  const bool own = (w >> 1) == kb;
  const int oso = opq((own ? OOB : 0) + O_OF + ((qt * 24 + 3 * w) * 4 + kb) * 1024 + ((g & 1) * 32 + (g >> 1) * 16 + n16) * 16);
  const int roo = opq(((own ? OOB : 0) | l63) + O_RO + ((qt * 24 + 3 * w) * 4 + kb) * 128);
  lp mxr = opq(lds + L_MX + n16 * 32), mxw = opq(lds + L_MX + n16 * 32 + w * 4);
  lp plw = opq(lds + lane * 16 + w * 2048);
  lp pl = opq(lds + lane * 16);
  const int ooff = opq(((qt * 32 + 8 * (n16 >> 2) + 4 * (lane >> 5) + (n16 & 3)) * 384 + (6 * kb + w) * 16 + 8 * (g & 1)) * 2);
  const v4u xr = __builtin_amdgcn_raw_buffer_load_b128(rx, w < 6 ? ooff : OOB, 0, 0);
  const int mlo = opq(O_ML + (qt * 128 + (lane >> 5) * 16 + n16) * 16);
  const int s_ol = sopq(O_OF + (qt * 24 + 6 * kb + w) * 4096), rlo = opq((lane < 4 && lane != kb) ? O_RO + ((qt * 24 + 6 * kb + w) * 4 + lane) * 128 : OOB);
  v4u vf[8][3];
#define VF(i) vf[(i) & 7][(i) >> 3]
#define VLOAD(i) VF(i) = LD128(l16 + ((i) >> 3) * 1024, s_vl + ((i) & 7) * 24576)
  {
    lp qw = opq(lds + L_QS + (lane >> 5) * 12288 + 3 * w * 512 + (lane & 31) * 16);
    SB();
    __builtin_amdgcn_s_barrier();
    if (DLY2) __builtin_amdgcn_s_sleep(DLY2);
    HOP2_LOADS();
#pragma unroll
    for (int i = 0; i < NV0; ++i) VLOAD(i);
    SB();
#define QBAD() (wsum(sum4(sum4(sum4(-qr, qf[0]), qf[1]), qf[2])) + 3u * tag != 0u)
#pragma unroll
    for (int i = 0; i < 3; ++i) LDS128(qw, i * 512) = qf[i];
    if (UNLIKELY(QBAD())) {
      unsigned it = 0;
      do { RETRY(2); __builtin_amdgcn_s_sleep(3); CBAR(); HOP2_LOADS(); } while (QBAD() && ++it < SPIN_CAP);
#pragma unroll
      for (int i = 0; i < 3; ++i) LDS128(qw, i * 512) = qf[i];
    }
  }
  STAMP(13);
  BAR();
  float m[2]; unsigned vr, hv0 = 0;
  {
    v4u q[24];
#define QREAD(i) q[i] = LDS128(fl, 24576 + (((i) & 1) * 12 + ((i) >> 1)) * 1024)
#pragma unroll
    for (int i = 0; i < PD; ++i) QREAD(i);
    SB();
    f32x4 acc[2][2] = {};
#pragma unroll
    for (int i = 0; i < 24; ++i) {
      if (i + PD < 24) QREAD(i + PD);
      acc[i & 1][0] = mfma(xf[0][i >> 1], q[i], acc[i & 1][0]); acc[i & 1][1] = mfma(xf[1][i >> 1], q[i], acc[i & 1][1]);
      if ((i * NVS) / 24 != ((i + 1) * NVS) / 24) VLOAD(NV0 + (i * NVS) / 24);
      if (i >= 24 - NH) hv0 = opq(sum4(hv0, VF(i - (24 - NH))));
      SB();
    }
#pragma unroll
    for (int u = 0; u < 2; ++u) {
      float mx = max3(max3(acc[u][0][0], acc[u][0][1], acc[u][0][2]), acc[u][0][3], acc[u][1][0]);
      mx = max_g(max2(max3(mx, acc[u][1][1], acc[u][1][2]), acc[u][1][3]));
      *(LAS float*)(mxw + u * 512) = mx;
    }
    STAMP(14);
    BAR();
    f32x4 ma[2], mb[2];
#pragma unroll
    for (int u = 0; u < 2; ++u) { ma[u] = LDSF4(mxr, u * 512); mb[u] = LDSF4(mxr, u * 512 + 16); }
    vr = LD32(vro);
    SB();
#pragma unroll
    for (int u = 0; u < 2; ++u) {
      m[u] = max2(max3(max3(ma[u][0], ma[u][1], ma[u][2]), ma[u][3], mb[u][0]), max3(mb[u][1], mb[u][2], mb[u][3]));
      float p[8];
#pragma unroll
      for (int i = 0; i < 8; ++i) p[i] = __builtin_amdgcn_exp2f(acc[u][i >> 2][i & 3] - m[u]);
      LDS128(plw, u * 1024) = v4u{cvtpk(p[0], p[1]), cvtpk(p[2], p[3]), cvtpk(p[4], p[5]), cvtpk(p[6], p[7])};
      if (NV0 + NVS + u < NV2) VLOAD(NV0 + NVS + u);
      SB();
    }
#pragma unroll
    for (int i = NV0 + NVS + 2; i < NV2; ++i) VLOAD(i);
  }
  STAMP(15);
  BAR();
  v4u ml[4];
#define ML_LOADS() do { _Pragma("unroll") for (int k = 0; k < 4; ++k) ml[k] = LD128(mlo, ((kb + k) & 3) * 512); } while (0)
  {
    v4u pf[16];
    f32x4 acc[3][2] = {};
    v4u ov[3];
    unsigned hv[3];
#define OSTORE(et) do { const f32x4 v0 = acc[et][0], v1 = acc[et][1]; \
      auto r0 = __builtin_amdgcn_permlane16_swap(cvtpk(v0[0], v0[1]), cvtpk(v1[0], v1[1]), false, false); \
      auto r1 = __builtin_amdgcn_permlane16_swap(cvtpk(v0[2], v0[3]), cvtpk(v1[2], v1[3]), false, false); \
      ov[et] = v4u{r0[0], r1[0], r0[1], r1[1]}; ST128S(ov[et], oso, (et) * 4096); \
      if (own) LDS128(lds + 16384 + ((w & 1) * 3 + (et)) * 1024, ((g & 1) * 32 + (g >> 1) * 16 + n16) * 16) = ov[et]; } while (0)
#define ORECORD(et) ST32(scan63(sum4(0u, ov[et])) + tag, roo + (et) * 512)
#define VCHECK(et) do { if (UNLIKELY(wsum(hv[et] - ((lane >> 3) == (et) ? vr : 0u)) + 8u * tag != 0u)) { unsigned it = 0; \
        do { RETRY(3); __builtin_amdgcn_s_sleep(2); CBAR(); \
          _Pragma("unroll") for (int i = 0; i < 8; ++i) VLOAD(8 * (et) + i); \
          vr = LD32(vro); hv[et] = 0; \
          _Pragma("unroll") for (int i = 0; i < 8; ++i) hv[et] = sum4(hv[et], VF(8 * (et) + i)); \
        } while (wsum(hv[et] - ((lane >> 3) == (et) ? vr : 0u)) + 8u * tag != 0u && ++it < SPIN_CAP); \
        acc[et][0] = acc[et][1] = f32x4{0.f, 0.f, 0.f, 0.f}; \
        _Pragma("unroll") for (int i = 0; i < 16; ++i) acc[et][i & 1] = mfma(vf[i >> 1][et], pf[i], acc[et][i & 1]); } } while (0)
#pragma unroll
    for (int i = 0; i < 8; ++i) pf[i] = LDS128(pl, i * 1024);
    SB();
    hv[0] = hv0; hv[1] = hv[2] = 0;
#pragma unroll
    for (int i = 0; i < 16; ++i) {
      if (i + 8 < 16) pf[i + 8] = LDS128(pl, (i + 8) * 1024);
      if ((i & 1) && NV2 + (i >> 1) < 24) VLOAD(NV2 + (i >> 1));
      acc[0][i & 1] = mfma(vf[i >> 1][0], pf[i], acc[0][i & 1]);
      if ((i & 1) && (i >> 1) >= NH) hv[0] = opq(sum4(hv[0], vf[i >> 1][0]));
      if (!(i & 1)) hv[1] = opq(sum4(hv[1], vf[i >> 1][1]));
      SB();
    }
    if (w == 0) {
      const v4u ones = {0x3f803f80u, 0x3f803f80u, 0x3f803f80u, 0x3f803f80u};
      f32x4 ls[2] = {};
#pragma unroll
      for (int i = 0; i < 16; ++i) ls[i & 1] = mfma(ones, pf[i], ls[i & 1]);
      ST128((v4u{__float_as_uint(g ? m[1] : m[0]), tag, __float_as_uint(g ? ls[1][0] : ls[0][0]), tag}), mso);
    }
    VCHECK(0);
#pragma unroll
    for (int i = 0; i < 16; ++i) {
      acc[1][i & 1] = mfma(vf[i >> 1][1], pf[i], acc[1][i & 1]);
      if (i == 0) OSTORE(0);
      if (i == 6) ORECORD(0);
      if ((i & 3) == 3) SB();
    }
    VCHECK(1);
#pragma unroll
    for (int i = 0; i < 16; ++i) {
      acc[2][i & 1] = mfma(vf[i >> 1][2], pf[i], acc[2][i & 1]);
      if (i & 1) hv[2] = opq(sum4(hv[2], vf[i >> 1][2]));
      if (i == 0) OSTORE(1);
      if (i == 6) ORECORD(1);
      if (i == 12) ML_LOADS();
      if ((i & 3) == 3) SB();
    }
    VCHECK(2);
    STAMP(16);
    OSTORE(2);
    ORECORD(2);
  }
  STAMP(17);
  BAR();
  if (w < 6) {
    v4u pp[4]; unsigned orr;
#define MLOK() __all(((ml[0][1] ^ tag) | (ml[1][1] ^ tag) | (ml[2][1] ^ tag) | (ml[3][1] ^ tag) | (ml[0][3] ^ tag) | (ml[1][3] ^ tag) | (ml[2][3] ^ tag) | (ml[3][3] ^ tag)) == 0u)
#define HOP3_LOADS() do { _Pragma("unroll") for (int k = 1; k < 4; ++k) pp[k] = LD128(l16, s_ol + ((kb + k) & 3) * 1024); orr = LD32(rlo); } while (0)
#define OBAD() (wsum(sum4(sum4(sum4(-orr, pp[1]), pp[2]), pp[3])) + 3u * tag != 0u)
    pp[0] = LDS128(pl, 16384 + w * 1024);
    SB();
    if (DLY3) __builtin_amdgcn_s_sleep(DLY3);
    HOP3_LOADS();
    SB();
    STAMP(18);
    if (UNLIKELY(!MLOK())) {
      unsigned it = 0;
      do { RETRY(6); __builtin_amdgcn_s_sleep(2); CBAR(); ML_LOADS(); } while (!MLOK() && ++it < SPIN_CAP);
    }
    const float mm = max2(max2(__uint_as_float(ml[0][0]), __uint_as_float(ml[1][0])), max2(__uint_as_float(ml[2][0]), __uint_as_float(ml[3][0])));
    float wk[4], o[8];
#pragma unroll
    for (int k = 0; k < 4; ++k) wk[k] = __builtin_amdgcn_exp2f(__uint_as_float(ml[k][0]) - mm);
    const float inv = __builtin_amdgcn_rcpf((wk[0] * __uint_as_float(ml[0][2]) + wk[1] * __uint_as_float(ml[1][2])) + (wk[2] * __uint_as_float(ml[2][2]) + wk[3] * __uint_as_float(ml[3][2])));
#pragma unroll
    for (int k = 0; k < 4; ++k) wk[k] *= inv;
#pragma unroll
    for (int e = 0; e < 4; ++e) { o[2 * e] = opq(fmaf(wk[0], bflo(pp[0][e]), bflo(xr[e]))); o[2 * e + 1] = opq(fmaf(wk[0], bfhi(pp[0][e]), bfhi(xr[e]))); }
    SB();
    if (UNLIKELY(OBAD())) {
      unsigned it = 0;
      do { RETRY(5); __builtin_amdgcn_s_sleep(3); CBAR(); HOP3_LOADS(); } while (OBAD() && ++it < SPIN_CAP);
    }
    STAMP(1);
    v4u ov;
#pragma unroll
    for (int e = 0; e < 4; ++e) {
#pragma unroll
      for (int k = 1; k < 4; ++k) { o[2 * e] = fmaf(wk[k], bflo(pp[k][e]), o[2 * e]); o[2 * e + 1] = fmaf(wk[k], bfhi(pp[k][e]), o[2 * e + 1]); }
      ov[e] = cvtpk(o[2 * e], o[2 * e + 1]);
    }
    *(v4u*)((unsigned char*)out + ooff) = ov;
  } else if (UNLIKELY(bid == 255 && w == 7)) {
#pragma unroll 1
    for (unsigned it = 0; it < SPIN_CAP; ++it) {
      CBAR();
      unsigned bad = 0;
#pragma unroll
      for (int k = 0; k < 4; ++k) bad |= LD32(O_CL + (k * 64 + lane) * 128) ^ tag;
      if (__all(bad == 0)) break;
    }
    if (lane == 0) __hip_atomic_store((gu32*)(ws + O_EPOCH), tag, __ATOMIC_RELAXED, __HIP_MEMORY_SCOPE_AGENT);
  }
  STAMP(19);
  STFLUSH();
}

extern "C" void kernel_launch(void* const* d_in, const int* in_sizes, int n_in, void* d_out, int out_size, void* d_ws, size_t ws_size,
                              hipStream_t stream) {
  (void)hipFuncSetAttribute((const void*)dit_kernel, hipFuncAttributeMaxDynamicSharedMemorySize, L_TOTAL);
  dit_kernel<<<256, 512, L_TOTAL, stream>>>((const u16*)d_in[0], (const u16*)d_in[1], (const u16*)d_in[2], (const u16*)d_in[3],
                                            (const u16*)d_in[4], (unsigned char*)d_ws, (u16*)d_out);
}
```

```cpp
#include <hip/hip_runtime.h>
#include <hip/hip_bf16.h>

typedef short bf16x8 __attribute__((ext_vector_type(8)));
typedef short s16x4 __attribute__((ext_vector_type(4)));
typedef float f32x4 __attribute__((ext_vector_type(4)));
typedef float f32x2 __attribute__((ext_vector_type(2)));
typedef unsigned v4u __attribute__((ext_vector_type(4)));
typedef unsigned v2u __attribute__((ext_vector_type(2)));
typedef __bf16 bf2 __attribute__((ext_vector_type(2)));
typedef unsigned short u16;
#define DI __device__ __forceinline__
#define LAS __attribute__((address_space(3)))
#define GAS __attribute__((address_space(1)))
typedef GAS unsigned gu32;
typedef LAS unsigned char* lp;
#define STDECL
#define STAMP(i)
#define STFLUSH()
#define RETRY(k)
#define DLY0 30
#define DLY1 8
#define DLY2 0
#define PRE 1
#define HP 6
#define MP 2
#define TOUCH 1
#define CODEPF 18000
#define XS 0
#define DLY3 3
#define PD 4
#define SLP 2
#define ROT 0
#define NX1 16
#define NEARLY 0
#ifndef NV0
#define NV0 0
#endif
#define AUXO 16
#define NH 6
#ifndef NV2
#define NV2 18
#endif
#define HL 8
#define MC 9
#define DLY4 18
#define DLY5 8
#define BA 4
#define GATE 2
#ifndef NVS
#define NVS 13
#endif

constexpr int O_EPOCH = 0;
constexpr int O_MOD = 4096;
constexpr int O_CL = 16384;
constexpr int O_MT = 131072;
constexpr int O_NF = O_MT + 589824;
constexpr int O_QF = O_NF + 1572864;
constexpr int O_VF = O_QF + 1572864;
constexpr int O_OF = O_VF + 1572864;
constexpr int O_ML = O_OF + 6291456;
constexpr int O_RM = O_ML + 131072;
constexpr int O_RN = O_RM + 1152 * 128;
constexpr int O_RQ = O_RN + 512 * 128;
constexpr int O_RV = O_RQ + 1536 * 128;
constexpr int O_RO = O_RV + 1536 * 128;
constexpr int WS_BYTES = O_RO + 6144 * 128;
constexpr int OOB = 0x40000000;

constexpr int L_P0 = 0;
constexpr int L_MS = 61440, L_QS = L_MS + 24576, L_CS = L_QS + 24576, L_MX = L_CS + 512, L_LS = L_MX + 1024, L_BN = L_LS + 1024;
constexpr int L_SCR = L_BN + 6144, L_TAG = L_SCR + 256;
constexpr int L_TOTAL = L_TAG + 32;

static_assert(NV2 >= 16 && NV0 + NVS <= NV2 && NV2 <= 24, "");
static_assert(BA < NX1 && MC + 3 < NX1 && MP < MC && HP < NX1 && NX1 >= 9 && NX1 <= 21, "");
constexpr int AUX_SC1 = 16;
constexpr unsigned SPIN_CAP = 1u << 16;

DI float bflo(unsigned v) { return __uint_as_float(v << 16); }
DI float bfhi(unsigned v) { return __uint_as_float(v & 0xffff0000u); }
DI unsigned cvtpk(float lo, float hi) {
  f32x2 x = {lo, hi};
  return __builtin_bit_cast(unsigned, __builtin_convertvector(x, bf2));
}
DI float bfround(float x) { return bflo(cvtpk(x, 0.f)); }
DI unsigned pkhi(unsigned lo, unsigned hi) { return __builtin_amdgcn_perm(hi, lo, 0x07060302u); }
DI unsigned tagged(float v, unsigned tag16) { return (cvtpk(0.f, v) & 0xffff0000u) | tag16; }
DI unsigned tchk(unsigned a, v4u v, unsigned tag) { return a | (v[0] ^ tag) | (v[1] ^ tag) | (v[2] ^ tag) | (v[3] ^ tag); }
DI unsigned sum4(unsigned a, v4u v) { return a + v[0] + v[1] + v[2] + v[3]; }
DI unsigned scan63(unsigned v) {
  v += __builtin_amdgcn_update_dpp(0u, v, 0x111, 0xf, 0xf, true);
  v += __builtin_amdgcn_update_dpp(0u, v, 0x112, 0xf, 0xf, true);
  v += __builtin_amdgcn_update_dpp(0u, v, 0x114, 0xf, 0xf, true);
  v += __builtin_amdgcn_update_dpp(0u, v, 0x118, 0xf, 0xf, true);
  v += __builtin_amdgcn_update_dpp(0u, v, 0x142, 0xa, 0xf, true);
  v += __builtin_amdgcn_update_dpp(0u, v, 0x143, 0xc, 0xf, true);
  return v;
}
DI float sum_g(float v);
DI float qsumf(float v) {
#define FSTEP(ctl) v += __uint_as_float(__builtin_amdgcn_update_dpp(0u, __float_as_uint(v), ctl, 0xf, 0xf, true))
  FSTEP(0x128); FSTEP(0x124);
#undef FSTEP
  return sum_g(v);
}
DI unsigned wsum(unsigned v) { return __builtin_amdgcn_readlane(scan63(v), 63); }
DI float max3(float a, float b, float c) { float r; asm("v_max3_f32 %0, %1, %2, %3" : "=v"(r) : "v"(a), "v"(b), "v"(c)); return r; }
DI float max2(float a, float b) { float r; asm("v_max_f32 %0, %1, %2" : "=v"(r) : "v"(a), "v"(b)); return r; }
DI float dot2(unsigned a, unsigned b, float c) { return __builtin_amdgcn_fdot2_f32_bf16(__builtin_bit_cast(bf2, a), __builtin_bit_cast(bf2, b), c, false); }
DI f32x4 mfma(v4u a, v4u b, f32x4 c) {
  return __builtin_amdgcn_mfma_f32_16x16x32_bf16(__builtin_bit_cast(bf16x8, a), __builtin_bit_cast(bf16x8, b), c, 0, 0, 0);
}
DI float sum_g(float v) {
  auto r = __builtin_amdgcn_permlane16_swap(__float_as_uint(v), __float_as_uint(v), false, false);
  v = __uint_as_float(r[0]) + __uint_as_float(r[1]);
  auto q = __builtin_amdgcn_permlane32_swap(__float_as_uint(v), __float_as_uint(v), false, false);
  return __uint_as_float(q[0]) + __uint_as_float(q[1]);
}
DI float max_g(float v) {
  auto r = __builtin_amdgcn_permlane16_swap(__float_as_uint(v), __float_as_uint(v), false, false);
  v = max2(__uint_as_float(r[0]), __uint_as_float(r[1]));
  auto q = __builtin_amdgcn_permlane32_swap(__float_as_uint(v), __float_as_uint(v), false, false);
  return max2(__uint_as_float(q[0]), __uint_as_float(q[1]));
}
DI int sopq(int v) { v = __builtin_amdgcn_readfirstlane(v); asm volatile("" : "+s"(v)); return v; }
template <typename T> DI T opq(T v) { asm volatile("" : "+v"(v)); return v; }
#define BAR() do { asm volatile("s_waitcnt lgkmcnt(0)" ::: "memory"); __builtin_amdgcn_s_barrier(); asm volatile("" ::: "memory"); } while (0)
#define VMWAIT(n) asm volatile("s_waitcnt vmcnt(%0)" :: "i"(n) : "memory")
#define SB() __builtin_amdgcn_sched_barrier(0)
#define CBAR() asm volatile("" ::: "memory")
#define LD128(off, so) __builtin_amdgcn_raw_buffer_load_b128(rws, (off), (so), AUX_SC1)
#define LD64(off) __builtin_amdgcn_raw_buffer_load_b64(rws, (off), 0, AUX_SC1)
#define LD32(off) __builtin_amdgcn_raw_buffer_load_b32(rws, (off), 0, AUX_SC1)
#define ST128(v, off) __builtin_amdgcn_raw_buffer_store_b128((v), rws, (off), 0, AUX_SC1)
#define ST128S(v, off, so) __builtin_amdgcn_raw_buffer_store_b128((v), rws, (off), (so), AUX_SC1)
#define ST64(v, off) __builtin_amdgcn_raw_buffer_store_b64((v), rws, (off), 0, AUX_SC1)
#define ST32(v, off) __builtin_amdgcn_raw_buffer_store_b32((v), rws, (off), 0, AUX_SC1)
#define ST16(v, off) __builtin_amdgcn_raw_buffer_store_b16((v), rws, (off), 0, AUX_SC1)
#define LDS128(p, off) (*(LAS v4u*)((p) + (off)))
#define LDSF4(p, off) (*(LAS f32x4*)((p) + (off)))
DI void dma(unsigned ldsaddr, int voff, __amdgpu_buffer_rsrc_t rs, int soff) {
  asm volatile("s_mov_b32 m0, %0\n\ts_nop 0\n\tbuffer_load_dwordx4 %1, %2, %3 offen lds" :: "s"(ldsaddr), "v"(voff), "s"(rs), "s"(soff) : "memory");
}

DI void touch(unsigned ldsaddr, int voff, __amdgpu_buffer_rsrc_t rs) {
  asm volatile("s_mov_b32 m0, %0\n\ts_nop 0\n\tbuffer_load_dword %1, %2, 0 offen lds" :: "s"(ldsaddr), "v"(voff), "s"(rs) : "memory");
}
#define UNLIKELY(c) __builtin_expect(!!(c), 0)

__global__ __launch_bounds__(512, 2) void dit_kernel(const u16* __restrict__ x, const u16* __restrict__ c, const u16* __restrict__ wqkv,
                                                     const u16* __restrict__ wo, const u16* __restrict__ wmod, unsigned char* ws,
                                                     u16* __restrict__ out) {
  extern __shared__ __attribute__((aligned(16))) unsigned char lds_[];
  lp lds = (lp)lds_;
  const unsigned ldsb = (unsigned)(size_t)lds;
  STDECL;
  STAMP(0);
  const unsigned long long pc0 = __builtin_amdgcn_s_getpc();
  const int tid = threadIdx.x, lane = tid & 63, w = __builtin_amdgcn_readfirstlane(tid >> 6);
  const int n16 = lane & 15, g = lane >> 4;
  const int bid = blockIdx.x, xg = bid & 7, r = bid >> 3;
  const int b = xg >> 2, ql = r & 7, kb = r >> 3;
  const int qt = b * 32 + (xg & 3) * 8 + ql;
  const int qt3 = b * 32 + (((xg & 3) + XS) & 3) * 8 + ql;
  const bool act1 = ql < 6;
  const int cb = (xg & 3) * 6 + (act1 ? ql : ql - 2);
  const __amdgpu_buffer_rsrc_t rws = __builtin_amdgcn_make_buffer_rsrc(ws, 0, WS_BYTES, 0x00020000);
  const __amdgpu_buffer_rsrc_t rx = __builtin_amdgcn_make_buffer_rsrc((void*)x, 0, 2048 * 768, 0x00020000);
  const int dch = 16 * ((lane & 3) ^ ((lane & 32) ? 3 : 0));
  const int rslot = (4 * n16 + (g ^ ((n16 & 8) ? 3 : 0))) * 16;
  const int l63 = lane == 63 ? 0 : OOB;

  const int mu = (((r - 24) >> 1) * 8 + xg) * 2 + (r & 1);
  const int role = r < 12 ? 0 : (r < 24 ? 1 : (mu < 36 ? 2 : 3));
  const int ru = role == 1 ? r - 12 : r;
  const int u12 = 6 * (xg & 1) + ru % 6, u8 = 2 * (xg >> 1) + ru / 6;
  unsigned ep;
  asm volatile("s_nop 4\n\tbuffer_load_dword %0, off, %1, 0 sc1" : "=v"(ep) : "s"(rws) : "memory");
  v4u nv[3];
  const int grp = bid * 2 + (w & 1);
  if (w < 6) {
    unsigned cv = 0;
    if (role == 2) cv = ((const unsigned*)c)[tid];
    if (role < 2) {
      const __amdgpu_buffer_rsrc_t rq = __builtin_amdgcn_make_buffer_rsrc((void*)wqkv, 0, 384 * 2304, 0x00020000);
      const int v = (lane >> 2) * 2304 + dch;
      const int s3 = 48 * u8 * 2304 + (role ? 1536 : 768) + 128 * w;
#pragma unroll
      for (int t = 0; t < 3; ++t) {
        dma(ldsb + L_P0 + (t * 12 + 2 * w) * 1024, v, rq, s3 + 16 * t * 2304);
        dma(ldsb + L_P0 + (t * 12 + 2 * w + 1) * 1024, v, rq, s3 + 16 * t * 2304 + 64);
      }
      if (role == 0) {
        const int s2 = 32 * u12 * 2304 + 128 * w;
#pragma unroll
        for (int t = 0; t < 2; ++t) {
          dma(ldsb + L_P0 + ((3 + t) * 12 + 2 * w) * 1024, v, rq, s2 + 16 * t * 2304);
          dma(ldsb + L_P0 + ((3 + t) * 12 + 2 * w + 1) * 1024, v, rq, s2 + 16 * t * 2304 + 64);
        }
      }
    }
    if (role == 1 || role == 2) {
      const int ld = role == 1 ? 768 : 4608;
      const __amdgpu_buffer_rsrc_t rc = __builtin_amdgcn_make_buffer_rsrc((void*)(role == 1 ? wo : wmod), 0, 384 * ld, 0x00020000);
      const int v = (lane >> 2) * ld + dch;
#pragma unroll
      for (int j = 0; j < 4; ++j) dma(ldsb + L_P0 + (36 + 4 * w + j) * 1024, v, rc, (role == 1 ? u12 : mu) * 64 + (4 * w + j) * 16 * ld);
    }
    if (role == 2) {
      const float f0 = bflo(cv), f1 = bfhi(cv);
      const int bb = tid >= 192, i2 = 2 * (tid - 192 * bb);
      *(LAS unsigned*)(lds + L_P0 + (i2 >> 5) * 1024 + (bb * 4 + ((i2 >> 3) & 3)) * 16 + (i2 & 7) * 2) =
          cvtpk(f0 * __builtin_amdgcn_rcpf(1.f + __expf(-f0)), f1 * __builtin_amdgcn_rcpf(1.f + __expf(-f1)));
    }
    VMWAIT(0);
  } else {
    __builtin_amdgcn_s_setprio(3);
    v4u xs[3];
    asm volatile("buffer_load_dwordx4 %0, %3, %4, 0 offen\n\tbuffer_load_dwordx4 %1, %3, %4, 0 offen offset:256\n\tbuffer_load_dwordx4 %2, %3, %4, 0 offen offset:512"
                 : "=&v"(xs[0]), "=&v"(xs[1]), "=&v"(xs[2]) : "v"((4 * grp + (lane & 3)) * 768 + (lane >> 2) * 16), "s"(rx) : "memory");
    {
      const unsigned scr = ldsb + L_SCR;
      const int dead = ((TOUCH & 1) && lane < 36) ? 0 : OOB;
      const __amdgpu_buffer_rsrc_t rq = __builtin_amdgcn_make_buffer_rsrc((void*)wqkv, 0, 384 * 2304, 0x00020000);
      touch(scr, dead + (w == 6 ? (96 * (xg >> 1) + 3 * r + lane / 12) * 2304 + 768 + (lane % 12) * 128 : (192 * (xg & 1) + 6 * r + lane / 6) * 2304 + (lane % 6) * 128), rq);
      const __amdgpu_buffer_rsrc_t r2 = __builtin_amdgcn_make_buffer_rsrc(w == 6 ? (void*)wo : (void*)(pc0 & ~127ull), 0, w == 6 ? 384 * 768 : CODEPF, 0x00020000);
      touch(scr, w == 6 ? dead + (12 * r + lane / 3) * 768 + 384 * (xg & 1) + (lane % 3) * 128 : (lane < 5 ? (r * 5 + lane) * 128 : OOB), r2);
    }
    if (w == 7) {
      const __amdgpu_buffer_rsrc_t r3 = __builtin_amdgcn_make_buffer_rsrc((void*)wmod, 0, 384 * 4608, 0x00020000);
      touch(ldsb + L_SCR, (lane < (xg < 2 ? 36 : 24) ? 0 : OOB) + (12 * r + lane % 12) * 4608 + ((lane / 12) * 8 + xg) * 128, r3);
    }
    else touch(ldsb + L_SCR, OOB, rx);
    asm volatile("s_waitcnt vmcnt(3)" : "+v"(xs[0]), "+v"(xs[1]), "+v"(xs[2]) :: "memory");
    float f[24], sm = 0.f, q = 0.f;
#pragma unroll
    for (int i = 0; i < 12; ++i) { f[2 * i] = bflo(xs[i >> 2][i & 3]); f[2 * i + 1] = bfhi(xs[i >> 2][i & 3]); sm += f[2 * i] + f[2 * i + 1]; }
    sm = qsumf(sm) * (1.f / 384.f);
#pragma unroll
    for (int i = 0; i < 24; ++i) { f[i] -= sm; q = fmaf(f[i], f[i], q); }
    const float rs = __builtin_amdgcn_rsqf(qsumf(q) * (1.f / 384.f) + 1e-5f);
#pragma unroll
    for (int i = 0; i < 3; ++i)
      nv[i] = opq(v4u{cvtpk(f[8 * i] * rs, f[8 * i + 1] * rs), cvtpk(f[8 * i + 2] * rs, f[8 * i + 3] * rs), cvtpk(f[8 * i + 4] * rs, f[8 * i + 5] * rs), cvtpk(f[8 * i + 6] * rs, f[8 * i + 7] * rs)});
  }
  asm volatile("" : "+v"(ep));
  const unsigned tag = __builtin_amdgcn_readfirstlane(ep) + 1u;
#define NSTORE() do { if (w >= 6) {     \
    const int no = O_NF + (((grp >> 3) * 2 + (grp & 1)) * 12 + g) * 1024 + (((lane >> 2) & 3) * 16 + ((grp >> 1) & 3) * 4 + (lane & 3)) * 16; \
    _Pragma("unroll") for (int i = 0; i < 3; ++i) ST128(nv[i], no + i * 4096); \
    ST64((v2u{scan63(sum4(sum4(sum4(0u, nv[0]), nv[1]), nv[2])) + tag, tag}), l63 + O_RN + grp * 128); \
    __builtin_amdgcn_s_setprio(0); } } while (0)
  if (NEARLY || role >= 2) NSTORE();
  STAMP(2);
  BAR();
  STAMP(3);
  if (!NEARLY && role < 2) NSTORE();
  const int rg = b * 32 + kb * 8 + w;
  const int nro = lane < 8 ? O_RN + (rg * 8 + lane) * 128 : OOB;
  if (w < (role == 2 ? 2 : 6) && role != 3) {
    const int t3 = w >> 1, t2 = w & 1;
    f32x4 acc = {};
    v4u a[12], bq[12];
    if (role == 0) {
      lp ap = opq(lds + L_P0 + (3 + t2) * 12288 + rslot), bp = opq(lds + L_P0 + t3 * 12288 + rslot);
#pragma unroll
      for (int k = 0; k < 12; ++k) {
        a[k] = LDS128(ap, k * 1024); bq[k] = LDS128(bp, k * 1024);
        if (k >= 5) acc = mfma(a[k - 5], bq[k - 5], acc);
        SB();
      }
    } else {
      lp ap = opq(lds + L_P0 + (role == 2 ? ((n16 & 1) * 4 + g) * 16 : t3 * 12288 + rslot));
      lp tb = opq(lds + L_P0 + 36864 + (8 * g + (n16 >> 2)) * 64 + 16 * ((2 * t2 + ((n16 >> 1) & 1)) ^ ((g & 1) ? 3 : 0)) + 8 * (n16 & 1));
#pragma unroll
      for (int k = 0; k < 12; ++k) {
        a[k] = LDS128(ap, k * 1024);
        bq[k] = __builtin_bit_cast(v4u, __builtin_shufflevector(__builtin_amdgcn_ds_read_tr16_b64_v4i16((LAS s16x4*)(tb + k * 2048)),
                                                                __builtin_amdgcn_ds_read_tr16_b64_v4i16((LAS s16x4*)(tb + k * 2048 + 256)), 0, 1, 2, 3, 4, 5, 6, 7));
        if (k >= 5) acc = mfma(a[k - 5], bq[k - 5], acc);
        SB();
      }
    }
#pragma unroll
    for (int k = 7; k < 12; ++k) acc = mfma(a[k], bq[k], acc);
    if (role != 2) {
      const int blk = role * 576 + (role == 0 ? (3 * u8 + t3) * 24 + 2 * u12 + t2 : (2 * u12 + t2) * 24 + 3 * u8 + t3);
      const unsigned o0 = cvtpk(acc[0], acc[1]), o1 = cvtpk(acc[2], acc[3]);
      auto r0 = __builtin_amdgcn_permlane16_swap(o0, o0, false, false);
      auto r1 = __builtin_amdgcn_permlane16_swap(o1, o1, false, false);
      ST128((v4u{r0[0], r1[0], r0[1], r1[1]}), ((g & 1) ? OOB : 0) + O_MT + blk * 512 + ((g >> 1) * 16 + n16) * 16);
      ST32(scan63(o0 + o1) + tag, l63 + O_RM + blk * 128);
    } else if (g < 2) {
      float v = bfround(g ? acc[1] : acc[0]);
      if (mu >= 12 && mu < 24) v += 1.f;
      ST32(tagged(v, tag & 0xffffu), O_MOD + (g * 1152 + 32 * mu + 16 * t2 + n16) * 4);
    }
  }
  STAMP(4);
  if (role == 3) __builtin_amdgcn_s_sleep(DLY0);
  else if (role == 2) { if (w >= 2) __builtin_amdgcn_s_sleep(DLY4); }
  else if (w >= 6) __builtin_amdgcn_s_sleep(DLY1);
  v2u nr = LD64(nro);
  SB();
  const int l16 = opq(lane * 16);
  const int s_n = O_NF + rg * 24576;
  const int rot = ROT ? (ql * 3) >> 1 : 0;
  const int l16b = opq(lane * 16 + 12288);
  v4u xf[2][12];
#define XF(i) xf[(i) & 1][(i) >> 1]
#define XLOAD(i) XF(i) = LD128(((i) & 1) ? l16b : l16, s_n + (((i) >> 1) + rot - (((i) >> 1) + rot >= 12 ? 12 : 0)) * 1024)
  lp fl = opq(lds + L_MS + lane * 16);
  v4u qf[3]; unsigned qr;
  const int qro = opq(lane < 3 ? O_RQ + (qt * 24 + 3 * w + lane) * 128 : OOB);
  const int s_ql = sopq(O_QF + (qt * 24 + 3 * w) * 1024);
#define HOP2_LOADS() do { _Pragma("unroll") for (int i = 0; i < 3; ++i) qf[i] = LD128(l16 + i * 1024, s_ql); qr = LD32(qro); } while (0)
  {
    const int s0 = 3 * (w & 3);
    const int mo = opq(O_MOD + (b * 1152 + (lane < 24 ? 384 + 32 * s0 + 4 * lane : (lane < 48 ? 32 * s0 + 4 * (lane - 24) : (w < 4 ? 384 : 768) + 16 * cb + 4 * (lane & 3)))) * 4);
    const int fb = (w >> 2) * 576 + cb * 24 + 2 * s0;
    const int ro = opq(lane < 6 ? O_RM + (fb + lane) * 128 : OOB);
    const int s_f = sopq(O_MT + fb * 512);
    const int blk1 = rg * 24 + cb, dead1 = act1 ? 0 : OOB;
    const int qso = opq(dead1 + O_QF + blk1 * 1024 + ((g & 1) * 32 + (g >> 1) * 16 + n16) * 16);
    const int vso = opq(dead1 + O_VF + blk1 * 1024 + lane * 16);
    const int rso = opq((dead1 | l63) + O_RQ + blk1 * 128);
    const int rvo = opq((dead1 | l63) + O_RV + (((b * 4 + kb) * 24 + cb) * 8 + w) * 128);
    lp cg = opq(lds + L_CS + g * 16), cn = opq(lds + L_CS + 256 + n16 * 4), csw = opq(lds + L_CS + w * 64 + n16 * 4);
    lp bn = lds + L_BN + w * 768, bg = opq(bn + g * 32), msw[3];
#pragma unroll
    for (int i = 0; i < 3; ++i) { int sl = s0 + i - rot; if (sl < 0) sl += 12; msw[i] = opq(fl + (w >> 2) * 12288 + sl * 1024); }
    SB();
    if (UNLIKELY(!__all(lane >= 8 || nr[1] == tag))) {
      unsigned it = 0;
      do { RETRY(0); __builtin_amdgcn_s_sleep(2); CBAR(); nr = LD64(nro); } while (!__all(lane >= 8 || nr[1] == tag) && ++it < SPIN_CAP);
    }
    if (GATE == 2) __builtin_amdgcn_s_barrier();
    STAMP(5);
    unsigned hx = 0;
    v4u mf[3], mv4; unsigned mr;
    f32x4 sa[3], sb[3]; v4u tf[3]; float oc_;
#define MODOK() __all((tchk(0u, mv4, tag) & 0xffffu) == 0u)
#define HOP1_LOADS() do { _Pragma("unroll") for (int i = 0; i < 3; ++i) mf[i] = LD128(l16 + i * 1024, s_f); mr = LD32(ro); } while (0)
#pragma unroll
    for (int i = 0; i < NX1; ++i) {
      XLOAD(i);
      if (i >= HL) hx = sum4(hx, XF(i - HL));
      if (i == MP) mv4 = LD128(mo, 0);
      if (i == HP) HOP1_LOADS();
      if (i == NX1 - 1) nr = LD64(nro);
      if (i == BA && w >= 4) __builtin_amdgcn_s_barrier();
      if (i == MC) {
        if (UNLIKELY(!MODOK())) {
          unsigned it = 0;
          do { RETRY(1); __builtin_amdgcn_s_sleep(8); CBAR(); mv4 = LD128(mo, 0); } while (!MODOK() && ++it < SPIN_CAP);
        }
        if (lane >= 24 && lane < 48) *(LAS v2u*)(bn + 512 + (lane - 24) * 8) = v2u{pkhi(mv4[0], mv4[1]), pkhi(mv4[2], mv4[3])};
        else LDS128(bn, (lane < 24 ? lane : (lane & 3) + 24) * 16) = mv4 & 0xffff0000u;
        CBAR();
#pragma unroll
        for (int k = 0; k < 3; ++k) { sa[k] = LDSF4(bg, 128 * k); sb[k] = LDSF4(bg, 128 * k + 16); tf[k] = LDS128(bn, 512 + 64 * k + 16 * g); }
        oc_ = *(LAS float*)(bn + 384 + n16 * 4);
      }
      if (i == MC + 1) { if (w < 4) oc_ *= 0.05103103630798288f * 1.4426950408889634f; sa[0] = opq(sa[0] * oc_); sb[0] = opq(sb[0] * oc_); }
      if (i == MC + 2) { sa[1] = opq(sa[1] * oc_); sb[1] = opq(sb[1] * oc_); }
      if (i == MC + 3) { sa[2] = opq(sa[2] * oc_); sb[2] = opq(sb[2] * oc_); }
      asm volatile("" : "+v"(hx));
      SB();
    }
    STAMP(6);
#define FBAD() (wsum(sum4(sum4(sum4(-mr, mf[0]), mf[1]), mf[2])) + 6u * tag != 0u)
    if (UNLIKELY(FBAD())) {
      unsigned it = 0;
      do { RETRY(9); __builtin_amdgcn_s_sleep(6); CBAR(); HOP1_LOADS(); } while (FBAD() && ++it < SPIN_CAP);
    }
    STAMP(7);
    {
      f32x4 au = {};
#pragma unroll
      for (int i = 0; i < 3; ++i) {
        au = mfma(tf[i], mf[i], au);
        const v4u o = {cvtpk(bflo(mf[i][0]) * sa[i][0], bfhi(mf[i][0]) * sa[i][1]), cvtpk(bflo(mf[i][1]) * sa[i][2], bfhi(mf[i][1]) * sa[i][3]),
                       cvtpk(bflo(mf[i][2]) * sb[i][0], bfhi(mf[i][2]) * sb[i][1]), cvtpk(bflo(mf[i][3]) * sb[i][2], bfhi(mf[i][3]) * sb[i][3])};
        LDS128(msw[i], 0) = o;
        if (NX1 + i < 24) XLOAD(NX1 + i);
        hx = opq(sum4(hx, XF(NX1 - HL + i)));
        SB();
      }
      *(LAS float*)(csw) = au[0] * oc_;
    }
    STAMP(9);
    __builtin_amdgcn_s_setprio(2);
    BAR();
    v4u a[24];
    f32x4 uu[4]; float zz[4], zs;
#define AOP(k) do { if ((k) + PD < ((k) < 12 ? 12 : 24)) a[(k) + PD] = LDS128(fl, ((k) + PD) * 1024); \
      if ((k) < 12) { aq[0] = mfma(a[k], xf[0][(k) % 12], aq[0]); aq[1] = mfma(a[k], xf[1][(k) % 12], aq[1]); } \
      else { av[0] = mfma(xf[0][(k) % 12], a[k], av[0]); av[1] = mfma(xf[1][(k) % 12], a[k], av[1]); } } while (0)
#pragma unroll
    for (int k = 0; k < PD; ++k) a[k] = LDS128(fl, k * 1024);
    SB();
    f32x4 aq[2] = {}, av[2] = {};
#pragma unroll
    for (int k = 0; k < 12; ++k) {
      if (NX1 + 3 + k < 24) XLOAD(NX1 + 3 + k);
      if (k >= 2 && k < 6) uu[k - 2] = LDSF4(cg, (k - 2) * 64);
      AOP(k);
      if (2 * k >= NX1 - HL + 3) hx = sum4(hx, XF(2 * k));
      if (2 * k + 1 >= NX1 - HL + 3) hx = sum4(hx, XF(2 * k + 1));
      if (k == 9) { uu[0] = uu[0] + uu[1]; uu[2] = uu[2] + uu[3]; }
      if (k == 10) uu[0] = uu[0] + uu[2];
      asm volatile("" : "+v"(hx));
      SB();
    }
    if (UNLIKELY(wsum(hx - nr[0]) + 8u * tag != 0u)) {
      unsigned it = 0;
      do { RETRY(4); __builtin_amdgcn_s_sleep(4); CBAR();
        hx = -LD32(nro);
#pragma unroll
        for (int i = 0; i < 24; ++i) XLOAD(i);
#pragma unroll
        for (int i = 0; i < 24; ++i) hx = sum4(hx, XF(i));
      } while (wsum(hx) + 8u * tag != 0u && ++it < SPIN_CAP);
      aq[0] = aq[1] = f32x4{0.f, 0.f, 0.f, 0.f};
#pragma unroll
      for (int s = 0; s < 12; ++s) {
        const v4u a0 = LDS128(fl, s * 1024);
        aq[0] = mfma(a0, xf[0][s], aq[0]); aq[1] = mfma(a0, xf[1][s], aq[1]);
      }
    }
    {
      const f32x4 v0 = aq[0] + uu[0], v1 = aq[1] + uu[0];
      auto r0 = __builtin_amdgcn_permlane16_swap(cvtpk(v0[0], v0[1]), cvtpk(v1[0], v1[1]), false, false);
      auto r1 = __builtin_amdgcn_permlane16_swap(cvtpk(v0[2], v0[3]), cvtpk(v1[2], v1[3]), false, false);
      const v4u ov = {r0[0], r1[0], r0[1], r1[1]};
      ST128(ov, qso);
      ST32(scan63(sum4(0u, ov)) + tag, rso);
      __builtin_amdgcn_s_setprio(0);
      STAMP(12);
      if (w < 4) BAR();
      CBAR();
#pragma unroll
      for (int k = 12; k < 12 + PD; ++k) a[k] = LDS128(fl, k * 1024);
      SB();
#pragma unroll
      for (int k = 12; k < 24; ++k) {
        if (k >= 14 && k < 18) zz[k - 14] = *(LAS float*)(cn + (k - 14) * 64);
        AOP(k);
        if (k == 21) zs = (zz[0] + zz[1]) + (zz[2] + zz[3]);
        SB();
      }
      const f32x4 w0 = av[0] + zs, w1 = av[1] + zs;
      const v4u o = {cvtpk(w0[0], w0[1]), cvtpk(w0[2], w0[3]), cvtpk(w1[0], w1[1]), cvtpk(w1[2], w1[3])};
      ST128(o, vso);
      ST32(scan63(sum4(0u, o)) + tag, rvo);
    }
    STAMP(10);
    SB();
  }
  STAMP(11);
  if (tid == 448) __hip_atomic_store((gu32*)(ws + O_CL + bid * 128), tag, __ATOMIC_RELAXED, __HIP_MEMORY_SCOPE_AGENT);
  const int vro = opq(lane < 24 ? O_RV + (((b * 4 + kb) * 8 + w) * 24 + lane) * 128 : OOB);
  const int mso = opq((w == 0 && g < 2) ? O_ML + ((qt * 4 + kb) * 32 + lane) * 16 : OOB);
  const int s_vl = sopq(O_VF + ((b * 32 + 8 * kb) * 24 + 3 * w) * 1024);
  const bool own = (w >> 1) == kb;
  const int oso = opq((own ? OOB : 0) + O_OF + ((qt * 24 + 3 * w) * 4 + kb) * 1024 + ((g & 1) * 32 + (g >> 1) * 16 + n16) * 16);
  const int roo = opq(((own ? OOB : 0) | l63) + O_RO + ((qt * 24 + 3 * w) * 4 + kb) * 128);
  lp mxr = opq(lds + L_MX + n16 * 32), mxw = opq(lds + L_MX + n16 * 32 + w * 4);
  lp plw = opq(lds + lane * 16 + w * 2048);
  lp pl = opq(lds + lane * 16);
  const int ooff = opq(((qt * 32 + 8 * (n16 >> 2) + 4 * (lane >> 5) + (n16 & 3)) * 384 + (6 * kb + w) * 16 + 8 * (g & 1)) * 2);
  const v4u xr = __builtin_amdgcn_raw_buffer_load_b128(rx, w < 6 ? ooff : OOB, 0, 0);
  const int mlo = opq(O_ML + (qt * 128 + (lane >> 5) * 16 + n16) * 16);
  const int s_ol = sopq(O_OF + (qt * 24 + 6 * kb + w) * 4096), rlo = opq((lane < 4 && lane != kb) ? O_RO + ((qt * 24 + 6 * kb + w) * 4 + lane) * 128 : OOB);
  v4u vf[8][3];
#define VF(i) vf[(i) & 7][(i) >> 3]
#define VLOAD(i) VF(i) = LD128(l16 + ((i) >> 3) * 1024, s_vl + ((i) & 7) * 24576)
  {
    lp qw = opq(lds + L_QS + (lane >> 5) * 12288 + 3 * w * 512 + (lane & 31) * 16);
    SB();
    __builtin_amdgcn_s_barrier();
    if (DLY2) __builtin_amdgcn_s_sleep(DLY2);
    HOP2_LOADS();
#pragma unroll
    for (int i = 0; i < NV0; ++i) VLOAD(i);
    SB();
#define QBAD() (wsum(sum4(sum4(sum4(-qr, qf[0]), qf[1]), qf[2])) + 3u * tag != 0u)
#pragma unroll
    for (int i = 0; i < 3; ++i) LDS128(qw, i * 512) = qf[i];
    if (UNLIKELY(QBAD())) {
      unsigned it = 0;
      do { RETRY(2); __builtin_amdgcn_s_sleep(3); CBAR(); HOP2_LOADS(); } while (QBAD() && ++it < SPIN_CAP);
#pragma unroll
      for (int i = 0; i < 3; ++i) LDS128(qw, i * 512) = qf[i];
    }
  }
  STAMP(13);
  BAR();
  float m[2]; unsigned vr, hv0 = 0;
  {
    v4u q[24];
#define QREAD(i) q[i] = LDS128(fl, 24576 + (((i) & 1) * 12 + ((i) >> 1)) * 1024)
#pragma unroll
    for (int i = 0; i < PD; ++i) QREAD(i);
    SB();
    f32x4 acc[2][2] = {};
#pragma unroll
    for (int i = 0; i < 24; ++i) {
      if (i + PD < 24) QREAD(i + PD);
      acc[i & 1][0] = mfma(xf[0][i >> 1], q[i], acc[i & 1][0]); acc[i & 1][1] = mfma(xf[1][i >> 1], q[i], acc[i & 1][1]);
      if ((i * NVS) / 24 != ((i + 1) * NVS) / 24) VLOAD(NV0 + (i * NVS) / 24);
      if (i >= 24 - NH) hv0 = opq(sum4(hv0, VF(i - (24 - NH))));
      SB();
    }
#pragma unroll
    for (int u = 0; u < 2; ++u) {
      float mx = max3(max3(acc[u][0][0], acc[u][0][1], acc[u][0][2]), acc[u][0][3], acc[u][1][0]);
      mx = max_g(max2(max3(mx, acc[u][1][1], acc[u][1][2]), acc[u][1][3]));
      *(LAS float*)(mxw + u * 512) = mx;
    }
    STAMP(14);
    BAR();
    f32x4 ma[2], mb[2];
#pragma unroll
    for (int u = 0; u < 2; ++u) { ma[u] = LDSF4(mxr, u * 512); mb[u] = LDSF4(mxr, u * 512 + 16); }
    vr = LD32(vro);
    SB();
#pragma unroll
    for (int u = 0; u < 2; ++u) {
      m[u] = max2(max3(max3(ma[u][0], ma[u][1], ma[u][2]), ma[u][3], mb[u][0]), max3(mb[u][1], mb[u][2], mb[u][3]));
      float p[8];
#pragma unroll
      for (int i = 0; i < 8; ++i) p[i] = __builtin_amdgcn_exp2f(acc[u][i >> 2][i & 3] - m[u]);
      LDS128(plw, u * 1024) = v4u{cvtpk(p[0], p[1]), cvtpk(p[2], p[3]), cvtpk(p[4], p[5]), cvtpk(p[6], p[7])};
      if (NV0 + NVS + u < NV2) VLOAD(NV0 + NVS + u);
      SB();
    }
#pragma unroll
    for (int i = NV0 + NVS + 2; i < NV2; ++i) VLOAD(i);
  }
  STAMP(15);
  BAR();
  v4u ml[4];
#define ML_LOADS() do { _Pragma("unroll") for (int k = 0; k < 4; ++k) ml[k] = LD128(mlo, ((kb + k) & 3) * 512); } while (0)
  {
    v4u pf[16];
    f32x4 acc[3][2] = {};
    v4u ov[3];
    unsigned hv[3];
#define OSTORE(et) do { const f32x4 v0 = acc[et][0], v1 = acc[et][1]; \
      auto r0 = __builtin_amdgcn_permlane16_swap(cvtpk(v0[0], v0[1]), cvtpk(v1[0], v1[1]), false, false); \
      auto r1 = __builtin_amdgcn_permlane16_swap(cvtpk(v0[2], v0[3]), cvtpk(v1[2], v1[3]), false, false); \
      ov[et] = v4u{r0[0], r1[0], r0[1], r1[1]}; ST128S(ov[et], oso, (et) * 4096); \
      if (own) LDS128(lds + 16384 + ((w & 1) * 3 + (et)) * 1024, ((g & 1) * 32 + (g >> 1) * 16 + n16) * 16) = ov[et]; } while (0)
#define ORECORD(et) ST32(scan63(sum4(0u, ov[et])) + tag, roo + (et) * 512)
#define VCHECK(et) do { if (UNLIKELY(wsum(hv[et] - ((lane >> 3) == (et) ? vr : 0u)) + 8u * tag != 0u)) { unsigned it = 0; \
        do { RETRY(3); __builtin_amdgcn_s_sleep(2); CBAR(); \
          _Pragma("unroll") for (int i = 0; i < 8; ++i) VLOAD(8 * (et) + i); \
          vr = LD32(vro); hv[et] = 0; \
          _Pragma("unroll") for (int i = 0; i < 8; ++i) hv[et] = sum4(hv[et], VF(8 * (et) + i)); \
        } while (wsum(hv[et] - ((lane >> 3) == (et) ? vr : 0u)) + 8u * tag != 0u && ++it < SPIN_CAP); \
        acc[et][0] = acc[et][1] = f32x4{0.f, 0.f, 0.f, 0.f}; \
        _Pragma("unroll") for (int i = 0; i < 16; ++i) acc[et][i & 1] = mfma(vf[i >> 1][et], pf[i], acc[et][i & 1]); } } while (0)
#pragma unroll
    for (int i = 0; i < 8; ++i) pf[i] = LDS128(pl, i * 1024);
    SB();
    hv[0] = hv0; hv[1] = hv[2] = 0;
#pragma unroll
    for (int i = 0; i < 16; ++i) {
      if (i + 8 < 16) pf[i + 8] = LDS128(pl, (i + 8) * 1024);
      if ((i & 1) && NV2 + (i >> 1) < 24) VLOAD(NV2 + (i >> 1));
      acc[0][i & 1] = mfma(vf[i >> 1][0], pf[i], acc[0][i & 1]);
      if ((i & 1) && (i >> 1) >= NH) hv[0] = opq(sum4(hv[0], vf[i >> 1][0]));
      if (!(i & 1)) hv[1] = opq(sum4(hv[1], vf[i >> 1][1]));
      SB();
    }
    if (w == 0) {
      const v4u ones = {0x3f803f80u, 0x3f803f80u, 0x3f803f80u, 0x3f803f80u};
      f32x4 ls[2] = {};
#pragma unroll
      for (int i = 0; i < 16; ++i) ls[i & 1] = mfma(ones, pf[i], ls[i & 1]);
      ST128((v4u{__float_as_uint(g ? m[1] : m[0]), tag, __float_as_uint(g ? ls[1][0] : ls[0][0]), tag}), mso);
    }
    VCHECK(0);
#pragma unroll
    for (int i = 0; i < 16; ++i) {
      acc[1][i & 1] = mfma(vf[i >> 1][1], pf[i], acc[1][i & 1]);
      if (i == 0) OSTORE(0);
      if (i == 6) ORECORD(0);
      if ((i & 3) == 3) SB();
    }
    VCHECK(1);
#pragma unroll
    for (int i = 0; i < 16; ++i) {
      acc[2][i & 1] = mfma(vf[i >> 1][2], pf[i], acc[2][i & 1]);
      if (i & 1) hv[2] = opq(sum4(hv[2], vf[i >> 1][2]));
      if (i == 0) OSTORE(1);
      if (i == 6) ORECORD(1);
      if (i == 12) ML_LOADS();
      if ((i & 3) == 3) SB();
    }
    VCHECK(2);
    STAMP(16);
    OSTORE(2);
    ORECORD(2);
  }
  STAMP(17);
  BAR();
  if (w < 6) {
    v4u pp[4]; unsigned orr;
#define MLOK() __all(((ml[0][1] ^ tag) | (ml[1][1] ^ tag) | (ml[2][1] ^ tag) | (ml[3][1] ^ tag) | (ml[0][3] ^ tag) | (ml[1][3] ^ tag) | (ml[2][3] ^ tag) | (ml[3][3] ^ tag)) == 0u)
#define HOP3_LOADS() do { _Pragma("unroll") for (int k = 1; k < 4; ++k) pp[k] = LD128(l16, s_ol + ((kb + k) & 3) * 1024); orr = LD32(rlo); } while (0)
#define OBAD() (wsum(sum4(sum4(sum4(-orr, pp[1]), pp[2]), pp[3])) + 3u * tag != 0u)
    pp[0] = LDS128(pl, 16384 + w * 1024);
    SB();
    if (DLY3) __builtin_amdgcn_s_sleep(DLY3);
    HOP3_LOADS();
    SB();
    STAMP(18);
    if (UNLIKELY(!MLOK())) {
      unsigned it = 0;
      do { RETRY(6); __builtin_amdgcn_s_sleep(2); CBAR(); ML_LOADS(); } while (!MLOK() && ++it < SPIN_CAP);
    }
    const float mm = max2(max2(__uint_as_float(ml[0][0]), __uint_as_float(ml[1][0])), max2(__uint_as_float(ml[2][0]), __uint_as_float(ml[3][0])));
    float wk[4], o[8];
#pragma unroll
    for (int k = 0; k < 4; ++k) wk[k] = __builtin_amdgcn_exp2f(__uint_as_float(ml[k][0]) - mm);
    const float inv = __builtin_amdgcn_rcpf((wk[0] * __uint_as_float(ml[0][2]) + wk[1] * __uint_as_float(ml[1][2])) + (wk[2] * __uint_as_float(ml[2][2]) + wk[3] * __uint_as_float(ml[3][2])));
#pragma unroll
    for (int k = 0; k < 4; ++k) wk[k] *= inv;
#pragma unroll
    for (int e = 0; e < 4; ++e) { o[2 * e] = opq(fmaf(wk[0], bflo(pp[0][e]), bflo(xr[e]))); o[2 * e + 1] = opq(fmaf(wk[0], bfhi(pp[0][e]), bfhi(xr[e]))); }
    SB();
    if (UNLIKELY(OBAD())) {
      unsigned it = 0;
      do { RETRY(5); __builtin_amdgcn_s_sleep(3); CBAR(); HOP3_LOADS(); } while (OBAD() && ++it < SPIN_CAP);
    }
    STAMP(1);
    v4u ov;
#pragma unroll
    for (int e = 0; e < 4; ++e) {
#pragma unroll
      for (int k = 1; k < 4; ++k) { o[2 * e] = fmaf(wk[k], bflo(pp[k][e]), o[2 * e]); o[2 * e + 1] = fmaf(wk[k], bfhi(pp[k][e]), o[2 * e + 1]); }
      ov[e] = cvtpk(o[2 * e], o[2 * e + 1]);
    }
    *(v4u*)((unsigned char*)out + ooff) = ov;
  } else if (UNLIKELY(bid == 255 && w == 7)) {
#pragma unroll 1
    for (unsigned it = 0; it < SPIN_CAP; ++it) {
      CBAR();
      unsigned bad = 0;
#pragma unroll
      for (int k = 0; k < 4; ++k) bad |= LD32(O_CL + (k * 64 + lane) * 128) ^ tag;
      if (__all(bad == 0)) break;
    }
    if (lane == 0) __hip_atomic_store((gu32*)(ws + O_EPOCH), tag, __ATOMIC_RELAXED, __HIP_MEMORY_SCOPE_AGENT);
  }
  STAMP(19);
  STFLUSH();
}

extern "C" void kernel_launch(void* const* d_in, const int* in_sizes, int n_in, void* d_out, int out_size, void* d_ws, size_t ws_size,
                              hipStream_t stream) {
  (void)hipFuncSetAttribute((const void*)dit_kernel, hipFuncAttributeMaxDynamicSharedMemorySize, L_TOTAL);
  dit_kernel<<<256, 512, L_TOTAL, stream>>>((const u16*)d_in[0], (const u16*)d_in[1], (const u16*)d_in[2], (const u16*)d_in[3],
                                            (const u16*)d_in[4], (unsigned char*)d_ws, (u16*)d_out);
}
```

```cpp
#include <hip/hip_runtime.h>
#include <hip/hip_bf16.h>

typedef short bf16x8 __attribute__((ext_vector_type(8)));
typedef short s16x4 __attribute__((ext_vector_type(4)));
typedef float f32x4 __attribute__((ext_vector_type(4)));
typedef float f32x2 __attribute__((ext_vector_type(2)));
typedef unsigned v4u __attribute__((ext_vector_type(4)));
typedef unsigned v2u __attribute__((ext_vector_type(2)));
typedef __bf16 bf2 __attribute__((ext_vector_type(2)));
typedef unsigned short u16;
#define DI __device__ __forceinline__
#define LAS __attribute__((address_space(3)))
#define GAS __attribute__((address_space(1)))
typedef GAS unsigned gu32;
typedef LAS unsigned char* lp;
#define STDECL
#define STAMP(i)
#define STFLUSH()
#define RETRY(k)
#define DLY0 30
#define DLY1 8
#define DLY2 0
#define PRE 1
#define HP 6
#define MP 2
#define TOUCH 1
#define CODEPF 18000
#define XS 0
#define DLY3 3
#define PD 4
#define SLP 2
#define ROT 0
#define NX1 16
#define NEARLY 0
#ifndef NV0
#define NV0 0
#endif
#define AUXO 16
#define NH 6
#ifndef NV2
#define NV2 18
#endif
#define HL 8
#define MC 9
#define DLY4 18
#define DLY5 8
#define BA 4
#define GATE 2
#define RL 4
#ifndef NVS
#define NVS 13
#endif

constexpr int O_EPOCH = 0;
constexpr int O_MOD = 4096;
constexpr int O_CL = 16384;
constexpr int O_MT = 131072;
constexpr int O_NF = O_MT + 589824;
constexpr int O_QF = O_NF + 1572864;
constexpr int O_VF = O_QF + 1572864;
constexpr int O_OF = O_VF + 1572864;
constexpr int O_ML = O_OF + 6291456;
constexpr int O_RM = O_ML + 131072;
constexpr int O_RN = O_RM + 1152 * 128;
constexpr int O_RQ = O_RN + 512 * 128;
constexpr int O_RV = O_RQ + 1536 * 128;
constexpr int O_RO = O_RV + 1536 * 128;
constexpr int WS_BYTES = O_RO + 6144 * 128;
constexpr int OOB = 0x40000000;

constexpr int L_P0 = 0;
constexpr int L_MS = 61440, L_QS = L_MS + 24576, L_CS = L_QS + 24576, L_MX = L_CS + 512, L_LS = L_MX + 1024, L_BN = L_LS + 1024;
constexpr int L_SCR = L_BN + 6144, L_TAG = L_SCR + 256;
constexpr int L_TOTAL = L_TAG + 32;

static_assert(NV2 >= 16 && NV0 + NVS <= NV2 && NV2 <= 24, "");
static_assert(BA < NX1 && MC + 3 < NX1 && MP < MC && HP < NX1 && NX1 >= 9 && NX1 <= 21, "");
constexpr int AUX_SC1 = 16;
constexpr unsigned SPIN_CAP = 1u << 16;

DI float bflo(unsigned v) { return __uint_as_float(v << 16); }
DI float bfhi(unsigned v) { return __uint_as_float(v & 0xffff0000u); }
DI unsigned cvtpk(float lo, float hi) {
  f32x2 x = {lo, hi};
  return __builtin_bit_cast(unsigned, __builtin_convertvector(x, bf2));
}
DI float bfround(float x) { return bflo(cvtpk(x, 0.f)); }
DI unsigned pkhi(unsigned lo, unsigned hi) { return __builtin_amdgcn_perm(hi, lo, 0x07060302u); }
DI unsigned tagged(float v, unsigned tag16) { return (cvtpk(0.f, v) & 0xffff0000u) | tag16; }
DI unsigned tchk(unsigned a, v4u v, unsigned tag) { return a | (v[0] ^ tag) | (v[1] ^ tag) | (v[2] ^ tag) | (v[3] ^ tag); }
DI unsigned sum4(unsigned a, v4u v) { return a + v[0] + v[1] + v[2] + v[3]; }
DI unsigned scan63(unsigned v) {
  v += __builtin_amdgcn_update_dpp(0u, v, 0x111, 0xf, 0xf, true);
  v += __builtin_amdgcn_update_dpp(0u, v, 0x112, 0xf, 0xf, true);
  v += __builtin_amdgcn_update_dpp(0u, v, 0x114, 0xf, 0xf, true);
  v += __builtin_amdgcn_update_dpp(0u, v, 0x118, 0xf, 0xf, true);
  v += __builtin_amdgcn_update_dpp(0u, v, 0x142, 0xa, 0xf, true);
  v += __builtin_amdgcn_update_dpp(0u, v, 0x143, 0xc, 0xf, true);
  return v;
}
DI float sum_g(float v);
DI float qsumf(float v) {
#define FSTEP(ctl) v += __uint_as_float(__builtin_amdgcn_update_dpp(0u, __float_as_uint(v), ctl, 0xf, 0xf, true))
  FSTEP(0x128); FSTEP(0x124);
#undef FSTEP
  return sum_g(v);
}
DI unsigned wsum(unsigned v) { return __builtin_amdgcn_readlane(scan63(v), 63); }
DI float max3(float a, float b, float c) { float r; asm("v_max3_f32 %0, %1, %2, %3" : "=v"(r) : "v"(a), "v"(b), "v"(c)); return r; }
DI float max2(float a, float b) { float r; asm("v_max_f32 %0, %1, %2" : "=v"(r) : "v"(a), "v"(b)); return r; }
DI float dot2(unsigned a, unsigned b, float c) { return __builtin_amdgcn_fdot2_f32_bf16(__builtin_bit_cast(bf2, a), __builtin_bit_cast(bf2, b), c, false); }
DI f32x4 mfma(v4u a, v4u b, f32x4 c) {
  return __builtin_amdgcn_mfma_f32_16x16x32_bf16(__builtin_bit_cast(bf16x8, a), __builtin_bit_cast(bf16x8, b), c, 0, 0, 0);
}
DI float sum_g(float v) {
  auto r = __builtin_amdgcn_permlane16_swap(__float_as_uint(v), __float_as_uint(v), false, false);
  v = __uint_as_float(r[0]) + __uint_as_float(r[1]);
  auto q = __builtin_amdgcn_permlane32_swap(__float_as_uint(v), __float_as_uint(v), false, false);
  return __uint_as_float(q[0]) + __uint_as_float(q[1]);
}
DI float max_g(float v) {
  auto r = __builtin_amdgcn_permlane16_swap(__float_as_uint(v), __float_as_uint(v), false, false);
  v = max2(__uint_as_float(r[0]), __uint_as_float(r[1]));
  auto q = __builtin_amdgcn_permlane32_swap(__float_as_uint(v), __float_as_uint(v), false, false);
  return max2(__uint_as_float(q[0]), __uint_as_float(q[1]));
}
DI int sopq(int v) { v = __builtin_amdgcn_readfirstlane(v); asm volatile("" : "+s"(v)); return v; }
template <typename T> DI T opq(T v) { asm volatile("" : "+v"(v)); return v; }
#define BAR() do { asm volatile("s_waitcnt lgkmcnt(0)" ::: "memory"); __builtin_amdgcn_s_barrier(); asm volatile("" ::: "memory"); } while (0)
#define VMWAIT(n) asm volatile("s_waitcnt vmcnt(%0)" :: "i"(n) : "memory")
#define SB() __builtin_amdgcn_sched_barrier(0)
#define CBAR() asm volatile("" ::: "memory")
#define LD128(off, so) __builtin_amdgcn_raw_buffer_load_b128(rws, (off), (so), AUX_SC1)
#define LD64(off) __builtin_amdgcn_raw_buffer_load_b64(rws, (off), 0, AUX_SC1)
#define LD32(off) __builtin_amdgcn_raw_buffer_load_b32(rws, (off), 0, AUX_SC1)
#define ST128(v, off) __builtin_amdgcn_raw_buffer_store_b128((v), rws, (off), 0, AUX_SC1)
#define ST128S(v, off, so) __builtin_amdgcn_raw_buffer_store_b128((v), rws, (off), (so), AUX_SC1)
#define ST64(v, off) __builtin_amdgcn_raw_buffer_store_b64((v), rws, (off), 0, AUX_SC1)
#define ST32(v, off) __builtin_amdgcn_raw_buffer_store_b32((v), rws, (off), 0, AUX_SC1)
#define ST16(v, off) __builtin_amdgcn_raw_buffer_store_b16((v), rws, (off), 0, AUX_SC1)
#define RST(sum, off) ST128((v4u{(sum) + tag, tag, tag, tag}), (off))
#define LDS128(p, off) (*(LAS v4u*)((p) + (off)))
#define LDSF4(p, off) (*(LAS f32x4*)((p) + (off)))
DI void dma(unsigned ldsaddr, int voff, __amdgpu_buffer_rsrc_t rs, int soff) {
  asm volatile("s_mov_b32 m0, %0\n\ts_nop 0\n\tbuffer_load_dwordx4 %1, %2, %3 offen lds" :: "s"(ldsaddr), "v"(voff), "s"(rs), "s"(soff) : "memory");
}

DI void touch(unsigned ldsaddr, int voff, __amdgpu_buffer_rsrc_t rs) {
  asm volatile("s_mov_b32 m0, %0\n\ts_nop 0\n\tbuffer_load_dword %1, %2, 0 offen lds" :: "s"(ldsaddr), "v"(voff), "s"(rs) : "memory");
}
#define UNLIKELY(c) __builtin_expect(!!(c), 0)

__global__ __launch_bounds__(512, 2) void dit_kernel(const u16* __restrict__ x, const u16* __restrict__ c, const u16* __restrict__ wqkv,
                                                     const u16* __restrict__ wo, const u16* __restrict__ wmod, unsigned char* ws,
                                                     u16* __restrict__ out) {
  extern __shared__ __attribute__((aligned(16))) unsigned char lds_[];
  lp lds = (lp)lds_;
  const unsigned ldsb = (unsigned)(size_t)lds;
  STDECL;
  STAMP(0);
  const unsigned long long pc0 = __builtin_amdgcn_s_getpc();
  const int tid = threadIdx.x, lane = tid & 63, w = __builtin_amdgcn_readfirstlane(tid >> 6);
  const int n16 = lane & 15, g = lane >> 4;
  const int bid = blockIdx.x, xg = bid & 7, r = bid >> 3;
  const int b = xg >> 2, ql = r & 7, kb = r >> 3;
  const int qt = b * 32 + (xg & 3) * 8 + ql;
  const int qt3 = b * 32 + (((xg & 3) + XS) & 3) * 8 + ql;
  const bool act1 = ql < 6;
  const int cb = (xg & 3) * 6 + (act1 ? ql : ql - 2);
  const __amdgpu_buffer_rsrc_t rws = __builtin_amdgcn_make_buffer_rsrc(ws, 0, WS_BYTES, 0x00020000);
  const __amdgpu_buffer_rsrc_t rx = __builtin_amdgcn_make_buffer_rsrc((void*)x, 0, 2048 * 768, 0x00020000);
  const int dch = 16 * ((lane & 3) ^ ((lane & 32) ? 3 : 0));
  const int rslot = (4 * n16 + (g ^ ((n16 & 8) ? 3 : 0))) * 16;
  const int l63 = lane < RL ? lane * 16 : OOB;

  const int mu = (((r - 24) >> 1) * 8 + xg) * 2 + (r & 1);
  const int role = r < 12 ? 0 : (r < 24 ? 1 : (mu < 36 ? 2 : 3));
  const int ru = role == 1 ? r - 12 : r;
  const int u12 = 6 * (xg & 1) + ru % 6, u8 = 2 * (xg >> 1) + ru / 6;
  unsigned ep;
  asm volatile("s_nop 4\n\tbuffer_load_dword %0, off, %1, 0 sc1" : "=v"(ep) : "s"(rws) : "memory");
  v4u nv[3];
  const int grp = bid * 2 + (w & 1);
  if (w < 6) {
    unsigned cv = 0;
    if (role == 2) cv = ((const unsigned*)c)[tid];
    if (role < 2) {
      const __amdgpu_buffer_rsrc_t rq = __builtin_amdgcn_make_buffer_rsrc((void*)wqkv, 0, 384 * 2304, 0x00020000);
      const int v = (lane >> 2) * 2304 + dch;
      const int s3 = 48 * u8 * 2304 + (role ? 1536 : 768) + 128 * w;
#pragma unroll
      for (int t = 0; t < 3; ++t) {
        dma(ldsb + L_P0 + (t * 12 + 2 * w) * 1024, v, rq, s3 + 16 * t * 2304);
        dma(ldsb + L_P0 + (t * 12 + 2 * w + 1) * 1024, v, rq, s3 + 16 * t * 2304 + 64);
      }
      if (role == 0) {
        const int s2 = 32 * u12 * 2304 + 128 * w;
#pragma unroll
        for (int t = 0; t < 2; ++t) {
          dma(ldsb + L_P0 + ((3 + t) * 12 + 2 * w) * 1024, v, rq, s2 + 16 * t * 2304);
          dma(ldsb + L_P0 + ((3 + t) * 12 + 2 * w + 1) * 1024, v, rq, s2 + 16 * t * 2304 + 64);
        }
      }
    }
    if (role == 1 || role == 2) {
      const int ld = role == 1 ? 768 : 4608;
      const __amdgpu_buffer_rsrc_t rc = __builtin_amdgcn_make_buffer_rsrc((void*)(role == 1 ? wo : wmod), 0, 384 * ld, 0x00020000);
      const int v = (lane >> 2) * ld + dch;
#pragma unroll
      for (int j = 0; j < 4; ++j) dma(ldsb + L_P0 + (36 + 4 * w + j) * 1024, v, rc, (role == 1 ? u12 : mu) * 64 + (4 * w + j) * 16 * ld);
    }
    if (role == 2) {
      const float f0 = bflo(cv), f1 = bfhi(cv);
      const int bb = tid >= 192, i2 = 2 * (tid - 192 * bb);
      *(LAS unsigned*)(lds + L_P0 + (i2 >> 5) * 1024 + (bb * 4 + ((i2 >> 3) & 3)) * 16 + (i2 & 7) * 2) =
          cvtpk(f0 * __builtin_amdgcn_rcpf(1.f + __expf(-f0)), f1 * __builtin_amdgcn_rcpf(1.f + __expf(-f1)));
    }
    VMWAIT(0);
  } else {
    __builtin_amdgcn_s_setprio(3);
    v4u xs[3];
    asm volatile("buffer_load_dwordx4 %0, %3, %4, 0 offen\n\tbuffer_load_dwordx4 %1, %3, %4, 0 offen offset:256\n\tbuffer_load_dwordx4 %2, %3, %4, 0 offen offset:512"
                 : "=&v"(xs[0]), "=&v"(xs[1]), "=&v"(xs[2]) : "v"((4 * grp + (lane & 3)) * 768 + (lane >> 2) * 16), "s"(rx) : "memory");
    {
      const unsigned scr = ldsb + L_SCR;
      const int dead = ((TOUCH & 1) && lane < 36) ? 0 : OOB;
      const __amdgpu_buffer_rsrc_t rq = __builtin_amdgcn_make_buffer_rsrc((void*)wqkv, 0, 384 * 2304, 0x00020000);
      touch(scr, dead + (w == 6 ? (96 * (xg >> 1) + 3 * r + lane / 12) * 2304 + 768 + (lane % 12) * 128 : (192 * (xg & 1) + 6 * r + lane / 6) * 2304 + (lane % 6) * 128), rq);
      const __amdgpu_buffer_rsrc_t r2 = __builtin_amdgcn_make_buffer_rsrc(w == 6 ? (void*)wo : (void*)(pc0 & ~127ull), 0, w == 6 ? 384 * 768 : CODEPF, 0x00020000);
      touch(scr, w == 6 ? dead + (12 * r + lane / 3) * 768 + 384 * (xg & 1) + (lane % 3) * 128 : (lane < 5 ? (r * 5 + lane) * 128 : OOB), r2);
    }
    if (w == 7) {
      const __amdgpu_buffer_rsrc_t r3 = __builtin_amdgcn_make_buffer_rsrc((void*)wmod, 0, 384 * 4608, 0x00020000);
      touch(ldsb + L_SCR, (lane < (xg < 2 ? 36 : 24) ? 0 : OOB) + (12 * r + lane % 12) * 4608 + ((lane / 12) * 8 + xg) * 128, r3);
    }
    else touch(ldsb + L_SCR, OOB, rx);
    asm volatile("s_waitcnt vmcnt(3)" : "+v"(xs[0]), "+v"(xs[1]), "+v"(xs[2]) :: "memory");
    float f[24], sm = 0.f, q = 0.f;
#pragma unroll
    for (int i = 0; i < 12; ++i) { f[2 * i] = bflo(xs[i >> 2][i & 3]); f[2 * i + 1] = bfhi(xs[i >> 2][i & 3]); sm += f[2 * i] + f[2 * i + 1]; }
    sm = qsumf(sm) * (1.f / 384.f);
#pragma unroll
    for (int i = 0; i < 24; ++i) { f[i] -= sm; q = fmaf(f[i], f[i], q); }
    const float rs = __builtin_amdgcn_rsqf(qsumf(q) * (1.f / 384.f) + 1e-5f);
#pragma unroll
    for (int i = 0; i < 3; ++i)
      nv[i] = opq(v4u{cvtpk(f[8 * i] * rs, f[8 * i + 1] * rs), cvtpk(f[8 * i + 2] * rs, f[8 * i + 3] * rs), cvtpk(f[8 * i + 4] * rs, f[8 * i + 5] * rs), cvtpk(f[8 * i + 6] * rs, f[8 * i + 7] * rs)});
  }
  asm volatile("" : "+v"(ep));
  const unsigned tag = __builtin_amdgcn_readfirstlane(ep) + 1u;
#define NSTORE() do { if (w >= 6) {     \
    const int no = O_NF + (((grp >> 3) * 2 + (grp & 1)) * 12 + g) * 1024 + (((lane >> 2) & 3) * 16 + ((grp >> 1) & 3) * 4 + (lane & 3)) * 16; \
    _Pragma("unroll") for (int i = 0; i < 3; ++i) ST128(nv[i], no + i * 4096); \
    RST(wsum(sum4(sum4(sum4(0u, nv[0]), nv[1]), nv[2])), l63 + O_RN + grp * 128); \
    __builtin_amdgcn_s_setprio(0); } } while (0)
  if (NEARLY || role >= 2) NSTORE();
  STAMP(2);
  BAR();
  STAMP(3);
  if (!NEARLY && role < 2) NSTORE();
  const int rg = b * 32 + kb * 8 + w;
  const int nro = lane < 8 ? O_RN + (rg * 8 + lane) * 128 : OOB;
  if (w < (role == 2 ? 2 : 6) && role != 3) {
    const int t3 = w >> 1, t2 = w & 1;
    f32x4 acc = {};
    v4u a[12], bq[12];
    if (role == 0) {
      lp ap = opq(lds + L_P0 + (3 + t2) * 12288 + rslot), bp = opq(lds + L_P0 + t3 * 12288 + rslot);
#pragma unroll
      for (int k = 0; k < 12; ++k) {
        a[k] = LDS128(ap, k * 1024); bq[k] = LDS128(bp, k * 1024);
        if (k >= 5) acc = mfma(a[k - 5], bq[k - 5], acc);
        SB();
      }
    } else {
      lp ap = opq(lds + L_P0 + (role == 2 ? ((n16 & 1) * 4 + g) * 16 : t3 * 12288 + rslot));
      lp tb = opq(lds + L_P0 + 36864 + (8 * g + (n16 >> 2)) * 64 + 16 * ((2 * t2 + ((n16 >> 1) & 1)) ^ ((g & 1) ? 3 : 0)) + 8 * (n16 & 1));
#pragma unroll
      for (int k = 0; k < 12; ++k) {
        a[k] = LDS128(ap, k * 1024);
        bq[k] = __builtin_bit_cast(v4u, __builtin_shufflevector(__builtin_amdgcn_ds_read_tr16_b64_v4i16((LAS s16x4*)(tb + k * 2048)),
                                                                __builtin_amdgcn_ds_read_tr16_b64_v4i16((LAS s16x4*)(tb + k * 2048 + 256)), 0, 1, 2, 3, 4, 5, 6, 7));
        if (k >= 5) acc = mfma(a[k - 5], bq[k - 5], acc);
        SB();
      }
    }
#pragma unroll
    for (int k = 7; k < 12; ++k) acc = mfma(a[k], bq[k], acc);
    if (role != 2) {
      const int blk = role * 576 + (role == 0 ? (3 * u8 + t3) * 24 + 2 * u12 + t2 : (2 * u12 + t2) * 24 + 3 * u8 + t3);
      const unsigned o0 = cvtpk(acc[0], acc[1]), o1 = cvtpk(acc[2], acc[3]);
      auto r0 = __builtin_amdgcn_permlane16_swap(o0, o0, false, false);
      auto r1 = __builtin_amdgcn_permlane16_swap(o1, o1, false, false);
      ST128((v4u{r0[0], r1[0], r0[1], r1[1]}), ((g & 1) ? OOB : 0) + O_MT + blk * 512 + ((g >> 1) * 16 + n16) * 16);
      RST(wsum(o0 + o1), l63 + O_RM + blk * 128);
    } else if (g < 2) {
      float v = bfround(g ? acc[1] : acc[0]);
      if (mu >= 12 && mu < 24) v += 1.f;
      ST32(tagged(v, tag & 0xffffu), O_MOD + (g * 1152 + 32 * mu + 16 * t2 + n16) * 4);
    }
  }
  STAMP(4);
  if (role == 3) __builtin_amdgcn_s_sleep(DLY0);
  else if (role == 2) { if (w >= 2) __builtin_amdgcn_s_sleep(DLY4); }
  else if (w >= 6) __builtin_amdgcn_s_sleep(DLY1);
  v2u nr = LD64(nro);
  SB();
  const int l16 = opq(lane * 16);
  const int s_n = O_NF + rg * 24576;
  const int rot = ROT ? (ql * 3) >> 1 : 0;
  const int l16b = opq(lane * 16 + 12288);
  v4u xf[2][12];
#define XF(i) xf[(i) & 1][(i) >> 1]
#define XLOAD(i) XF(i) = LD128(((i) & 1) ? l16b : l16, s_n + (((i) >> 1) + rot - (((i) >> 1) + rot >= 12 ? 12 : 0)) * 1024)
  lp fl = opq(lds + L_MS + lane * 16);
  v4u qf[3]; unsigned qr;
  const int qro = opq(lane < 3 ? O_RQ + (qt * 24 + 3 * w + lane) * 128 : OOB);
  const int s_ql = sopq(O_QF + (qt * 24 + 3 * w) * 1024);
#define HOP2_LOADS() do { _Pragma("unroll") for (int i = 0; i < 3; ++i) qf[i] = LD128(l16 + i * 1024, s_ql); qr = LD32(qro); } while (0)
  {
    const int s0 = 3 * (w & 3);
    const int mo = opq(O_MOD + (b * 1152 + (lane < 24 ? 384 + 32 * s0 + 4 * lane : (lane < 48 ? 32 * s0 + 4 * (lane - 24) : (w < 4 ? 384 : 768) + 16 * cb + 4 * (lane & 3)))) * 4);
    const int fb = (w >> 2) * 576 + cb * 24 + 2 * s0;
    const int ro = opq(lane < 6 ? O_RM + (fb + lane) * 128 : OOB);
    const int s_f = sopq(O_MT + fb * 512);
    const int blk1 = rg * 24 + cb, dead1 = act1 ? 0 : OOB;
    const int qso = opq(dead1 + O_QF + blk1 * 1024 + ((g & 1) * 32 + (g >> 1) * 16 + n16) * 16);
    const int vso = opq(dead1 + O_VF + blk1 * 1024 + lane * 16);
    const int rso = opq((dead1 | l63) + O_RQ + blk1 * 128);
    const int rvo = opq((dead1 | l63) + O_RV + (((b * 4 + kb) * 24 + cb) * 8 + w) * 128);
    lp cg = opq(lds + L_CS + g * 16), cn = opq(lds + L_CS + 256 + n16 * 4), csw = opq(lds + L_CS + w * 64 + n16 * 4);
    lp bn = lds + L_BN + w * 768, bg = opq(bn + g * 32), msw[3];
#pragma unroll
    for (int i = 0; i < 3; ++i) { int sl = s0 + i - rot; if (sl < 0) sl += 12; msw[i] = opq(fl + (w >> 2) * 12288 + sl * 1024); }
    SB();
    if (UNLIKELY(!__all(lane >= 8 || nr[1] == tag))) {
      unsigned it = 0;
      do { RETRY(0); __builtin_amdgcn_s_sleep(2); CBAR(); nr = LD64(nro); } while (!__all(lane >= 8 || nr[1] == tag) && ++it < SPIN_CAP);
    }
    if (GATE == 2) __builtin_amdgcn_s_barrier();
    STAMP(5);
    unsigned hx = 0;
    v4u mf[3], mv4; unsigned mr;
    f32x4 sa[3], sb[3]; v4u tf[3]; float oc_;
#define MODOK() __all((tchk(0u, mv4, tag) & 0xffffu) == 0u)
#define HOP1_LOADS() do { _Pragma("unroll") for (int i = 0; i < 3; ++i) mf[i] = LD128(l16 + i * 1024, s_f); mr = LD32(ro); } while (0)
#pragma unroll
    for (int i = 0; i < NX1; ++i) {
      XLOAD(i);
      if (i >= HL) hx = sum4(hx, XF(i - HL));
      if (i == MP) mv4 = LD128(mo, 0);
      if (i == HP) HOP1_LOADS();
      if (i == NX1 - 1) nr = LD64(nro);
      if (i == BA && w >= 4) __builtin_amdgcn_s_barrier();
      if (i == MC) {
        if (UNLIKELY(!MODOK())) {
          unsigned it = 0;
          do { RETRY(1); __builtin_amdgcn_s_sleep(8); CBAR(); mv4 = LD128(mo, 0); } while (!MODOK() && ++it < SPIN_CAP);
        }
        if (lane >= 24 && lane < 48) *(LAS v2u*)(bn + 512 + (lane - 24) * 8) = v2u{pkhi(mv4[0], mv4[1]), pkhi(mv4[2], mv4[3])};
        else LDS128(bn, (lane < 24 ? lane : (lane & 3) + 24) * 16) = mv4 & 0xffff0000u;
        CBAR();
#pragma unroll
        for (int k = 0; k < 3; ++k) { sa[k] = LDSF4(bg, 128 * k); sb[k] = LDSF4(bg, 128 * k + 16); tf[k] = LDS128(bn, 512 + 64 * k + 16 * g); }
        oc_ = *(LAS float*)(bn + 384 + n16 * 4);
      }
      if (i == MC + 1) { if (w < 4) oc_ *= 0.05103103630798288f * 1.4426950408889634f; sa[0] = opq(sa[0] * oc_); sb[0] = opq(sb[0] * oc_); }
      if (i == MC + 2) { sa[1] = opq(sa[1] * oc_); sb[1] = opq(sb[1] * oc_); }
      if (i == MC + 3) { sa[2] = opq(sa[2] * oc_); sb[2] = opq(sb[2] * oc_); }
      asm volatile("" : "+v"(hx));
      SB();
    }
    STAMP(6);
#define FBAD() (wsum(sum4(sum4(sum4(-mr, mf[0]), mf[1]), mf[2])) + 6u * tag != 0u)
    if (UNLIKELY(FBAD())) {
      unsigned it = 0;
      do { RETRY(9); __builtin_amdgcn_s_sleep(6); CBAR(); HOP1_LOADS(); } while (FBAD() && ++it < SPIN_CAP);
    }
    STAMP(7);
    {
      f32x4 au = {};
#pragma unroll
      for (int i = 0; i < 3; ++i) {
        au = mfma(tf[i], mf[i], au);
        const v4u o = {cvtpk(bflo(mf[i][0]) * sa[i][0], bfhi(mf[i][0]) * sa[i][1]), cvtpk(bflo(mf[i][1]) * sa[i][2], bfhi(mf[i][1]) * sa[i][3]),
                       cvtpk(bflo(mf[i][2]) * sb[i][0], bfhi(mf[i][2]) * sb[i][1]), cvtpk(bflo(mf[i][3]) * sb[i][2], bfhi(mf[i][3]) * sb[i][3])};
        LDS128(msw[i], 0) = o;
        if (NX1 + i < 24) XLOAD(NX1 + i);
        hx = opq(sum4(hx, XF(NX1 - HL + i)));
        SB();
      }
      *(LAS float*)(csw) = au[0] * oc_;
    }
    STAMP(9);
    __builtin_amdgcn_s_setprio(2);
    BAR();
    v4u a[24];
    f32x4 uu[4]; float zz[4], zs;
#define AOP(k) do { if ((k) + PD < ((k) < 12 ? 12 : 24)) a[(k) + PD] = LDS128(fl, ((k) + PD) * 1024); \
      if ((k) < 12) { aq[0] = mfma(a[k], xf[0][(k) % 12], aq[0]); aq[1] = mfma(a[k], xf[1][(k) % 12], aq[1]); } \
      else { av[0] = mfma(xf[0][(k) % 12], a[k], av[0]); av[1] = mfma(xf[1][(k) % 12], a[k], av[1]); } } while (0)
#pragma unroll
    for (int k = 0; k < PD; ++k) a[k] = LDS128(fl, k * 1024);
    SB();
    f32x4 aq[2] = {}, av[2] = {};
#pragma unroll
    for (int k = 0; k < 12; ++k) {
      if (NX1 + 3 + k < 24) XLOAD(NX1 + 3 + k);
      if (k >= 2 && k < 6) uu[k - 2] = LDSF4(cg, (k - 2) * 64);
      AOP(k);
      if (2 * k >= NX1 - HL + 3) hx = sum4(hx, XF(2 * k));
      if (2 * k + 1 >= NX1 - HL + 3) hx = sum4(hx, XF(2 * k + 1));
      if (k == 9) { uu[0] = uu[0] + uu[1]; uu[2] = uu[2] + uu[3]; }
      if (k == 10) uu[0] = uu[0] + uu[2];
      asm volatile("" : "+v"(hx));
      SB();
    }
    if (UNLIKELY(wsum(hx - nr[0]) + 8u * tag != 0u)) {
      unsigned it = 0;
      do { RETRY(4); __builtin_amdgcn_s_sleep(4); CBAR();
        hx = -LD32(nro);
#pragma unroll
        for (int i = 0; i < 24; ++i) XLOAD(i);
#pragma unroll
        for (int i = 0; i < 24; ++i) hx = sum4(hx, XF(i));
      } while (wsum(hx) + 8u * tag != 0u && ++it < SPIN_CAP);
      aq[0] = aq[1] = f32x4{0.f, 0.f, 0.f, 0.f};
#pragma unroll
      for (int s = 0; s < 12; ++s) {
        const v4u a0 = LDS128(fl, s * 1024);
        aq[0] = mfma(a0, xf[0][s], aq[0]); aq[1] = mfma(a0, xf[1][s], aq[1]);
      }
    }
    {
      const f32x4 v0 = aq[0] + uu[0], v1 = aq[1] + uu[0];
      auto r0 = __builtin_amdgcn_permlane16_swap(cvtpk(v0[0], v0[1]), cvtpk(v1[0], v1[1]), false, false);
      auto r1 = __builtin_amdgcn_permlane16_swap(cvtpk(v0[2], v0[3]), cvtpk(v1[2], v1[3]), false, false);
      const v4u ov = {r0[0], r1[0], r0[1], r1[1]};
      ST128(ov, qso);
      RST(wsum(sum4(0u, ov)), rso);
      __builtin_amdgcn_s_setprio(0);
      STAMP(12);
      if (w < 4) BAR();
      CBAR();
#pragma unroll
      for (int k = 12; k < 12 + PD; ++k) a[k] = LDS128(fl, k * 1024);
      SB();
#pragma unroll
      for (int k = 12; k < 24; ++k) {
        if (k >= 14 && k < 18) zz[k - 14] = *(LAS float*)(cn + (k - 14) * 64);
        AOP(k);
        if (k == 21) zs = (zz[0] + zz[1]) + (zz[2] + zz[3]);
        SB();
      }
      const f32x4 w0 = av[0] + zs, w1 = av[1] + zs;
      const v4u o = {cvtpk(w0[0], w0[1]), cvtpk(w0[2], w0[3]), cvtpk(w1[0], w1[1]), cvtpk(w1[2], w1[3])};
      ST128(o, vso);
      RST(wsum(sum4(0u, o)), rvo);
    }
    STAMP(10);
    SB();
  }
  STAMP(11);
  if (tid == 448) __hip_atomic_store((gu32*)(ws + O_CL + bid * 128), tag, __ATOMIC_RELAXED, __HIP_MEMORY_SCOPE_AGENT);
  const int vro = opq(lane < 24 ? O_RV + (((b * 4 + kb) * 8 + w) * 24 + lane) * 128 : OOB);
  const int mso = opq((w == 0 && g < 2) ? O_ML + ((qt * 4 + kb) * 32 + lane) * 16 : OOB);
  const int s_vl = sopq(O_VF + ((b * 32 + 8 * kb) * 24 + 3 * w) * 1024);
  const bool own = (w >> 1) == kb;
  const int oso = opq((own ? OOB : 0) + O_OF + ((qt * 24 + 3 * w) * 4 + kb) * 1024 + ((g & 1) * 32 + (g >> 1) * 16 + n16) * 16);
  const int roo = opq(((own ? OOB : 0) | l63) + O_RO + ((qt * 24 + 3 * w) * 4 + kb) * 128);
  lp mxr = opq(lds + L_MX + n16 * 32), mxw = opq(lds + L_MX + n16 * 32 + w * 4);
  lp plw = opq(lds + lane * 16 + w * 2048);
  lp pl = opq(lds + lane * 16);
  const int ooff = opq(((qt * 32 + 8 * (n16 >> 2) + 4 * (lane >> 5) + (n16 & 3)) * 384 + (6 * kb + w) * 16 + 8 * (g & 1)) * 2);
  const v4u xr = __builtin_amdgcn_raw_buffer_load_b128(rx, w < 6 ? ooff : OOB, 0, 0);
  const int mlo = opq(O_ML + (qt * 128 + (lane >> 5) * 16 + n16) * 16);
  const int s_ol = sopq(O_OF + (qt * 24 + 6 * kb + w) * 4096), rlo = opq((lane < 4 && lane != kb) ? O_RO + ((qt * 24 + 6 * kb + w) * 4 + lane) * 128 : OOB);
  v4u vf[8][3];
#define VF(i) vf[(i) & 7][(i) >> 3]
#define VLOAD(i) VF(i) = LD128(l16 + ((i) >> 3) * 1024, s_vl + ((i) & 7) * 24576)
  {
    lp qw = opq(lds + L_QS + (lane >> 5) * 12288 + 3 * w * 512 + (lane & 31) * 16);
    SB();
    __builtin_amdgcn_s_barrier();
    if (DLY2) __builtin_amdgcn_s_sleep(DLY2);
    HOP2_LOADS();
#pragma unroll
    for (int i = 0; i < NV0; ++i) VLOAD(i);
    SB();
#define QBAD() (wsum(sum4(sum4(sum4(-qr, qf[0]), qf[1]), qf[2])) + 3u * tag != 0u)
#pragma unroll
    for (int i = 0; i < 3; ++i) LDS128(qw, i * 512) = qf[i];
    if (UNLIKELY(QBAD())) {
      unsigned it = 0;
      do { RETRY(2); __builtin_amdgcn_s_sleep(3); CBAR(); HOP2_LOADS(); } while (QBAD() && ++it < SPIN_CAP);
#pragma unroll
      for (int i = 0; i < 3; ++i) LDS128(qw, i * 512) = qf[i];
    }
  }
  STAMP(13);
  BAR();
  float m[2]; unsigned vr, hv0 = 0;
  {
    v4u q[24];
#define QREAD(i) q[i] = LDS128(fl, 24576 + (((i) & 1) * 12 + ((i) >> 1)) * 1024)
#pragma unroll
    for (int i = 0; i < PD; ++i) QREAD(i);
    SB();
    f32x4 acc[2][2] = {};
#pragma unroll
    for (int i = 0; i < 24; ++i) {
      if (i + PD < 24) QREAD(i + PD);
      acc[i & 1][0] = mfma(xf[0][i >> 1], q[i], acc[i & 1][0]); acc[i & 1][1] = mfma(xf[1][i >> 1], q[i], acc[i & 1][1]);
      if ((i * NVS) / 24 != ((i + 1) * NVS) / 24) VLOAD(NV0 + (i * NVS) / 24);
      if (i >= 24 - NH) hv0 = opq(sum4(hv0, VF(i - (24 - NH))));
      SB();
    }
#pragma unroll
    for (int u = 0; u < 2; ++u) {
      float mx = max3(max3(acc[u][0][0], acc[u][0][1], acc[u][0][2]), acc[u][0][3], acc[u][1][0]);
      mx = max_g(max2(max3(mx, acc[u][1][1], acc[u][1][2]), acc[u][1][3]));
      *(LAS float*)(mxw + u * 512) = mx;
    }
    STAMP(14);
    BAR();
    f32x4 ma[2], mb[2];
#pragma unroll
    for (int u = 0; u < 2; ++u) { ma[u] = LDSF4(mxr, u * 512); mb[u] = LDSF4(mxr, u * 512 + 16); }
    vr = LD32(vro);
    SB();
#pragma unroll
    for (int u = 0; u < 2; ++u) {
      m[u] = max2(max3(max3(ma[u][0], ma[u][1], ma[u][2]), ma[u][3], mb[u][0]), max3(mb[u][1], mb[u][2], mb[u][3]));
      float p[8];
#pragma unroll
      for (int i = 0; i < 8; ++i) p[i] = __builtin_amdgcn_exp2f(acc[u][i >> 2][i & 3] - m[u]);
      LDS128(plw, u * 1024) = v4u{cvtpk(p[0], p[1]), cvtpk(p[2], p[3]), cvtpk(p[4], p[5]), cvtpk(p[6], p[7])};
      if (NV0 + NVS + u < NV2) VLOAD(NV0 + NVS + u);
      SB();
    }
#pragma unroll
    for (int i = NV0 + NVS + 2; i < NV2; ++i) VLOAD(i);
  }
  STAMP(15);
  BAR();
  v4u ml[4];
#define ML_LOADS() do { _Pragma("unroll") for (int k = 0; k < 4; ++k) ml[k] = LD128(mlo, ((kb + k) & 3) * 512); } while (0)
  {
    v4u pf[16];
    f32x4 acc[3][2] = {};
    v4u ov[3];
    unsigned hv[3];
#define OSTORE(et) do { const f32x4 v0 = acc[et][0], v1 = acc[et][1]; \
      auto r0 = __builtin_amdgcn_permlane16_swap(cvtpk(v0[0], v0[1]), cvtpk(v1[0], v1[1]), false, false); \
      auto r1 = __builtin_amdgcn_permlane16_swap(cvtpk(v0[2], v0[3]), cvtpk(v1[2], v1[3]), false, false); \
      ov[et] = v4u{r0[0], r1[0], r0[1], r1[1]}; ST128S(ov[et], oso, (et) * 4096); \
      if (own) LDS128(lds + 16384 + ((w & 1) * 3 + (et)) * 1024, ((g & 1) * 32 + (g >> 1) * 16 + n16) * 16) = ov[et]; } while (0)
#define ORECORD(et) RST(wsum(sum4(0u, ov[et])), roo + (et) * 512)
#define VCHECK(et) do { if (UNLIKELY(wsum(hv[et] - ((lane >> 3) == (et) ? vr : 0u)) + 8u * tag != 0u)) { unsigned it = 0; \
        do { RETRY(3); __builtin_amdgcn_s_sleep(2); CBAR(); \
          _Pragma("unroll") for (int i = 0; i < 8; ++i) VLOAD(8 * (et) + i); \
          vr = LD32(vro); hv[et] = 0; \
          _Pragma("unroll") for (int i = 0; i < 8; ++i) hv[et] = sum4(hv[et], VF(8 * (et) + i)); \
        } while (wsum(hv[et] - ((lane >> 3) == (et) ? vr : 0u)) + 8u * tag != 0u && ++it < SPIN_CAP); \
        acc[et][0] = acc[et][1] = f32x4{0.f, 0.f, 0.f, 0.f}; \
        _Pragma("unroll") for (int i = 0; i < 16; ++i) acc[et][i & 1] = mfma(vf[i >> 1][et], pf[i], acc[et][i & 1]); } } while (0)
#pragma unroll
    for (int i = 0; i < 8; ++i) pf[i] = LDS128(pl, i * 1024);
    SB();
    hv[0] = hv0; hv[1] = hv[2] = 0;
#pragma unroll
    for (int i = 0; i < 16; ++i) {
      if (i + 8 < 16) pf[i + 8] = LDS128(pl, (i + 8) * 1024);
      if ((i & 1) && NV2 + (i >> 1) < 24) VLOAD(NV2 + (i >> 1));
      acc[0][i & 1] = mfma(vf[i >> 1][0], pf[i], acc[0][i & 1]);
      if ((i & 1) && (i >> 1) >= NH) hv[0] = opq(sum4(hv[0], vf[i >> 1][0]));
      if (!(i & 1)) hv[1] = opq(sum4(hv[1], vf[i >> 1][1]));
      SB();
    }
    if (w == 0) {
      const v4u ones = {0x3f803f80u, 0x3f803f80u, 0x3f803f80u, 0x3f803f80u};
      f32x4 ls[2] = {};
#pragma unroll
      for (int i = 0; i < 16; ++i) ls[i & 1] = mfma(ones, pf[i], ls[i & 1]);
      ST128((v4u{__float_as_uint(g ? m[1] : m[0]), tag, __float_as_uint(g ? ls[1][0] : ls[0][0]), tag}), mso);
    }
    VCHECK(0);
#pragma unroll
    for (int i = 0; i < 16; ++i) {
      acc[1][i & 1] = mfma(vf[i >> 1][1], pf[i], acc[1][i & 1]);
      if (i == 0) OSTORE(0);
      if (i == 6) ORECORD(0);
      if ((i & 3) == 3) SB();
    }
    VCHECK(1);
#pragma unroll
    for (int i = 0; i < 16; ++i) {
      acc[2][i & 1] = mfma(vf[i >> 1][2], pf[i], acc[2][i & 1]);
      if (i & 1) hv[2] = opq(sum4(hv[2], vf[i >> 1][2]));
      if (i == 0) OSTORE(1);
      if (i == 6) ORECORD(1);
      if (i == 12) ML_LOADS();
      if ((i & 3) == 3) SB();
    }
    VCHECK(2);
    STAMP(16);
    OSTORE(2);
    ORECORD(2);
  }
  STAMP(17);
  BAR();
  if (w < 6) {
    v4u pp[4]; unsigned orr;
#define MLOK() __all(((ml[0][1] ^ tag) | (ml[1][1] ^ tag) | (ml[2][1] ^ tag) | (ml[3][1] ^ tag) | (ml[0][3] ^ tag) | (ml[1][3] ^ tag) | (ml[2][3] ^ tag) | (ml[3][3] ^ tag)) == 0u)
#define HOP3_LOADS() do { _Pragma("unroll") for (int k = 1; k < 4; ++k) pp[k] = LD128(l16, s_ol + ((kb + k) & 3) * 1024); orr = LD32(rlo); } while (0)
#define OBAD() (wsum(sum4(sum4(sum4(-orr, pp[1]), pp[2]), pp[3])) + 3u * tag != 0u)
    pp[0] = LDS128(pl, 16384 + w * 1024);
    SB();
    if (DLY3) __builtin_amdgcn_s_sleep(DLY3);
    HOP3_LOADS();
    SB();
    STAMP(18);
    if (UNLIKELY(!MLOK())) {
      unsigned it = 0;
      do { RETRY(6); __builtin_amdgcn_s_sleep(2); CBAR(); ML_LOADS(); } while (!MLOK() && ++it < SPIN_CAP);
    }
    const float mm = max2(max2(__uint_as_float(ml[0][0]), __uint_as_float(ml[1][0])), max2(__uint_as_float(ml[2][0]), __uint_as_float(ml[3][0])));
    float wk[4], o[8];
#pragma unroll
    for (int k = 0; k < 4; ++k) wk[k] = __builtin_amdgcn_exp2f(__uint_as_float(ml[k][0]) - mm);
    const float inv = __builtin_amdgcn_rcpf((wk[0] * __uint_as_float(ml[0][2]) + wk[1] * __uint_as_float(ml[1][2])) + (wk[2] * __uint_as_float(ml[2][2]) + wk[3] * __uint_as_float(ml[3][2])));
#pragma unroll
    for (int k = 0; k < 4; ++k) wk[k] *= inv;
#pragma unroll
    for (int e = 0; e < 4; ++e) { o[2 * e] = opq(fmaf(wk[0], bflo(pp[0][e]), bflo(xr[e]))); o[2 * e + 1] = opq(fmaf(wk[0], bfhi(pp[0][e]), bfhi(xr[e]))); }
    SB();
    if (UNLIKELY(OBAD())) {
      unsigned it = 0;
      do { RETRY(5); __builtin_amdgcn_s_sleep(3); CBAR(); HOP3_LOADS(); } while (OBAD() && ++it < SPIN_CAP);
    }
    STAMP(1);
    v4u ov;
#pragma unroll
    for (int e = 0; e < 4; ++e) {
#pragma unroll
      for (int k = 1; k < 4; ++k) { o[2 * e] = fmaf(wk[k], bflo(pp[k][e]), o[2 * e]); o[2 * e + 1] = fmaf(wk[k], bfhi(pp[k][e]), o[2 * e + 1]); }
      ov[e] = cvtpk(o[2 * e], o[2 * e + 1]);
    }
    *(v4u*)((unsigned char*)out + ooff) = ov;
  } else if (UNLIKELY(bid == 255 && w == 7)) {
#pragma unroll 1
    for (unsigned it = 0; it < SPIN_CAP; ++it) {
      CBAR();
      unsigned bad = 0;
#pragma unroll
      for (int k = 0; k < 4; ++k) bad |= LD32(O_CL + (k * 64 + lane) * 128) ^ tag;
      if (__all(bad == 0)) break;
    }
    if (lane == 0) __hip_atomic_store((gu32*)(ws + O_EPOCH), tag, __ATOMIC_RELAXED, __HIP_MEMORY_SCOPE_AGENT);
  }
  STAMP(19);
  STFLUSH();
}

extern "C" void kernel_launch(void* const* d_in, const int* in_sizes, int n_in, void* d_out, int out_size, void* d_ws, size_t ws_size,
                              hipStream_t stream) {
  (void)hipFuncSetAttribute((const void*)dit_kernel, hipFuncAttributeMaxDynamicSharedMemorySize, L_TOTAL);
  dit_kernel<<<256, 512, L_TOTAL, stream>>>((const u16*)d_in[0], (const u16*)d_in[1], (const u16*)d_in[2], (const u16*)d_in[3],
                                            (const u16*)d_in[4], (unsigned char*)d_ws, (u16*)d_out);
}
```

```cpp
#include <hip/hip_runtime.h>
#include <hip/hip_bf16.h>

typedef short bf16x8 __attribute__((ext_vector_type(8)));
typedef short s16x4 __attribute__((ext_vector_type(4)));
typedef float f32x4 __attribute__((ext_vector_type(4)));
typedef float f32x2 __attribute__((ext_vector_type(2)));
typedef unsigned v4u __attribute__((ext_vector_type(4)));
typedef unsigned v2u __attribute__((ext_vector_type(2)));
typedef __bf16 bf2 __attribute__((ext_vector_type(2)));
typedef unsigned short u16;
#define DI __device__ __forceinline__
#define LAS __attribute__((address_space(3)))
#define GAS __attribute__((address_space(1)))
typedef GAS unsigned gu32;
typedef LAS unsigned char* lp;
#define STDECL
#define STAMP(i)
#define STFLUSH()
#define RETRY(k)
#define DLY0 30
#define DLY1 0
#define DLY2 4
#define PRE 1
#define HP 6
#define MP 2
#define TOUCH 1
#define CODEPF 18000
#define XS 0
#define DLY3 6
#define PD 4
#define SLP 2
#define ROT 0
#define NX1 16
#define NEARLY 0
#ifndef NV0
#define NV0 0
#endif
#define AUXO 16
#define NH 6
#ifndef NV2
#define NV2 18
#endif
#define HL 8
#define MC 9
#define DLY4 18
#define DLY5 8
#define BA 4
#define GATE 2
#define RL 8
#ifndef NVS
#define NVS 13
#endif

constexpr int O_EPOCH = 0;
constexpr int O_MOD = 4096;
constexpr int O_CL = 16384;
constexpr int O_MT = 131072;
constexpr int O_NF = O_MT + 589824;
constexpr int O_QF = O_NF + 1572864;
constexpr int O_VF = O_QF + 1572864;
constexpr int O_OF = O_VF + 1572864;
constexpr int O_ML = O_OF + 6291456;
constexpr int O_RM = O_ML + 131072;
constexpr int O_RN = O_RM + 1152 * 128;
constexpr int O_RQ = O_RN + 512 * 128;
constexpr int O_RV = O_RQ + 1536 * 128;
constexpr int O_RO = O_RV + 1536 * 128;
constexpr int WS_BYTES = O_RO + 6144 * 128;
constexpr int OOB = 0x40000000;

constexpr int L_P0 = 0;
constexpr int L_MS = 61440, L_QS = L_MS + 24576, L_CS = L_QS + 24576, L_MX = L_CS + 512, L_LS = L_MX + 1024, L_BN = L_LS + 1024;
constexpr int L_SCR = L_BN + 6144, L_TAG = L_SCR + 256;
constexpr int L_TOTAL = L_TAG + 32;

static_assert(NV2 >= 16 && NV0 + NVS <= NV2 && NV2 <= 24, "");
static_assert(BA < NX1 && MC + 3 < NX1 && MP < MC && HP < NX1 && NX1 >= 9 && NX1 <= 21, "");
constexpr int AUX_SC1 = 16;
constexpr unsigned SPIN_CAP = 1u << 16;

DI float bflo(unsigned v) { return __uint_as_float(v << 16); }
DI float bfhi(unsigned v) { return __uint_as_float(v & 0xffff0000u); }
DI unsigned cvtpk(float lo, float hi) {
  f32x2 x = {lo, hi};
  return __builtin_bit_cast(unsigned, __builtin_convertvector(x, bf2));
}
DI float bfround(float x) { return bflo(cvtpk(x, 0.f)); }
DI unsigned pkhi(unsigned lo, unsigned hi) { return __builtin_amdgcn_perm(hi, lo, 0x07060302u); }
DI unsigned tagged(float v, unsigned tag16) { return (cvtpk(0.f, v) & 0xffff0000u) | tag16; }
DI unsigned tchk(unsigned a, v4u v, unsigned tag) { return a | (v[0] ^ tag) | (v[1] ^ tag) | (v[2] ^ tag) | (v[3] ^ tag); }
DI unsigned sum4(unsigned a, v4u v) { return a + v[0] + v[1] + v[2] + v[3]; }
DI unsigned scan63(unsigned v) {
  v += __builtin_amdgcn_update_dpp(0u, v, 0x111, 0xf, 0xf, true);
  v += __builtin_amdgcn_update_dpp(0u, v, 0x112, 0xf, 0xf, true);
  v += __builtin_amdgcn_update_dpp(0u, v, 0x114, 0xf, 0xf, true);
  v += __builtin_amdgcn_update_dpp(0u, v, 0x118, 0xf, 0xf, true);
  v += __builtin_amdgcn_update_dpp(0u, v, 0x142, 0xa, 0xf, true);
  v += __builtin_amdgcn_update_dpp(0u, v, 0x143, 0xc, 0xf, true);
  return v;
}
DI float sum_g(float v);
DI float qsumf(float v) {
#define FSTEP(ctl) v += __uint_as_float(__builtin_amdgcn_update_dpp(0u, __float_as_uint(v), ctl, 0xf, 0xf, true))
  FSTEP(0x128); FSTEP(0x124);
#undef FSTEP
  return sum_g(v);
}
DI unsigned wsum(unsigned v) { return __builtin_amdgcn_readlane(scan63(v), 63); }
DI float max3(float a, float b, float c) { float r; asm("v_max3_f32 %0, %1, %2, %3" : "=v"(r) : "v"(a), "v"(b), "v"(c)); return r; }
DI float max2(float a, float b) { float r; asm("v_max_f32 %0, %1, %2" : "=v"(r) : "v"(a), "v"(b)); return r; }
DI float dot2(unsigned a, unsigned b, float c) { return __builtin_amdgcn_fdot2_f32_bf16(__builtin_bit_cast(bf2, a), __builtin_bit_cast(bf2, b), c, false); }
DI f32x4 mfma(v4u a, v4u b, f32x4 c) {
  return __builtin_amdgcn_mfma_f32_16x16x32_bf16(__builtin_bit_cast(bf16x8, a), __builtin_bit_cast(bf16x8, b), c, 0, 0, 0);
}
DI float sum_g(float v) {
  auto r = __builtin_amdgcn_permlane16_swap(__float_as_uint(v), __float_as_uint(v), false, false);
  v = __uint_as_float(r[0]) + __uint_as_float(r[1]);
  auto q = __builtin_amdgcn_permlane32_swap(__float_as_uint(v), __float_as_uint(v), false, false);
  return __uint_as_float(q[0]) + __uint_as_float(q[1]);
}
DI float max_g(float v) {
  auto r = __builtin_amdgcn_permlane16_swap(__float_as_uint(v), __float_as_uint(v), false, false);
  v = max2(__uint_as_float(r[0]), __uint_as_float(r[1]));
  auto q = __builtin_amdgcn_permlane32_swap(__float_as_uint(v), __float_as_uint(v), false, false);
  return max2(__uint_as_float(q[0]), __uint_as_float(q[1]));
}
DI int sopq(int v) { v = __builtin_amdgcn_readfirstlane(v); asm volatile("" : "+s"(v)); return v; }
template <typename T> DI T opq(T v) { asm volatile("" : "+v"(v)); return v; }
#define BAR() do { asm volatile("s_waitcnt lgkmcnt(0)" ::: "memory"); __builtin_amdgcn_s_barrier(); asm volatile("" ::: "memory"); } while (0)
#define VMWAIT(n) asm volatile("s_waitcnt vmcnt(%0)" :: "i"(n) : "memory")
#define SB() __builtin_amdgcn_sched_barrier(0)
#define CBAR() asm volatile("" ::: "memory")
#define LD128(off, so) __builtin_amdgcn_raw_buffer_load_b128(rws, (off), (so), AUX_SC1)
#define LD64(off) __builtin_amdgcn_raw_buffer_load_b64(rws, (off), 0, AUX_SC1)
#define LD32(off) __builtin_amdgcn_raw_buffer_load_b32(rws, (off), 0, AUX_SC1)
#define ST128(v, off) __builtin_amdgcn_raw_buffer_store_b128((v), rws, (off), 0, AUX_SC1)
#define ST128S(v, off, so) __builtin_amdgcn_raw_buffer_store_b128((v), rws, (off), (so), AUX_SC1)
#define ST64(v, off) __builtin_amdgcn_raw_buffer_store_b64((v), rws, (off), 0, AUX_SC1)
#define ST32(v, off) __builtin_amdgcn_raw_buffer_store_b32((v), rws, (off), 0, AUX_SC1)
#define ST16(v, off) __builtin_amdgcn_raw_buffer_store_b16((v), rws, (off), 0, AUX_SC1)
#define RST(sum, off) ST128((v4u{(sum) + tag, tag, tag, tag}), (off))
#define LDS128(p, off) (*(LAS v4u*)((p) + (off)))
#define LDSF4(p, off) (*(LAS f32x4*)((p) + (off)))
DI void dma(unsigned ldsaddr, int voff, __amdgpu_buffer_rsrc_t rs, int soff) {
  asm volatile("s_mov_b32 m0, %0\n\ts_nop 0\n\tbuffer_load_dwordx4 %1, %2, %3 offen lds" :: "s"(ldsaddr), "v"(voff), "s"(rs), "s"(soff) : "memory");
}

DI void touch(unsigned ldsaddr, int voff, __amdgpu_buffer_rsrc_t rs) {
  asm volatile("s_mov_b32 m0, %0\n\ts_nop 0\n\tbuffer_load_dword %1, %2, 0 offen lds" :: "s"(ldsaddr), "v"(voff), "s"(rs) : "memory");
}
#define UNLIKELY(c) __builtin_expect(!!(c), 0)

__global__ __launch_bounds__(512, 2) void dit_kernel(const u16* __restrict__ x, const u16* __restrict__ c, const u16* __restrict__ wqkv,
                                                     const u16* __restrict__ wo, const u16* __restrict__ wmod, unsigned char* ws,
                                                     u16* __restrict__ out) {
  extern __shared__ __attribute__((aligned(16))) unsigned char lds_[];
  lp lds = (lp)lds_;
  const unsigned ldsb = (unsigned)(size_t)lds;
  STDECL;
  STAMP(0);
  const unsigned long long pc0 = __builtin_amdgcn_s_getpc();
  const int tid = threadIdx.x, lane = tid & 63, w = __builtin_amdgcn_readfirstlane(tid >> 6);
  const int n16 = lane & 15, g = lane >> 4;
  const int bid = blockIdx.x, xg = bid & 7, r = bid >> 3;
  const int b = xg >> 2, ql = r & 7, kb = r >> 3;
  const int qt = b * 32 + (xg & 3) * 8 + ql;
  const int qt3 = b * 32 + (((xg & 3) + XS) & 3) * 8 + ql;
  const bool act1 = ql < 6;
  const int cb = (xg & 3) * 6 + (act1 ? ql : ql - 2);
  const __amdgpu_buffer_rsrc_t rws = __builtin_amdgcn_make_buffer_rsrc(ws, 0, WS_BYTES, 0x00020000);
  const __amdgpu_buffer_rsrc_t rx = __builtin_amdgcn_make_buffer_rsrc((void*)x, 0, 2048 * 768, 0x00020000);
  const int dch = 16 * ((lane & 3) ^ ((lane & 32) ? 3 : 0));
  const int rslot = (4 * n16 + (g ^ ((n16 & 8) ? 3 : 0))) * 16;
  const int l63 = lane < RL ? lane * 16 : OOB;

  const int mu = (((r - 24) >> 1) * 8 + xg) * 2 + (r & 1);
  const int role = r < 12 ? 0 : ((r >= 24 && mu < 36) ? 2 : 1);
  const int dead0 = (r >= 24 && mu >= 36) ? OOB : 0;
  const int ru = r < 12 ? r : (r < 24 ? r - 12 : r - 24);
  const int u12 = 6 * (xg & 1) + ru % 6, u8 = 2 * (xg >> 1) + ru / 6;
  unsigned ep;
  asm volatile("s_nop 4\n\tbuffer_load_dword %0, off, %1, 0 sc1" : "=v"(ep) : "s"(rws) : "memory");
  if (w < 6) {
    unsigned cv = 0;
    if (role == 2) cv = ((const unsigned*)c)[tid];
    if (role < 2) {
      const __amdgpu_buffer_rsrc_t rq = __builtin_amdgcn_make_buffer_rsrc((void*)wqkv, 0, 384 * 2304, 0x00020000);
      const int v = (lane >> 2) * 2304 + dch;
      const int s3 = 48 * u8 * 2304 + (role ? 1536 : 768) + 128 * w;
#pragma unroll
      for (int t = 0; t < 3; ++t) {
        dma(ldsb + L_P0 + (t * 12 + 2 * w) * 1024, v, rq, s3 + 16 * t * 2304);
        dma(ldsb + L_P0 + (t * 12 + 2 * w + 1) * 1024, v, rq, s3 + 16 * t * 2304 + 64);
      }
      if (role == 0) {
        const int s2 = 32 * u12 * 2304 + 128 * w;
#pragma unroll
        for (int t = 0; t < 2; ++t) {
          dma(ldsb + L_P0 + ((3 + t) * 12 + 2 * w) * 1024, v, rq, s2 + 16 * t * 2304);
          dma(ldsb + L_P0 + ((3 + t) * 12 + 2 * w + 1) * 1024, v, rq, s2 + 16 * t * 2304 + 64);
        }
      }
    }
    if (role == 1 || role == 2) {
      const int ld = role == 1 ? 768 : 4608;
      const __amdgpu_buffer_rsrc_t rc = __builtin_amdgcn_make_buffer_rsrc((void*)(role == 1 ? wo : wmod), 0, 384 * ld, 0x00020000);
      const int v = (lane >> 2) * ld + dch;
#pragma unroll
      for (int j = 0; j < 4; ++j) dma(ldsb + L_P0 + (36 + 4 * w + j) * 1024, v, rc, (role == 1 ? u12 : mu) * 64 + (4 * w + j) * 16 * ld);
    }
    if (role == 2) {
      const float f0 = bflo(cv), f1 = bfhi(cv);
      const int bb = tid >= 192, i2 = 2 * (tid - 192 * bb);
      *(LAS unsigned*)(lds + L_P0 + (i2 >> 5) * 1024 + (bb * 4 + ((i2 >> 3) & 3)) * 16 + (i2 & 7) * 2) =
          cvtpk(f0 * __builtin_amdgcn_rcpf(1.f + __expf(-f0)), f1 * __builtin_amdgcn_rcpf(1.f + __expf(-f1)));
    }
    VMWAIT(0);
  } else {
    __builtin_amdgcn_s_setprio(3);
    v4u xs[3];
    asm volatile("buffer_load_dwordx4 %0, %3, %4, 0 offen\n\tbuffer_load_dwordx4 %1, %3, %4, 0 offen offset:256\n\tbuffer_load_dwordx4 %2, %3, %4, 0 offen offset:512"
                 : "=&v"(xs[0]), "=&v"(xs[1]), "=&v"(xs[2]) : "v"((32 * (bid >> 2) + 16 * (bid & 1) + 8 * (w & 1) + 2 * (bid & 2) + (lane & 3)) * 768 + (lane >> 2) * 16), "s"(rx) : "memory");
    {
      const unsigned scr = ldsb + L_SCR;
      const int dead = ((TOUCH & 1) && lane < 36) ? 0 : OOB;
      const __amdgpu_buffer_rsrc_t rq = __builtin_amdgcn_make_buffer_rsrc((void*)wqkv, 0, 384 * 2304, 0x00020000);
      touch(scr, dead + (w == 6 ? (96 * (xg >> 1) + 3 * r + lane / 12) * 2304 + 768 + (lane % 12) * 128 : (192 * (xg & 1) + 6 * r + lane / 6) * 2304 + (lane % 6) * 128), rq);
      const __amdgpu_buffer_rsrc_t r2 = __builtin_amdgcn_make_buffer_rsrc(w == 6 ? (void*)wo : (void*)(pc0 & ~127ull), 0, w == 6 ? 384 * 768 : CODEPF, 0x00020000);
      touch(scr, w == 6 ? dead + (12 * r + lane / 3) * 768 + 384 * (xg & 1) + (lane % 3) * 128 : (lane < 5 ? (r * 5 + lane) * 128 : OOB), r2);
    }
    if (w == 7) {
      const __amdgpu_buffer_rsrc_t r3 = __builtin_amdgcn_make_buffer_rsrc((void*)wmod, 0, 384 * 4608, 0x00020000);
      touch(ldsb + L_SCR, (lane < (xg < 2 ? 36 : 24) ? 0 : OOB) + (12 * r + lane % 12) * 4608 + ((lane / 12) * 8 + xg) * 128, r3);
    }
    else touch(ldsb + L_SCR, OOB, rx);
    asm volatile("s_waitcnt vmcnt(3)" : "+v"(xs[0]), "+v"(xs[1]), "+v"(xs[2]) :: "memory");
    float f[24], sm = 0.f, q = 0.f;
#pragma unroll
    for (int i = 0; i < 12; ++i) { f[2 * i] = bflo(xs[i >> 2][i & 3]); f[2 * i + 1] = bfhi(xs[i >> 2][i & 3]); sm += f[2 * i] + f[2 * i + 1]; }
    sm = qsumf(sm) * (1.f / 384.f);
#pragma unroll
    for (int i = 0; i < 24; ++i) { f[i] -= sm; q = fmaf(f[i], f[i], q); }
    const float rs = __builtin_amdgcn_rsqf(qsumf(q) * (1.f / 384.f) + 1e-5f);
#pragma unroll
    for (int i = 0; i < 3; ++i)
      LDS128(lds, L_BN + ((lane >> 2) + 16 * i) * 128 + ((w & 1) * 4 + (lane & 3)) * 16) =
          v4u{cvtpk(f[8 * i] * rs, f[8 * i + 1] * rs), cvtpk(f[8 * i + 2] * rs, f[8 * i + 3] * rs), cvtpk(f[8 * i + 4] * rs, f[8 * i + 5] * rs), cvtpk(f[8 * i + 6] * rs, f[8 * i + 7] * rs)};
  }
  asm volatile("" : "+v"(ep));
  const unsigned tag = __builtin_amdgcn_readfirstlane(ep) + 1u;
  STAMP(2);
  BAR();
  STAMP(3);
  if (w >= 6) {
    v4u nv[3];
#pragma unroll
    for (int i = 0; i < 3; ++i) nv[i] = LDS128(lds, L_BN + (w & 1) * 3072 + i * 1024 + lane * 16);
    const int no = O_NF + (bid >> 1) * 12288 + ((w & 1) * 24 + (lane >> 3)) * 256 + (bid & 1) * 128 + (lane & 7) * 16;
#pragma unroll
    for (int i = 0; i < 3; ++i) ST128(nv[i], no + i * 2048);
    RST(wsum(sum4(sum4(sum4(0u, nv[0]), nv[1]), nv[2])), l63 + O_RN + (bid * 2 + (w & 1)) * 128);
    __builtin_amdgcn_s_setprio(0);
  }
  const int rg = b * 32 + kb * 8 + w;
  const int nro = lane < 8 ? O_RN + (rg * 8 + lane) * 128 : OOB;
  if (w < (role == 2 ? 2 : 6)) {
    const int t3 = w >> 1, t2 = w & 1;
    f32x4 acc = {};
    v4u a[12], bq[12];
    if (role == 0) {
      lp ap = opq(lds + L_P0 + (3 + t2) * 12288 + rslot), bp = opq(lds + L_P0 + t3 * 12288 + rslot);
#pragma unroll
      for (int k = 0; k < 12; ++k) {
        a[k] = LDS128(ap, k * 1024); bq[k] = LDS128(bp, k * 1024);
        if (k >= 5) acc = mfma(a[k - 5], bq[k - 5], acc);
        SB();
      }
    } else {
      lp ap = opq(lds + L_P0 + (role == 2 ? ((n16 & 1) * 4 + g) * 16 : t3 * 12288 + rslot));
      lp tb = opq(lds + L_P0 + 36864 + (8 * g + (n16 >> 2)) * 64 + 16 * ((2 * t2 + ((n16 >> 1) & 1)) ^ ((g & 1) ? 3 : 0)) + 8 * (n16 & 1));
#pragma unroll
      for (int k = 0; k < 12; ++k) {
        a[k] = LDS128(ap, k * 1024);
        bq[k] = __builtin_bit_cast(v4u, __builtin_shufflevector(__builtin_amdgcn_ds_read_tr16_b64_v4i16((LAS s16x4*)(tb + k * 2048)),
                                                                __builtin_amdgcn_ds_read_tr16_b64_v4i16((LAS s16x4*)(tb + k * 2048 + 256)), 0, 1, 2, 3, 4, 5, 6, 7));
        if (k >= 5) acc = mfma(a[k - 5], bq[k - 5], acc);
        SB();
      }
    }
#pragma unroll
    for (int k = 7; k < 12; ++k) acc = mfma(a[k], bq[k], acc);
    if (role != 2) {
      const int blk = role * 576 + (role == 0 ? (3 * u8 + t3) * 24 + 2 * u12 + t2 : (2 * u12 + t2) * 24 + 3 * u8 + t3);
      const unsigned o0 = cvtpk(acc[0], acc[1]), o1 = cvtpk(acc[2], acc[3]);
      auto r0 = __builtin_amdgcn_permlane16_swap(o0, o0, false, false);
      auto r1 = __builtin_amdgcn_permlane16_swap(o1, o1, false, false);
      ST128((v4u{r0[0], r1[0], r0[1], r1[1]}), (((g & 1) ? OOB : 0) | dead0) + O_MT + blk * 512 + ((g >> 1) * 16 + n16) * 16);
      RST(wsum(o0 + o1), (l63 | dead0) + O_RM + blk * 128);
    } else if (g < 2) {
      float v = bfround(g ? acc[1] : acc[0]);
      if (mu >= 12 && mu < 24) v += 1.f;
      ST32(tagged(v, tag & 0xffffu), O_MOD + (2 * mu + t2) * 128 + lane * 4);
    }
  }
  STAMP(4);
  __builtin_amdgcn_s_barrier();
  if (DLY1) __builtin_amdgcn_s_sleep(DLY1);
  v2u nr = LD64(nro);
  SB();
  const int l16 = opq(lane * 16);
  const int s_n = O_NF + rg * 24576;
  const int rot = ROT ? (ql * 3) >> 1 : 0;
  const int l16b = opq(lane * 16 + 12288);
  v4u xf[2][12];
#define XF(i) xf[(i) & 1][(i) >> 1]
#define XLOAD(i) XF(i) = LD128(((i) & 1) ? l16b : l16, s_n + (((i) >> 1) + rot - (((i) >> 1) + rot >= 12 ? 12 : 0)) * 1024)
  lp fl = opq(lds + L_MS + lane * 16);
  v4u qf[3]; unsigned qr;
  const int qro = opq(lane < 3 ? O_RQ + (qt * 24 + 3 * w + lane) * 128 : OOB);
  const int s_ql = sopq(O_QF + (qt * 24 + 3 * w) * 1024);
#define HOP2_LOADS() do { _Pragma("unroll") for (int i = 0; i < 3; ++i) qf[i] = LD128(l16 + i * 1024, s_ql); qr = LD32(qro); } while (0)
  {
    const int s0 = 3 * (w & 3);
    const int mc4 = lane < 24 ? 96 + 8 * s0 + lane : (lane < 48 ? 8 * s0 + lane - 24 : (w < 4 ? 96 : 192) + 4 * cb + (lane & 3));
    const int mo = opq(O_MOD + (mc4 >> 2) * 128 + b * 64 + (mc4 & 3) * 16);
    const int fb = (w >> 2) * 576 + cb * 24 + 2 * s0;
    const int ro = opq(lane < 6 ? O_RM + (fb + lane) * 128 : OOB);
    const int s_f = sopq(O_MT + fb * 512);
    const int blk1 = rg * 24 + cb, dead1 = act1 ? 0 : OOB;
    const int qso = opq(dead1 + O_QF + blk1 * 1024 + ((g & 1) * 32 + (g >> 1) * 16 + n16) * 16);
    const int vso = opq(dead1 + O_VF + blk1 * 1024 + lane * 16);
    const int rso = opq((dead1 | l63) + O_RQ + blk1 * 128);
    const int rvo = opq((dead1 | l63) + O_RV + (((b * 4 + kb) * 24 + cb) * 8 + w) * 128);
    lp cg = opq(lds + L_CS + g * 16), cn = opq(lds + L_CS + 256 + n16 * 4), csw = opq(lds + L_CS + w * 64 + n16 * 4);
    lp bn = lds + L_BN + w * 768, bg = opq(bn + g * 32), msw[3];
#pragma unroll
    for (int i = 0; i < 3; ++i) { int sl = s0 + i - rot; if (sl < 0) sl += 12; msw[i] = opq(fl + (w >> 2) * 12288 + sl * 1024); }
    SB();
    if (UNLIKELY(!__all(lane >= 8 || nr[1] == tag))) {
      unsigned it = 0;
      do { RETRY(0); __builtin_amdgcn_s_sleep(2); CBAR(); nr = LD64(nro); } while (!__all(lane >= 8 || nr[1] == tag) && ++it < SPIN_CAP);
    }
    if (GATE == 2) __builtin_amdgcn_s_barrier();
    STAMP(5);
    unsigned hx = 0;
    v4u mf[3], mv4; unsigned mr;
    f32x4 sa[3], sb[3]; v4u tf[3]; float oc_;
#define MODOK() __all((tchk(0u, mv4, tag) & 0xffffu) == 0u)
#define HOP1_LOADS() do { _Pragma("unroll") for (int i = 0; i < 3; ++i) mf[i] = LD128(l16 + i * 1024, s_f); mr = LD32(ro); } while (0)
#pragma unroll
    for (int i = 0; i < NX1; ++i) {
      XLOAD(i);
      if (i >= HL) hx = sum4(hx, XF(i - HL));
      if (i == MP) mv4 = LD128(mo, 0);
      if (i == HP) HOP1_LOADS();
      if (i == NX1 - 1) nr = LD64(nro);
      if (i == BA && w >= 4) __builtin_amdgcn_s_barrier();
      if (i == MC) {
        if (UNLIKELY(!MODOK())) {
          unsigned it = 0;
          do { RETRY(1); __builtin_amdgcn_s_sleep(8); CBAR(); mv4 = LD128(mo, 0); } while (!MODOK() && ++it < SPIN_CAP);
        }
        if (lane >= 24 && lane < 48) *(LAS v2u*)(bn + 512 + (lane - 24) * 8) = v2u{pkhi(mv4[0], mv4[1]), pkhi(mv4[2], mv4[3])};
        else LDS128(bn, (lane < 24 ? lane : (lane & 3) + 24) * 16) = mv4 & 0xffff0000u;
        CBAR();
#pragma unroll
        for (int k = 0; k < 3; ++k) { sa[k] = LDSF4(bg, 128 * k); sb[k] = LDSF4(bg, 128 * k + 16); tf[k] = LDS128(bn, 512 + 64 * k + 16 * g); }
        oc_ = *(LAS float*)(bn + 384 + n16 * 4);
      }
      if (i == MC + 1) { if (w < 4) oc_ *= 0.05103103630798288f * 1.4426950408889634f; sa[0] = opq(sa[0] * oc_); sb[0] = opq(sb[0] * oc_); }
      if (i == MC + 2) { sa[1] = opq(sa[1] * oc_); sb[1] = opq(sb[1] * oc_); }
      if (i == MC + 3) { sa[2] = opq(sa[2] * oc_); sb[2] = opq(sb[2] * oc_); }
      asm volatile("" : "+v"(hx));
      SB();
    }
    STAMP(6);
#define FBAD() (wsum(sum4(sum4(sum4(-mr, mf[0]), mf[1]), mf[2])) + 6u * tag != 0u)
    if (UNLIKELY(FBAD())) {
      unsigned it = 0;
      do { RETRY(9); __builtin_amdgcn_s_sleep(6); CBAR(); HOP1_LOADS(); } while (FBAD() && ++it < SPIN_CAP);
    }
    STAMP(7);
    {
      f32x4 au = {};
#pragma unroll
      for (int i = 0; i < 3; ++i) {
        au = mfma(tf[i], mf[i], au);
        const v4u o = {cvtpk(bflo(mf[i][0]) * sa[i][0], bfhi(mf[i][0]) * sa[i][1]), cvtpk(bflo(mf[i][1]) * sa[i][2], bfhi(mf[i][1]) * sa[i][3]),
                       cvtpk(bflo(mf[i][2]) * sb[i][0], bfhi(mf[i][2]) * sb[i][1]), cvtpk(bflo(mf[i][3]) * sb[i][2], bfhi(mf[i][3]) * sb[i][3])};
        LDS128(msw[i], 0) = o;
        if (NX1 + i < 24) XLOAD(NX1 + i);
        hx = opq(sum4(hx, XF(NX1 - HL + i)));
        SB();
      }
      *(LAS float*)(csw) = au[0] * oc_;
    }
    STAMP(9);
    __builtin_amdgcn_s_setprio(2);
    BAR();
    v4u a[24];
    f32x4 uu[4]; float zz[4], zs;
#define AOP(k) do { if ((k) + PD < ((k) < 12 ? 12 : 24)) a[(k) + PD] = LDS128(fl, ((k) + PD) * 1024); \
      if ((k) < 12) { aq[0] = mfma(a[k], xf[0][(k) % 12], aq[0]); aq[1] = mfma(a[k], xf[1][(k) % 12], aq[1]); } \
      else { av[0] = mfma(xf[0][(k) % 12], a[k], av[0]); av[1] = mfma(xf[1][(k) % 12], a[k], av[1]); } } while (0)
#pragma unroll
    for (int k = 0; k < PD; ++k) a[k] = LDS128(fl, k * 1024);
    SB();
    f32x4 aq[2] = {}, av[2] = {};
#pragma unroll
    for (int k = 0; k < 12; ++k) {
      if (NX1 + 3 + k < 24) XLOAD(NX1 + 3 + k);
      if (k >= 2 && k < 6) uu[k - 2] = LDSF4(cg, (k - 2) * 64);
      AOP(k);
      if (2 * k >= NX1 - HL + 3) hx = sum4(hx, XF(2 * k));
      if (2 * k + 1 >= NX1 - HL + 3) hx = sum4(hx, XF(2 * k + 1));
      if (k == 9) { uu[0] = uu[0] + uu[1]; uu[2] = uu[2] + uu[3]; }
      if (k == 10) uu[0] = uu[0] + uu[2];
      asm volatile("" : "+v"(hx));
      SB();
    }
    if (UNLIKELY(wsum(hx - nr[0]) + 8u * tag != 0u)) {
      unsigned it = 0;
      do { RETRY(4); __builtin_amdgcn_s_sleep(4); CBAR();
        hx = -LD32(nro);
#pragma unroll
        for (int i = 0; i < 24; ++i) XLOAD(i);
#pragma unroll
        for (int i = 0; i < 24; ++i) hx = sum4(hx, XF(i));
      } while (wsum(hx) + 8u * tag != 0u && ++it < SPIN_CAP);
      aq[0] = aq[1] = f32x4{0.f, 0.f, 0.f, 0.f};
#pragma unroll
      for (int s = 0; s < 12; ++s) {
        const v4u a0 = LDS128(fl, s * 1024);
        aq[0] = mfma(a0, xf[0][s], aq[0]); aq[1] = mfma(a0, xf[1][s], aq[1]);
      }
    }
    {
      const f32x4 v0 = aq[0] + uu[0], v1 = aq[1] + uu[0];
      auto r0 = __builtin_amdgcn_permlane16_swap(cvtpk(v0[0], v0[1]), cvtpk(v1[0], v1[1]), false, false);
      auto r1 = __builtin_amdgcn_permlane16_swap(cvtpk(v0[2], v0[3]), cvtpk(v1[2], v1[3]), false, false);
      const v4u ov = {r0[0], r1[0], r0[1], r1[1]};
      ST128(ov, qso);
      RST(wsum(sum4(0u, ov)), rso);
      __builtin_amdgcn_s_setprio(0);
      STAMP(12);
      if (w < 4) BAR();
      CBAR();
#pragma unroll
      for (int k = 12; k < 12 + PD; ++k) a[k] = LDS128(fl, k * 1024);
      SB();
#pragma unroll
      for (int k = 12; k < 24; ++k) {
        if (k >= 14 && k < 18) zz[k - 14] = *(LAS float*)(cn + (k - 14) * 64);
        AOP(k);
        if (k == 21) zs = (zz[0] + zz[1]) + (zz[2] + zz[3]);
        SB();
      }
      const f32x4 w0 = av[0] + zs, w1 = av[1] + zs;
      const v4u o = {cvtpk(w0[0], w0[1]), cvtpk(w0[2], w0[3]), cvtpk(w1[0], w1[1]), cvtpk(w1[2], w1[3])};
      ST128(o, vso);
      RST(wsum(sum4(0u, o)), rvo);
    }
    STAMP(10);
    SB();
  }
  STAMP(11);
  if (w == 7) ST128((v4u{tag, tag, tag, tag}), l63 + O_CL + bid * 128);
  const int vro = opq(lane < 24 ? O_RV + (((b * 4 + kb) * 8 + w) * 24 + lane) * 128 : OOB);
  const int mso = opq((w == 0 && g < 2) ? O_ML + ((qt * 4 + kb) * 32 + lane) * 16 : OOB);
  const int s_vl = sopq(O_VF + ((b * 32 + 8 * kb) * 24 + 3 * w) * 1024);
  const bool own = (w >> 1) == kb;
  const int oso = opq((own ? OOB : 0) + O_OF + ((qt * 24 + 3 * w) * 4 + kb) * 1024 + ((g & 1) * 32 + (g >> 1) * 16 + n16) * 16);
  const int roo = opq(((own ? OOB : 0) | l63) + O_RO + ((qt * 24 + 3 * w) * 4 + kb) * 128);
  lp mxr = opq(lds + L_MX + n16 * 32), mxw = opq(lds + L_MX + n16 * 32 + w * 4);
  lp plw = opq(lds + lane * 16 + w * 2048);
  lp pl = opq(lds + lane * 16);
  const int ooff = opq(((qt * 32 + 8 * (n16 >> 2) + 4 * (lane >> 5) + (n16 & 3)) * 384 + (6 * kb + w) * 16 + 8 * (g & 1)) * 2);
  const v4u xr = __builtin_amdgcn_raw_buffer_load_b128(rx, w < 6 ? ooff : OOB, 0, 0);
  const int mlo = opq(O_ML + (qt * 128 + (lane >> 5) * 16 + n16) * 16);
  const int s_ol = sopq(O_OF + (qt * 24 + 6 * kb + w) * 4096), rlo = opq((lane < 4 && lane != kb) ? O_RO + ((qt * 24 + 6 * kb + w) * 4 + lane) * 128 : OOB);
  v4u vf[8][3];
#define VF(i) vf[(i) & 7][(i) >> 3]
#define VLOAD(i) VF(i) = LD128(l16 + ((i) >> 3) * 1024, s_vl + ((i) & 7) * 24576)
  {
    lp qw = opq(lds + L_QS + (lane >> 5) * 12288 + 3 * w * 512 + (lane & 31) * 16);
    SB();
    __builtin_amdgcn_s_barrier();
    if (DLY2) __builtin_amdgcn_s_sleep(DLY2);
    HOP2_LOADS();
#pragma unroll
    for (int i = 0; i < NV0; ++i) VLOAD(i);
    SB();
#define QBAD() (wsum(sum4(sum4(sum4(-qr, qf[0]), qf[1]), qf[2])) + 3u * tag != 0u)
#pragma unroll
    for (int i = 0; i < 3; ++i) LDS128(qw, i * 512) = qf[i];
    if (UNLIKELY(QBAD())) {
      unsigned it = 0;
      do { RETRY(2); __builtin_amdgcn_s_sleep(3); CBAR(); HOP2_LOADS(); } while (QBAD() && ++it < SPIN_CAP);
#pragma unroll
      for (int i = 0; i < 3; ++i) LDS128(qw, i * 512) = qf[i];
    }
  }
  STAMP(13);
  BAR();
  float m[2]; unsigned vr, hv0 = 0;
  {
    v4u q[24];
#define QREAD(i) q[i] = LDS128(fl, 24576 + (((i) & 1) * 12 + ((i) >> 1)) * 1024)
#pragma unroll
    for (int i = 0; i < PD; ++i) QREAD(i);
    SB();
    f32x4 acc[2][2] = {};
#pragma unroll
    for (int i = 0; i < 24; ++i) {
      if (i + PD < 24) QREAD(i + PD);
      acc[i & 1][0] = mfma(xf[0][i >> 1], q[i], acc[i & 1][0]); acc[i & 1][1] = mfma(xf[1][i >> 1], q[i], acc[i & 1][1]);
      if ((i * NVS) / 24 != ((i + 1) * NVS) / 24) VLOAD(NV0 + (i * NVS) / 24);
      if (i >= 24 - NH) hv0 = opq(sum4(hv0, VF(i - (24 - NH))));
      SB();
    }
#pragma unroll
    for (int u = 0; u < 2; ++u) {
      float mx = max3(max3(acc[u][0][0], acc[u][0][1], acc[u][0][2]), acc[u][0][3], acc[u][1][0]);
      mx = max_g(max2(max3(mx, acc[u][1][1], acc[u][1][2]), acc[u][1][3]));
      *(LAS float*)(mxw + u * 512) = mx;
    }
    STAMP(14);
    BAR();
    f32x4 ma[2], mb[2];
#pragma unroll
    for (int u = 0; u < 2; ++u) { ma[u] = LDSF4(mxr, u * 512); mb[u] = LDSF4(mxr, u * 512 + 16); }
    vr = LD32(vro);
    SB();
#pragma unroll
    for (int u = 0; u < 2; ++u) {
      m[u] = max2(max3(max3(ma[u][0], ma[u][1], ma[u][2]), ma[u][3], mb[u][0]), max3(mb[u][1], mb[u][2], mb[u][3]));
      float p[8];
#pragma unroll
      for (int i = 0; i < 8; ++i) p[i] = __builtin_amdgcn_exp2f(acc[u][i >> 2][i & 3] - m[u]);
      LDS128(plw, u * 1024) = v4u{cvtpk(p[0], p[1]), cvtpk(p[2], p[3]), cvtpk(p[4], p[5]), cvtpk(p[6], p[7])};
      if (NV0 + NVS + u < NV2) VLOAD(NV0 + NVS + u);
      SB();
    }
#pragma unroll
    for (int i = NV0 + NVS + 2; i < NV2; ++i) VLOAD(i);
  }
  STAMP(15);
  BAR();
  v4u ml[4];
#define ML_LOADS() do { _Pragma("unroll") for (int k = 0; k < 4; ++k) ml[k] = LD128(mlo, ((kb + k) & 3) * 512); } while (0)
  {
    v4u pf[16];
    f32x4 acc[3][2] = {};
    v4u ov[3];
    unsigned hv[3];
#define OSTORE(et) do { const f32x4 v0 = acc[et][0], v1 = acc[et][1]; \
      auto r0 = __builtin_amdgcn_permlane16_swap(cvtpk(v0[0], v0[1]), cvtpk(v1[0], v1[1]), false, false); \
      auto r1 = __builtin_amdgcn_permlane16_swap(cvtpk(v0[2], v0[3]), cvtpk(v1[2], v1[3]), false, false); \
      ov[et] = v4u{r0[0], r1[0], r0[1], r1[1]}; ST128S(ov[et], oso, (et) * 4096); \
      if (own) LDS128(lds + 16384 + ((w & 1) * 3 + (et)) * 1024, ((g & 1) * 32 + (g >> 1) * 16 + n16) * 16) = ov[et]; } while (0)
#define ORECORD(et) RST(wsum(sum4(0u, ov[et])), roo + (et) * 512)
#define VCHECK(et) do { if (UNLIKELY(wsum(hv[et] - ((lane >> 3) == (et) ? vr : 0u)) + 8u * tag != 0u)) { unsigned it = 0; \
        do { RETRY(3); __builtin_amdgcn_s_sleep(2); CBAR(); \
          _Pragma("unroll") for (int i = 0; i < 8; ++i) VLOAD(8 * (et) + i); \
          vr = LD32(vro); hv[et] = 0; \
          _Pragma("unroll") for (int i = 0; i < 8; ++i) hv[et] = sum4(hv[et], VF(8 * (et) + i)); \
        } while (wsum(hv[et] - ((lane >> 3) == (et) ? vr : 0u)) + 8u * tag != 0u && ++it < SPIN_CAP); \
        acc[et][0] = acc[et][1] = f32x4{0.f, 0.f, 0.f, 0.f}; \
        _Pragma("unroll") for (int i = 0; i < 16; ++i) acc[et][i & 1] = mfma(vf[i >> 1][et], pf[i], acc[et][i & 1]); } } while (0)
#pragma unroll
    for (int i = 0; i < 8; ++i) pf[i] = LDS128(pl, i * 1024);
    SB();
    hv[0] = hv0; hv[1] = hv[2] = 0;
#pragma unroll
    for (int i = 0; i < 16; ++i) {
      if (i + 8 < 16) pf[i + 8] = LDS128(pl, (i + 8) * 1024);
      if ((i & 1) && NV2 + (i >> 1) < 24) VLOAD(NV2 + (i >> 1));
      acc[0][i & 1] = mfma(vf[i >> 1][0], pf[i], acc[0][i & 1]);
      if ((i & 1) && (i >> 1) >= NH) hv[0] = opq(sum4(hv[0], vf[i >> 1][0]));
      if (!(i & 1)) hv[1] = opq(sum4(hv[1], vf[i >> 1][1]));
      SB();
    }
    if (w == 0) {
      const v4u ones = {0x3f803f80u, 0x3f803f80u, 0x3f803f80u, 0x3f803f80u};
      f32x4 ls[2] = {};
#pragma unroll
      for (int i = 0; i < 16; ++i) ls[i & 1] = mfma(ones, pf[i], ls[i & 1]);
      ST128((v4u{__float_as_uint(g ? m[1] : m[0]), tag, __float_as_uint(g ? ls[1][0] : ls[0][0]), tag}), mso);
    }
    VCHECK(0);
#pragma unroll
    for (int i = 0; i < 16; ++i) {
      acc[1][i & 1] = mfma(vf[i >> 1][1], pf[i], acc[1][i & 1]);
      if (i == 0) OSTORE(0);
      if (i == 6) ORECORD(0);
      if ((i & 3) == 3) SB();
    }
    VCHECK(1);
#pragma unroll
    for (int i = 0; i < 16; ++i) {
      acc[2][i & 1] = mfma(vf[i >> 1][2], pf[i], acc[2][i & 1]);
      if (i & 1) hv[2] = opq(sum4(hv[2], vf[i >> 1][2]));
      if (i == 0) OSTORE(1);
      if (i == 6) ORECORD(1);
      if (i == 12) ML_LOADS();
      if ((i & 3) == 3) SB();
    }
    VCHECK(2);
    STAMP(16);
    OSTORE(2);
    ORECORD(2);
  }
  STAMP(17);
  BAR();
  if (w < 6) {
    v4u pp[4]; unsigned orr;
#define MLOK() __all(((ml[0][1] ^ tag) | (ml[1][1] ^ tag) | (ml[2][1] ^ tag) | (ml[3][1] ^ tag) | (ml[0][3] ^ tag) | (ml[1][3] ^ tag) | (ml[2][3] ^ tag) | (ml[3][3] ^ tag)) == 0u)
#define HOP3_LOADS() do { _Pragma("unroll") for (int k = 1; k < 4; ++k) pp[k] = LD128(l16, s_ol + ((kb + k) & 3) * 1024); orr = LD32(rlo); } while (0)
#define OBAD() (wsum(sum4(sum4(sum4(-orr, pp[1]), pp[2]), pp[3])) + 3u * tag != 0u)
    pp[0] = LDS128(pl, 16384 + w * 1024);
    SB();
    if (DLY3) __builtin_amdgcn_s_sleep(DLY3);
    HOP3_LOADS();
    SB();
    STAMP(18);
    if (UNLIKELY(!MLOK())) {
      unsigned it = 0;
      do { RETRY(6); __builtin_amdgcn_s_sleep(2); CBAR(); ML_LOADS(); } while (!MLOK() && ++it < SPIN_CAP);
    }
    const float mm = max2(max2(__uint_as_float(ml[0][0]), __uint_as_float(ml[1][0])), max2(__uint_as_float(ml[2][0]), __uint_as_float(ml[3][0])));
    float wk[4], o[8];
#pragma unroll
    for (int k = 0; k < 4; ++k) wk[k] = __builtin_amdgcn_exp2f(__uint_as_float(ml[k][0]) - mm);
    const float inv = __builtin_amdgcn_rcpf((wk[0] * __uint_as_float(ml[0][2]) + wk[1] * __uint_as_float(ml[1][2])) + (wk[2] * __uint_as_float(ml[2][2]) + wk[3] * __uint_as_float(ml[3][2])));
#pragma unroll
    for (int k = 0; k < 4; ++k) wk[k] *= inv;
#pragma unroll
    for (int e = 0; e < 4; ++e) { o[2 * e] = opq(fmaf(wk[0], bflo(pp[0][e]), bflo(xr[e]))); o[2 * e + 1] = opq(fmaf(wk[0], bfhi(pp[0][e]), bfhi(xr[e]))); }
    SB();
    if (UNLIKELY(OBAD())) {
      unsigned it = 0;
      do { RETRY(5); __builtin_amdgcn_s_sleep(3); CBAR(); HOP3_LOADS(); } while (OBAD() && ++it < SPIN_CAP);
    }
    STAMP(1);
    v4u ov;
#pragma unroll
    for (int e = 0; e < 4; ++e) {
#pragma unroll
      for (int k = 1; k < 4; ++k) { o[2 * e] = fmaf(wk[k], bflo(pp[k][e]), o[2 * e]); o[2 * e + 1] = fmaf(wk[k], bfhi(pp[k][e]), o[2 * e + 1]); }
      ov[e] = cvtpk(o[2 * e], o[2 * e + 1]);
    }
    *(v4u*)((unsigned char*)out + ooff) = ov;
  } else if (UNLIKELY(bid == 255 && w == 7)) {
#pragma unroll 1
    for (unsigned it = 0; it < SPIN_CAP; ++it) {
      CBAR();
      unsigned bad = 0;
#pragma unroll
      for (int k = 0; k < 4; ++k) bad |= LD32(O_CL + (k * 64 + lane) * 128) ^ tag;
      if (__all(bad == 0)) break;
    }
    if (lane == 0) __hip_atomic_store((gu32*)(ws + O_EPOCH), tag, __ATOMIC_RELAXED, __HIP_MEMORY_SCOPE_AGENT);
  }
  STAMP(19);
  STFLUSH();
}

extern "C" void kernel_launch(void* const* d_in, const int* in_sizes, int n_in, void* d_out, int out_size, void* d_ws, size_t ws_size,
                              hipStream_t stream) {
  (void)hipFuncSetAttribute((const void*)dit_kernel, hipFuncAttributeMaxDynamicSharedMemorySize, L_TOTAL);
  dit_kernel<<<256, 512, L_TOTAL, stream>>>((const u16*)d_in[0], (const u16*)d_in[1], (const u16*)d_in[2], (const u16*)d_in[3],
                                            (const u16*)d_in[4], (unsigned char*)d_ws, (u16*)d_out);
}
```

```cpp
#include <hip/hip_runtime.h>
#include <hip/hip_bf16.h>

typedef short bf16x8 __attribute__((ext_vector_type(8)));
typedef short s16x4 __attribute__((ext_vector_type(4)));
typedef float f32x4 __attribute__((ext_vector_type(4)));
typedef float f32x2 __attribute__((ext_vector_type(2)));
typedef unsigned v4u __attribute__((ext_vector_type(4)));
typedef unsigned v2u __attribute__((ext_vector_type(2)));
typedef __bf16 bf2 __attribute__((ext_vector_type(2)));
typedef unsigned short u16;
#define DI __device__ __forceinline__
#define LAS __attribute__((address_space(3)))
#define GAS __attribute__((address_space(1)))
typedef GAS unsigned gu32;
typedef LAS unsigned char* lp;
#define STDECL
#define STAMP(i)
#define STFLUSH()
#define RETRY(k)
#define DLY0 30
#define DLY1 4
#define DLY2 2
#define PRE 1
#define HP 6
#define MP 2
#define TOUCH 1
#define CODEPF 18432
#define XS 0
#define DLY3 4
#define PD 4
#define SLP 2
#define ROT 0
#define NX1 16
#define NEARLY 0
#ifndef NV0
#define NV0 0
#endif
#define AUXO 16
#define NH 6
#ifndef NV2
#define NV2 18
#endif
#define HL 8
#define MC 9
#define DLY4 18
#define DLY5 8
#define BA 4
#define GATE 2
#define RL 8
#define MLP 12
#define PREWR 1
#ifndef NVS
#define NVS 13
#endif

constexpr int O_EPOCH = 0;
constexpr int O_MOD = 4096;
constexpr int O_CL = 16384;
constexpr int O_MT = 131072;
constexpr int O_NF = O_MT + 589824;
constexpr int O_QF = O_NF + 1572864;
constexpr int O_VF = O_QF + 1572864;
constexpr int O_OF = O_VF + 1572864;
constexpr int O_ML = O_OF + 6291456;
constexpr int O_RM = O_ML + 131072;
constexpr int O_RN = O_RM + 1152 * 128;
constexpr int O_RQ = O_RN + 512 * 128;
constexpr int O_RV = O_RQ + 1536 * 128;
constexpr int O_RO = O_RV + 1536 * 128;
constexpr int WS_BYTES = O_RO + 6144 * 128;
constexpr int OOB = 0x40000000;

constexpr int L_P0 = 0;
constexpr int L_MS = 61440, L_QS = L_MS + 24576, L_CS = L_QS + 24576, L_MX = L_CS + 512, L_LS = L_MX + 1024, L_BN = L_LS + 1024;
constexpr int L_SCR = L_BN + 6144, L_TAG = L_SCR + 256;
constexpr int L_TOTAL = L_TAG + 32;

static_assert(NV2 >= 16 && NV0 + NVS <= NV2 && NV2 <= 24, "");
static_assert(BA < NX1 && MC + 3 < NX1 && MP < MC && HP < NX1 && NX1 >= 9 && NX1 <= 21, "");
constexpr int AUX_SC1 = 16;
constexpr unsigned SPIN_CAP = 1u << 16;

DI float bflo(unsigned v) { return __uint_as_float(v << 16); }
DI float bfhi(unsigned v) { return __uint_as_float(v & 0xffff0000u); }
DI unsigned cvtpk(float lo, float hi) {
  f32x2 x = {lo, hi};
  return __builtin_bit_cast(unsigned, __builtin_convertvector(x, bf2));
}
DI float bfround(float x) { return bflo(cvtpk(x, 0.f)); }
DI unsigned pkhi(unsigned lo, unsigned hi) { return __builtin_amdgcn_perm(hi, lo, 0x07060302u); }
DI unsigned tagged(float v, unsigned tag16) { return (cvtpk(0.f, v) & 0xffff0000u) | tag16; }
DI unsigned tchk(unsigned a, v4u v, unsigned tag) { return a | (v[0] ^ tag) | (v[1] ^ tag) | (v[2] ^ tag) | (v[3] ^ tag); }
DI unsigned sum4(unsigned a, v4u v) { return a + v[0] + v[1] + v[2] + v[3]; }
DI unsigned scan63(unsigned v) {
  v += __builtin_amdgcn_update_dpp(0u, v, 0x111, 0xf, 0xf, true);
  v += __builtin_amdgcn_update_dpp(0u, v, 0x112, 0xf, 0xf, true);
  v += __builtin_amdgcn_update_dpp(0u, v, 0x114, 0xf, 0xf, true);
  v += __builtin_amdgcn_update_dpp(0u, v, 0x118, 0xf, 0xf, true);
  v += __builtin_amdgcn_update_dpp(0u, v, 0x142, 0xa, 0xf, true);
  v += __builtin_amdgcn_update_dpp(0u, v, 0x143, 0xc, 0xf, true);
  return v;
}
DI float sum_g(float v);
DI float qsumf(float v) {
#define FSTEP(ctl) v += __uint_as_float(__builtin_amdgcn_update_dpp(0u, __float_as_uint(v), ctl, 0xf, 0xf, true))
  FSTEP(0x128); FSTEP(0x124);
#undef FSTEP
  return sum_g(v);
}
DI unsigned wsum(unsigned v) { return __builtin_amdgcn_readlane(scan63(v), 63); }
DI float max3(float a, float b, float c) { float r; asm("v_max3_f32 %0, %1, %2, %3" : "=v"(r) : "v"(a), "v"(b), "v"(c)); return r; }
DI float max2(float a, float b) { float r; asm("v_max_f32 %0, %1, %2" : "=v"(r) : "v"(a), "v"(b)); return r; }
DI float dot2(unsigned a, unsigned b, float c) { return __builtin_amdgcn_fdot2_f32_bf16(__builtin_bit_cast(bf2, a), __builtin_bit_cast(bf2, b), c, false); }
DI f32x4 mfma(v4u a, v4u b, f32x4 c) {
  return __builtin_amdgcn_mfma_f32_16x16x32_bf16(__builtin_bit_cast(bf16x8, a), __builtin_bit_cast(bf16x8, b), c, 0, 0, 0);
}
DI float sum_g(float v) {
  auto r = __builtin_amdgcn_permlane16_swap(__float_as_uint(v), __float_as_uint(v), false, false);
  v = __uint_as_float(r[0]) + __uint_as_float(r[1]);
  auto q = __builtin_amdgcn_permlane32_swap(__float_as_uint(v), __float_as_uint(v), false, false);
  return __uint_as_float(q[0]) + __uint_as_float(q[1]);
}
DI float max_g(float v) {
  auto r = __builtin_amdgcn_permlane16_swap(__float_as_uint(v), __float_as_uint(v), false, false);
  v = max2(__uint_as_float(r[0]), __uint_as_float(r[1]));
  auto q = __builtin_amdgcn_permlane32_swap(__float_as_uint(v), __float_as_uint(v), false, false);
  return max2(__uint_as_float(q[0]), __uint_as_float(q[1]));
}
DI int sopq(int v) { v = __builtin_amdgcn_readfirstlane(v); asm volatile("" : "+s"(v)); return v; }
template <typename T> DI T opq(T v) { asm volatile("" : "+v"(v)); return v; }
#define BAR() do { asm volatile("s_waitcnt lgkmcnt(0)" ::: "memory"); __builtin_amdgcn_s_barrier(); asm volatile("" ::: "memory"); } while (0)
#define VMWAIT(n) asm volatile("s_waitcnt vmcnt(%0)" :: "i"(n) : "memory")
#define SB() __builtin_amdgcn_sched_barrier(0)
#define CBAR() asm volatile("" ::: "memory")
#define LD128(off, so) __builtin_amdgcn_raw_buffer_load_b128(rws, (off), (so), AUX_SC1)
#define LD64(off) __builtin_amdgcn_raw_buffer_load_b64(rws, (off), 0, AUX_SC1)
#define LD32(off) __builtin_amdgcn_raw_buffer_load_b32(rws, (off), 0, AUX_SC1)
#define ST128(v, off) __builtin_amdgcn_raw_buffer_store_b128((v), rws, (off), 0, AUX_SC1)
#define ST128S(v, off, so) __builtin_amdgcn_raw_buffer_store_b128((v), rws, (off), (so), AUX_SC1)
#define ST64(v, off) __builtin_amdgcn_raw_buffer_store_b64((v), rws, (off), 0, AUX_SC1)
#define ST32(v, off) __builtin_amdgcn_raw_buffer_store_b32((v), rws, (off), 0, AUX_SC1)
#define ST16(v, off) __builtin_amdgcn_raw_buffer_store_b16((v), rws, (off), 0, AUX_SC1)
#define RST(sum, off) ST128((v4u{(sum) + tag, tag, tag, tag}), (off))
#define RECOK(v, off) __all((off) >= OOB || (v) == tag)
#define WAITREC(off) do { unsigned w_ = 0; CBAR(); while (!RECOK(LD32((off) + 4), (off)) && ++w_ < SPIN_CAP) { __builtin_amdgcn_s_sleep(2); CBAR(); } CBAR(); } while (0)
#define LDS128(p, off) (*(LAS v4u*)((p) + (off)))
#define LDSF4(p, off) (*(LAS f32x4*)((p) + (off)))
DI void dma(unsigned ldsaddr, int voff, __amdgpu_buffer_rsrc_t rs, int soff) {
  asm volatile("s_mov_b32 m0, %0\n\ts_nop 0\n\tbuffer_load_dwordx4 %1, %2, %3 offen lds" :: "s"(ldsaddr), "v"(voff), "s"(rs), "s"(soff) : "memory");
}

DI void touch(unsigned ldsaddr, int voff, __amdgpu_buffer_rsrc_t rs) {
  asm volatile("s_mov_b32 m0, %0\n\ts_nop 0\n\tbuffer_load_dword %1, %2, 0 offen lds" :: "s"(ldsaddr), "v"(voff), "s"(rs) : "memory");
}
#define UNLIKELY(c) __builtin_expect(!!(c), 0)

__global__ __launch_bounds__(512, 2) void dit_kernel(const u16* __restrict__ x, const u16* __restrict__ c, const u16* __restrict__ wqkv,
                                                     const u16* __restrict__ wo, const u16* __restrict__ wmod, unsigned char* ws,
                                                     u16* __restrict__ out) {
  extern __shared__ __attribute__((aligned(16))) unsigned char lds_[];
  lp lds = (lp)lds_;
  const unsigned ldsb = (unsigned)(size_t)lds;
  STDECL;
  STAMP(0);
  const unsigned long long pc0 = __builtin_amdgcn_s_getpc();
  const int tid = threadIdx.x, lane = tid & 63, w = __builtin_amdgcn_readfirstlane(tid >> 6);
  const int n16 = lane & 15, g = lane >> 4;
  const int bid = blockIdx.x, xg = bid & 7, r = bid >> 3;
  const int b = xg >> 2, ql = r & 7, kb = r >> 3;
  const int qt = b * 32 + (xg & 3) * 8 + ql;
  const int qt3 = b * 32 + (((xg & 3) + XS) & 3) * 8 + ql;
  const bool act1 = ql < 6;
  const int cb = (xg & 3) * 6 + (act1 ? ql : ql - 2);
  const __amdgpu_buffer_rsrc_t rws = __builtin_amdgcn_make_buffer_rsrc(ws, 0, WS_BYTES, 0x00020000);
  const __amdgpu_buffer_rsrc_t rx = __builtin_amdgcn_make_buffer_rsrc((void*)x, 0, 2048 * 768, 0x00020000);
  const int dch = 16 * ((lane & 3) ^ ((lane & 32) ? 3 : 0));
  const int rslot = (4 * n16 + (g ^ ((n16 & 8) ? 3 : 0))) * 16;
  const int l63 = lane < RL ? lane * 16 : OOB;

  const int mu = (((r - 24) >> 1) * 8 + xg) * 2 + (r & 1);
  const int role = r < 12 ? 0 : ((r >= 24 && mu < 36) ? 2 : 1);
  const int dead0 = (r >= 24 && mu >= 36) ? OOB : 0;
  const int ru = r < 12 ? r : (r < 24 ? r - 12 : r - 24);
  const int u12 = 6 * (xg & 1) + ru % 6, u8 = 2 * (xg >> 1) + ru / 6;
  unsigned ep;
  asm volatile("s_nop 4\n\tbuffer_load_dword %0, off, %1, 0 sc1" : "=v"(ep) : "s"(rws) : "memory");
  if (w < 6) {
    unsigned cv = 0;
    if (role == 2) cv = ((const unsigned*)c)[tid];
    if (role < 2) {
      const __amdgpu_buffer_rsrc_t rq = __builtin_amdgcn_make_buffer_rsrc((void*)wqkv, 0, 384 * 2304, 0x00020000);
      const int v = (lane >> 2) * 2304 + dch;
      const int s3 = 48 * u8 * 2304 + (role ? 1536 : 768) + 128 * w;
#pragma unroll
      for (int t = 0; t < 3; ++t) {
        dma(ldsb + L_P0 + (t * 12 + 2 * w) * 1024, v, rq, s3 + 16 * t * 2304);
        dma(ldsb + L_P0 + (t * 12 + 2 * w + 1) * 1024, v, rq, s3 + 16 * t * 2304 + 64);
      }
      if (role == 0) {
        const int s2 = 32 * u12 * 2304 + 128 * w;
#pragma unroll
        for (int t = 0; t < 2; ++t) {
          dma(ldsb + L_P0 + ((3 + t) * 12 + 2 * w) * 1024, v, rq, s2 + 16 * t * 2304);
          dma(ldsb + L_P0 + ((3 + t) * 12 + 2 * w + 1) * 1024, v, rq, s2 + 16 * t * 2304 + 64);
        }
      }
    }
    if (role == 1 || role == 2) {
      const int ld = role == 1 ? 768 : 4608;
      const __amdgpu_buffer_rsrc_t rc = __builtin_amdgcn_make_buffer_rsrc((void*)(role == 1 ? wo : wmod), 0, 384 * ld, 0x00020000);
      const int v = (lane >> 2) * ld + dch;
#pragma unroll
      for (int j = 0; j < 4; ++j) dma(ldsb + L_P0 + (36 + 4 * w + j) * 1024, v, rc, (role == 1 ? u12 : mu) * 64 + (4 * w + j) * 16 * ld);
    }
    if (role == 2) {
      const float f0 = bflo(cv), f1 = bfhi(cv);
      const int bb = tid >= 192, i2 = 2 * (tid - 192 * bb);
      *(LAS unsigned*)(lds + L_P0 + (i2 >> 5) * 1024 + (bb * 4 + ((i2 >> 3) & 3)) * 16 + (i2 & 7) * 2) =
          cvtpk(f0 * __builtin_amdgcn_rcpf(1.f + __expf(-f0)), f1 * __builtin_amdgcn_rcpf(1.f + __expf(-f1)));
    }
    VMWAIT(0);
  } else {
    __builtin_amdgcn_s_setprio(3);
    v4u xs[3];
    asm volatile("buffer_load_dwordx4 %0, %3, %4, 0 offen\n\tbuffer_load_dwordx4 %1, %3, %4, 0 offen offset:256\n\tbuffer_load_dwordx4 %2, %3, %4, 0 offen offset:512"
                 : "=&v"(xs[0]), "=&v"(xs[1]), "=&v"(xs[2]) : "v"((32 * (bid >> 2) + 16 * (bid & 1) + 8 * (w & 1) + 2 * (bid & 2) + (lane & 3)) * 768 + (lane >> 2) * 16), "s"(rx) : "memory");
    {
      const unsigned scr = ldsb + L_SCR;
      const int dead = ((TOUCH & 1) && lane < 36) ? 0 : OOB;
      const __amdgpu_buffer_rsrc_t rq = __builtin_amdgcn_make_buffer_rsrc((void*)wqkv, 0, 384 * 2304, 0x00020000);
      touch(scr, dead + (w == 6 ? (96 * (xg >> 1) + 3 * r + lane / 12) * 2304 + 768 + (lane % 12) * 128 : (192 * (xg & 1) + 6 * r + lane / 6) * 2304 + (lane % 6) * 128), rq);
      const __amdgpu_buffer_rsrc_t r2 = __builtin_amdgcn_make_buffer_rsrc(w == 6 ? (void*)wo : (void*)(pc0 & ~127ull), 0, w == 6 ? 384 * 768 : CODEPF, 0x00020000);
      touch(scr, w == 6 ? dead + (12 * r + lane / 3) * 768 + 384 * (xg & 1) + (lane % 3) * 128 : (lane < 5 ? (r * 5 + lane) * 128 : OOB), r2);
    }
    if (w == 7) {
      const __amdgpu_buffer_rsrc_t r3 = __builtin_amdgcn_make_buffer_rsrc((void*)wmod, 0, 384 * 4608, 0x00020000);
      touch(ldsb + L_SCR, (lane < (xg < 2 ? 36 : 24) ? 0 : OOB) + (12 * r + lane % 12) * 4608 + ((lane / 12) * 8 + xg) * 128, r3);
    }
    else touch(ldsb + L_SCR, OOB, rx);
    asm volatile("s_waitcnt vmcnt(3)" : "+v"(xs[0]), "+v"(xs[1]), "+v"(xs[2]) :: "memory");
    if (PREWR & 2) ST128((v4u{0u, 0u, 0u, 0u}), l63 + O_RN + (bid * 2 + (w & 1)) * 128);
    float f[24], sm = 0.f, q = 0.f;
#pragma unroll
    for (int i = 0; i < 12; ++i) { f[2 * i] = bflo(xs[i >> 2][i & 3]); f[2 * i + 1] = bfhi(xs[i >> 2][i & 3]); sm += f[2 * i] + f[2 * i + 1]; }
    sm = qsumf(sm) * (1.f / 384.f);
#pragma unroll
    for (int i = 0; i < 24; ++i) { f[i] -= sm; q = fmaf(f[i], f[i], q); }
    const float rs = __builtin_amdgcn_rsqf(qsumf(q) * (1.f / 384.f) + 1e-5f);
#pragma unroll
    for (int i = 0; i < 3; ++i)
      LDS128(lds, L_BN + ((lane >> 2) + 16 * i) * 128 + ((w & 1) * 4 + (lane & 3)) * 16) =
          v4u{cvtpk(f[8 * i] * rs, f[8 * i + 1] * rs), cvtpk(f[8 * i + 2] * rs, f[8 * i + 3] * rs), cvtpk(f[8 * i + 4] * rs, f[8 * i + 5] * rs), cvtpk(f[8 * i + 6] * rs, f[8 * i + 7] * rs)};
  }
  asm volatile("" : "+v"(ep));
  const unsigned tag = __builtin_amdgcn_readfirstlane(ep) + 1u;
  STAMP(2);
  BAR();
  STAMP(3);
  if (w >= 6) {
    v4u nv[3];
#pragma unroll
    for (int i = 0; i < 3; ++i) nv[i] = LDS128(lds, L_BN + (w & 1) * 3072 + i * 1024 + lane * 16);
    const int no = O_NF + (bid >> 1) * 12288 + ((w & 1) * 24 + (lane >> 3)) * 256 + (bid & 1) * 128 + (lane & 7) * 16;
#pragma unroll
    for (int i = 0; i < 3; ++i) ST128(nv[i], no + i * 2048);
    RST(wsum(sum4(sum4(sum4(0u, nv[0]), nv[1]), nv[2])), l63 + O_RN + (bid * 2 + (w & 1)) * 128);
    __builtin_amdgcn_s_setprio(0);
  }
  const int rg = b * 32 + kb * 8 + w;
  const int nro = lane < 8 ? O_RN + (rg * 8 + lane) * 128 : OOB;
  if (w < (role == 2 ? 2 : 6)) {
    const int t3 = w >> 1, t2 = w & 1;
    f32x4 acc = {};
    v4u a[12], bq[12];
    if (role == 0) {
      lp ap = opq(lds + L_P0 + (3 + t2) * 12288 + rslot), bp = opq(lds + L_P0 + t3 * 12288 + rslot);
#pragma unroll
      for (int k = 0; k < 12; ++k) {
        a[k] = LDS128(ap, k * 1024); bq[k] = LDS128(bp, k * 1024);
        if (k >= 5) acc = mfma(a[k - 5], bq[k - 5], acc);
        SB();
      }
    } else {
      lp ap = opq(lds + L_P0 + (role == 2 ? ((n16 & 1) * 4 + g) * 16 : t3 * 12288 + rslot));
      lp tb = opq(lds + L_P0 + 36864 + (8 * g + (n16 >> 2)) * 64 + 16 * ((2 * t2 + ((n16 >> 1) & 1)) ^ ((g & 1) ? 3 : 0)) + 8 * (n16 & 1));
#pragma unroll
      for (int k = 0; k < 12; ++k) {
        a[k] = LDS128(ap, k * 1024);
        bq[k] = __builtin_bit_cast(v4u, __builtin_shufflevector(__builtin_amdgcn_ds_read_tr16_b64_v4i16((LAS s16x4*)(tb + k * 2048)),
                                                                __builtin_amdgcn_ds_read_tr16_b64_v4i16((LAS s16x4*)(tb + k * 2048 + 256)), 0, 1, 2, 3, 4, 5, 6, 7));
        if (k >= 5) acc = mfma(a[k - 5], bq[k - 5], acc);
        SB();
      }
    }
#pragma unroll
    for (int k = 7; k < 12; ++k) acc = mfma(a[k], bq[k], acc);
    if (role != 2) {
      const int blk = role * 576 + (role == 0 ? (3 * u8 + t3) * 24 + 2 * u12 + t2 : (2 * u12 + t2) * 24 + 3 * u8 + t3);
      const unsigned o0 = cvtpk(acc[0], acc[1]), o1 = cvtpk(acc[2], acc[3]);
      auto r0 = __builtin_amdgcn_permlane16_swap(o0, o0, false, false);
      auto r1 = __builtin_amdgcn_permlane16_swap(o1, o1, false, false);
      ST128((v4u{r0[0], r1[0], r0[1], r1[1]}), (((g & 1) ? OOB : 0) | dead0) + O_MT + blk * 512 + ((g >> 1) * 16 + n16) * 16);
      RST(wsum(o0 + o1), (l63 | dead0) + O_RM + blk * 128);
    } else if (g < 2) {
      float v = bfround(g ? acc[1] : acc[0]);
      if (mu >= 12 && mu < 24) v += 1.f;
      ST32(tagged(v, tag & 0xffffu), O_MOD + (2 * mu + t2) * 128 + lane * 4);
    }
  }
  STAMP(4);
  __builtin_amdgcn_s_barrier();
  if (DLY1) __builtin_amdgcn_s_sleep(DLY1);
  v2u nr = LD64(nro);
  SB();
  const int l16 = opq(lane * 16);
  const int s_n = O_NF + rg * 24576;
  const int rot = ROT ? (ql * 3) >> 1 : 0;
  const int l16b = opq(lane * 16 + 12288);
  v4u xf[2][12];
#define XF(i) xf[(i) & 1][(i) >> 1]
#define XLOAD(i) XF(i) = LD128(((i) & 1) ? l16b : l16, s_n + (((i) >> 1) + rot - (((i) >> 1) + rot >= 12 ? 12 : 0)) * 1024)
  lp fl = opq(lds + L_MS + lane * 16);
  v4u qf[3]; unsigned qr;
  const int qro = opq(lane < 3 ? O_RQ + (qt * 24 + 3 * w + lane) * 128 : OOB);
  const int s_ql = sopq(O_QF + (qt * 24 + 3 * w) * 1024);
#define HOP2_LOADS() do { _Pragma("unroll") for (int i = 0; i < 3; ++i) qf[i] = LD128(l16 + i * 1024, s_ql); qr = LD32(qro); } while (0)
  {
    const int s0 = 3 * (w & 3);
    const int mc4 = lane < 24 ? 96 + 8 * s0 + lane : (lane < 48 ? 8 * s0 + lane - 24 : (w < 4 ? 96 : 192) + 4 * cb + (lane & 3));
    const int mo = opq(O_MOD + (mc4 >> 2) * 128 + b * 64 + (mc4 & 3) * 16);
    const int fb = (w >> 2) * 576 + cb * 24 + 2 * s0;
    const int ro = opq(lane < 6 ? O_RM + (fb + lane) * 128 : OOB);
    const int s_f = sopq(O_MT + fb * 512);
    const int blk1 = rg * 24 + cb, dead1 = act1 ? 0 : OOB;
    const int qso = opq(dead1 + O_QF + blk1 * 1024 + ((g & 1) * 32 + (g >> 1) * 16 + n16) * 16);
    const int vso = opq(dead1 + O_VF + blk1 * 1024 + lane * 16);
    const int rso = opq((dead1 | l63) + O_RQ + blk1 * 128);
    const int rvo = opq((dead1 | l63) + O_RV + (((b * 4 + kb) * 24 + cb) * 8 + w) * 128);
    lp cg = opq(lds + L_CS + g * 16), cn = opq(lds + L_CS + 256 + n16 * 4), csw = opq(lds + L_CS + w * 64 + n16 * 4);
    lp bn = lds + L_BN + w * 768, bg = opq(bn + g * 32), msw[3];
#pragma unroll
    for (int i = 0; i < 3; ++i) { int sl = s0 + i - rot; if (sl < 0) sl += 12; msw[i] = opq(fl + (w >> 2) * 12288 + sl * 1024); }
    SB();
    if (UNLIKELY(!__all(lane >= 8 || nr[1] == tag))) {
      unsigned it = 0;
      do { RETRY(0); __builtin_amdgcn_s_sleep(2); CBAR(); nr = LD64(nro); } while (!__all(lane >= 8 || nr[1] == tag) && ++it < SPIN_CAP);
    }
    if (GATE == 2) __builtin_amdgcn_s_barrier();
    STAMP(5);
    unsigned hx = 0;
    v4u mf[3], mv4; unsigned mr;
    f32x4 sa[3], sb[3]; v4u tf[3]; float oc_;
#define MODOK() __all((tchk(0u, mv4, tag) & 0xffffu) == 0u)
#define HOP1_LOADS() do { _Pragma("unroll") for (int i = 0; i < 3; ++i) mf[i] = LD128(l16 + i * 1024, s_f); mr = LD32(ro); } while (0)
#pragma unroll
    for (int i = 0; i < NX1; ++i) {
      XLOAD(i);
      if (i >= HL) hx = sum4(hx, XF(i - HL));
      if (i == MP) mv4 = LD128(mo, 0);
      if (i == HP) HOP1_LOADS();
      if (i == NX1 - 1) nr = LD64(nro);
      if (i == BA && w >= 4) __builtin_amdgcn_s_barrier();
      if (i == MC) {
        if (UNLIKELY(!MODOK())) {
          unsigned it = 0;
          do { RETRY(1); __builtin_amdgcn_s_sleep(8); CBAR(); mv4 = LD128(mo, 0); } while (!MODOK() && ++it < SPIN_CAP);
        }
        if (lane >= 24 && lane < 48) *(LAS v2u*)(bn + 512 + (lane - 24) * 8) = v2u{pkhi(mv4[0], mv4[1]), pkhi(mv4[2], mv4[3])};
        else LDS128(bn, (lane < 24 ? lane : (lane & 3) + 24) * 16) = mv4 & 0xffff0000u;
        CBAR();
#pragma unroll
        for (int k = 0; k < 3; ++k) { sa[k] = LDSF4(bg, 128 * k); sb[k] = LDSF4(bg, 128 * k + 16); tf[k] = LDS128(bn, 512 + 64 * k + 16 * g); }
        oc_ = *(LAS float*)(bn + 384 + n16 * 4);
      }
      if (i == MC + 1) { if (w < 4) oc_ *= 0.05103103630798288f * 1.4426950408889634f; sa[0] = opq(sa[0] * oc_); sb[0] = opq(sb[0] * oc_); }
      if (i == MC + 2) { sa[1] = opq(sa[1] * oc_); sb[1] = opq(sb[1] * oc_); }
      if (i == MC + 3) { sa[2] = opq(sa[2] * oc_); sb[2] = opq(sb[2] * oc_); }
      asm volatile("" : "+v"(hx));
      SB();
    }
    STAMP(6);
#define FBAD() (wsum(sum4(sum4(sum4(-mr, mf[0]), mf[1]), mf[2])) + 6u * tag != 0u)
    if (UNLIKELY(FBAD())) {
      unsigned it = 0;
      do { RETRY(9); __builtin_amdgcn_s_sleep(6); CBAR(); HOP1_LOADS(); } while (FBAD() && ++it < SPIN_CAP);
    }
    STAMP(7);
    {
      f32x4 au = {};
#pragma unroll
      for (int i = 0; i < 3; ++i) {
        au = mfma(tf[i], mf[i], au);
        const v4u o = {cvtpk(bflo(mf[i][0]) * sa[i][0], bfhi(mf[i][0]) * sa[i][1]), cvtpk(bflo(mf[i][1]) * sa[i][2], bfhi(mf[i][1]) * sa[i][3]),
                       cvtpk(bflo(mf[i][2]) * sb[i][0], bfhi(mf[i][2]) * sb[i][1]), cvtpk(bflo(mf[i][3]) * sb[i][2], bfhi(mf[i][3]) * sb[i][3])};
        LDS128(msw[i], 0) = o;
        if (NX1 + i < 24) XLOAD(NX1 + i);
        hx = opq(sum4(hx, XF(NX1 - HL + i)));
        SB();
      }
      *(LAS float*)(csw) = au[0] * oc_;
    }
    STAMP(9);
    __builtin_amdgcn_s_setprio(2);
    BAR();
    v4u a[24];
    f32x4 uu[4]; float zz[4], zs;
#define AOP(k) do { if ((k) + PD < ((k) < 12 ? 12 : 24)) a[(k) + PD] = LDS128(fl, ((k) + PD) * 1024); \
      if ((k) < 12) { aq[0] = mfma(a[k], xf[0][(k) % 12], aq[0]); aq[1] = mfma(a[k], xf[1][(k) % 12], aq[1]); } \
      else { av[0] = mfma(xf[0][(k) % 12], a[k], av[0]); av[1] = mfma(xf[1][(k) % 12], a[k], av[1]); } } while (0)
#pragma unroll
    for (int k = 0; k < PD; ++k) a[k] = LDS128(fl, k * 1024);
    SB();
    f32x4 aq[2] = {}, av[2] = {};
#pragma unroll
    for (int k = 0; k < 12; ++k) {
      if (NX1 + 3 + k < 24) XLOAD(NX1 + 3 + k);
      if (k >= 2 && k < 6) uu[k - 2] = LDSF4(cg, (k - 2) * 64);
      AOP(k);
      if (2 * k >= NX1 - HL + 3) hx = sum4(hx, XF(2 * k));
      if (2 * k + 1 >= NX1 - HL + 3) hx = sum4(hx, XF(2 * k + 1));
      if (k == 9) { uu[0] = uu[0] + uu[1]; uu[2] = uu[2] + uu[3]; }
      if (k == 10) uu[0] = uu[0] + uu[2];
      asm volatile("" : "+v"(hx));
      SB();
    }
    if (UNLIKELY(wsum(hx - nr[0]) + 8u * tag != 0u)) {
      unsigned it = 0;
      do { RETRY(4); __builtin_amdgcn_s_sleep(4); CBAR();
        hx = -LD32(nro);
#pragma unroll
        for (int i = 0; i < 24; ++i) XLOAD(i);
#pragma unroll
        for (int i = 0; i < 24; ++i) hx = sum4(hx, XF(i));
      } while (wsum(hx) + 8u * tag != 0u && ++it < SPIN_CAP);
      aq[0] = aq[1] = f32x4{0.f, 0.f, 0.f, 0.f};
#pragma unroll
      for (int s = 0; s < 12; ++s) {
        const v4u a0 = LDS128(fl, s * 1024);
        aq[0] = mfma(a0, xf[0][s], aq[0]); aq[1] = mfma(a0, xf[1][s], aq[1]);
      }
    }
    {
      const f32x4 v0 = aq[0] + uu[0], v1 = aq[1] + uu[0];
      auto r0 = __builtin_amdgcn_permlane16_swap(cvtpk(v0[0], v0[1]), cvtpk(v1[0], v1[1]), false, false);
      auto r1 = __builtin_amdgcn_permlane16_swap(cvtpk(v0[2], v0[3]), cvtpk(v1[2], v1[3]), false, false);
      const v4u ov = {r0[0], r1[0], r0[1], r1[1]};
      ST128(ov, qso);
      RST(wsum(sum4(0u, ov)), rso);
      __builtin_amdgcn_s_setprio(0);
      STAMP(12);
      if (w < 4) BAR();
      CBAR();
#pragma unroll
      for (int k = 12; k < 12 + PD; ++k) a[k] = LDS128(fl, k * 1024);
      SB();
#pragma unroll
      for (int k = 12; k < 24; ++k) {
        if (k >= 14 && k < 18) zz[k - 14] = *(LAS float*)(cn + (k - 14) * 64);
        AOP(k);
        if (k == 21) zs = (zz[0] + zz[1]) + (zz[2] + zz[3]);
        SB();
      }
      const f32x4 w0 = av[0] + zs, w1 = av[1] + zs;
      const v4u o = {cvtpk(w0[0], w0[1]), cvtpk(w0[2], w0[3]), cvtpk(w1[0], w1[1]), cvtpk(w1[2], w1[3])};
      ST128(o, vso);
      RST(wsum(sum4(0u, o)), rvo);
    }
    STAMP(10);
    SB();
  }
  STAMP(11);
  if (w == 7) ST128((v4u{tag, tag, tag, tag}), l63 + O_CL + bid * 128);
  const int vro = opq(lane < 24 ? O_RV + (((b * 4 + kb) * 8 + w) * 24 + lane) * 128 : OOB);
  const int mso = opq((w == 0 && g < 2) ? O_ML + ((qt * 4 + kb) * 32 + lane) * 16 : OOB);
  const int s_vl = sopq(O_VF + ((b * 32 + 8 * kb) * 24 + 3 * w) * 1024);
  const bool own = (w >> 1) == kb;
  const int oso = opq((own ? OOB : 0) + O_OF + ((qt * 24 + 3 * w) * 4 + kb) * 1024 + ((g & 1) * 32 + (g >> 1) * 16 + n16) * 16);
  const int roo = opq(((own ? OOB : 0) | l63) + O_RO + ((qt * 24 + 3 * w) * 4 + kb) * 128);
  lp mxr = opq(lds + L_MX + n16 * 32), mxw = opq(lds + L_MX + n16 * 32 + w * 4);
  lp plw = opq(lds + lane * 16 + w * 2048);
  lp pl = opq(lds + lane * 16);
  const int ooff = opq(((qt * 32 + 8 * (n16 >> 2) + 4 * (lane >> 5) + (n16 & 3)) * 384 + (6 * kb + w) * 16 + 8 * (g & 1)) * 2);
  const v4u xr = __builtin_amdgcn_raw_buffer_load_b128(rx, w < 6 ? ooff : OOB, 0, 0);
  const int mlo = opq(O_ML + (qt * 128 + (lane >> 5) * 16 + n16) * 16);
  const int s_ol = sopq(O_OF + (qt * 24 + 6 * kb + w) * 4096), rlo = opq((lane < 4 && lane != kb) ? O_RO + ((qt * 24 + 6 * kb + w) * 4 + lane) * 128 : OOB);
  if (PREWR & 1) {
    const unsigned nt_ = ~tag;
    ST128((v4u{nt_, nt_, nt_, nt_}), lane < 24 ? O_RO + ((qt * 24 + 3 * w + (lane >> 3)) * 4 + kb) * 128 + (lane & 7) * 16 : ((w == 0 && lane >= 32) ? O_ML + ((qt * 4 + kb) * 32 + lane - 32) * 16 : OOB));
  }
  v4u vf[8][3];
#define VF(i) vf[(i) & 7][(i) >> 3]
#define VLOAD(i) VF(i) = LD128(l16 + ((i) >> 3) * 1024, s_vl + ((i) & 7) * 24576)
  {
    lp qw = opq(lds + L_QS + (lane >> 5) * 12288 + 3 * w * 512 + (lane & 31) * 16);
    SB();
    __builtin_amdgcn_s_barrier();
    if (DLY2) __builtin_amdgcn_s_sleep(DLY2);
    HOP2_LOADS();
#pragma unroll
    for (int i = 0; i < NV0; ++i) VLOAD(i);
    SB();
#define QBAD() (wsum(sum4(sum4(sum4(-qr, qf[0]), qf[1]), qf[2])) + 3u * tag != 0u)
#pragma unroll
    for (int i = 0; i < 3; ++i) LDS128(qw, i * 512) = qf[i];
    if (UNLIKELY(QBAD())) {
      unsigned it = 0;
      do { RETRY(2); __builtin_amdgcn_s_sleep(3); CBAR(); HOP2_LOADS(); } while (QBAD() && ++it < SPIN_CAP);
#pragma unroll
      for (int i = 0; i < 3; ++i) LDS128(qw, i * 512) = qf[i];
    }
  }
  STAMP(13);
  BAR();
  float m[2]; unsigned vr, hv0 = 0;
  {
    v4u q[24];
#define QREAD(i) q[i] = LDS128(fl, 24576 + (((i) & 1) * 12 + ((i) >> 1)) * 1024)
#pragma unroll
    for (int i = 0; i < PD; ++i) QREAD(i);
    SB();
    f32x4 acc[2][2] = {};
#pragma unroll
    for (int i = 0; i < 24; ++i) {
      if (i + PD < 24) QREAD(i + PD);
      acc[i & 1][0] = mfma(xf[0][i >> 1], q[i], acc[i & 1][0]); acc[i & 1][1] = mfma(xf[1][i >> 1], q[i], acc[i & 1][1]);
      if ((i * NVS) / 24 != ((i + 1) * NVS) / 24) VLOAD(NV0 + (i * NVS) / 24);
      if (i >= 24 - NH) hv0 = opq(sum4(hv0, VF(i - (24 - NH))));
      SB();
    }
#pragma unroll
    for (int u = 0; u < 2; ++u) {
      float mx = max3(max3(acc[u][0][0], acc[u][0][1], acc[u][0][2]), acc[u][0][3], acc[u][1][0]);
      mx = max_g(max2(max3(mx, acc[u][1][1], acc[u][1][2]), acc[u][1][3]));
      *(LAS float*)(mxw + u * 512) = mx;
    }
    STAMP(14);
    BAR();
    f32x4 ma[2], mb[2];
#pragma unroll
    for (int u = 0; u < 2; ++u) { ma[u] = LDSF4(mxr, u * 512); mb[u] = LDSF4(mxr, u * 512 + 16); }
    vr = LD32(vro);
    SB();
#pragma unroll
    for (int u = 0; u < 2; ++u) {
      m[u] = max2(max3(max3(ma[u][0], ma[u][1], ma[u][2]), ma[u][3], mb[u][0]), max3(mb[u][1], mb[u][2], mb[u][3]));
      float p[8];
#pragma unroll
      for (int i = 0; i < 8; ++i) p[i] = __builtin_amdgcn_exp2f(acc[u][i >> 2][i & 3] - m[u]);
      LDS128(plw, u * 1024) = v4u{cvtpk(p[0], p[1]), cvtpk(p[2], p[3]), cvtpk(p[4], p[5]), cvtpk(p[6], p[7])};
      if (NV0 + NVS + u < NV2) VLOAD(NV0 + NVS + u);
      SB();
    }
#pragma unroll
    for (int i = NV0 + NVS + 2; i < NV2; ++i) VLOAD(i);
  }
  STAMP(15);
  BAR();
  v4u ml[4];
#define ML_LOADS() do { _Pragma("unroll") for (int k = 0; k < 4; ++k) ml[k] = LD128(mlo, ((kb + k) & 3) * 512); } while (0)
  {
    v4u pf[16];
    f32x4 acc[3][2] = {};
    v4u ov[3];
    unsigned hv[3];
#define OSTORE(et) do { const f32x4 v0 = acc[et][0], v1 = acc[et][1]; \
      auto r0 = __builtin_amdgcn_permlane16_swap(cvtpk(v0[0], v0[1]), cvtpk(v1[0], v1[1]), false, false); \
      auto r1 = __builtin_amdgcn_permlane16_swap(cvtpk(v0[2], v0[3]), cvtpk(v1[2], v1[3]), false, false); \
      ov[et] = v4u{r0[0], r1[0], r0[1], r1[1]}; ST128S(ov[et], oso, (et) * 4096); \
      if (own) LDS128(lds + 16384 + ((w & 1) * 3 + (et)) * 1024, ((g & 1) * 32 + (g >> 1) * 16 + n16) * 16) = ov[et]; } while (0)
#define ORECORD(et) RST(wsum(sum4(0u, ov[et])), roo + (et) * 512)
#define VCHECK(et) do { if (UNLIKELY(wsum(hv[et] - ((lane >> 3) == (et) ? vr : 0u)) + 8u * tag != 0u)) { unsigned it = 0; \
        do { RETRY(3); __builtin_amdgcn_s_sleep(2); CBAR(); \
          _Pragma("unroll") for (int i = 0; i < 8; ++i) VLOAD(8 * (et) + i); \
          vr = LD32(vro); hv[et] = 0; \
          _Pragma("unroll") for (int i = 0; i < 8; ++i) hv[et] = sum4(hv[et], VF(8 * (et) + i)); \
        } while (wsum(hv[et] - ((lane >> 3) == (et) ? vr : 0u)) + 8u * tag != 0u && ++it < SPIN_CAP); \
        acc[et][0] = acc[et][1] = f32x4{0.f, 0.f, 0.f, 0.f}; \
        _Pragma("unroll") for (int i = 0; i < 16; ++i) acc[et][i & 1] = mfma(vf[i >> 1][et], pf[i], acc[et][i & 1]); } } while (0)
#pragma unroll
    for (int i = 0; i < 8; ++i) pf[i] = LDS128(pl, i * 1024);
    SB();
    hv[0] = hv0; hv[1] = hv[2] = 0;
#pragma unroll
    for (int i = 0; i < 16; ++i) {
      if (i + 8 < 16) pf[i + 8] = LDS128(pl, (i + 8) * 1024);
      if ((i & 1) && NV2 + (i >> 1) < 24) VLOAD(NV2 + (i >> 1));
      acc[0][i & 1] = mfma(vf[i >> 1][0], pf[i], acc[0][i & 1]);
      if ((i & 1) && (i >> 1) >= NH) hv[0] = opq(sum4(hv[0], vf[i >> 1][0]));
      if (!(i & 1)) hv[1] = opq(sum4(hv[1], vf[i >> 1][1]));
      SB();
    }
    if (w == 0) {
      const v4u ones = {0x3f803f80u, 0x3f803f80u, 0x3f803f80u, 0x3f803f80u};
      f32x4 ls[2] = {};
#pragma unroll
      for (int i = 0; i < 16; ++i) ls[i & 1] = mfma(ones, pf[i], ls[i & 1]);
      ST128((v4u{__float_as_uint(g ? m[1] : m[0]), tag, __float_as_uint(g ? ls[1][0] : ls[0][0]), tag}), mso);
    }
    VCHECK(0);
#pragma unroll
    for (int i = 0; i < 16; ++i) {
      acc[1][i & 1] = mfma(vf[i >> 1][1], pf[i], acc[1][i & 1]);
      if (i == 0) OSTORE(0);
      if (i == 6) ORECORD(0);
      if ((i & 3) == 3) SB();
    }
    VCHECK(1);
#pragma unroll
    for (int i = 0; i < 16; ++i) {
      acc[2][i & 1] = mfma(vf[i >> 1][2], pf[i], acc[2][i & 1]);
      if (i & 1) hv[2] = opq(sum4(hv[2], vf[i >> 1][2]));
      if (i == 0) OSTORE(1);
      if (i == 6) ORECORD(1);
      if (i == MLP) ML_LOADS();
      if ((i & 3) == 3) SB();
    }
    VCHECK(2);
    STAMP(16);
    OSTORE(2);
    ORECORD(2);
  }
  STAMP(17);
  BAR();
  if (w < 6) {
    v4u pp[4]; unsigned orr;
#define MLOK() __all(((ml[0][1] ^ tag) | (ml[1][1] ^ tag) | (ml[2][1] ^ tag) | (ml[3][1] ^ tag) | (ml[0][3] ^ tag) | (ml[1][3] ^ tag) | (ml[2][3] ^ tag) | (ml[3][3] ^ tag)) == 0u)
#define HOP3_LOADS() do { _Pragma("unroll") for (int k = 1; k < 4; ++k) pp[k] = LD128(l16, s_ol + ((kb + k) & 3) * 1024); orr = LD32(rlo); } while (0)
#define OBAD() (wsum(sum4(sum4(sum4(-orr, pp[1]), pp[2]), pp[3])) + 3u * tag != 0u)
    pp[0] = LDS128(pl, 16384 + w * 1024);
    SB();
    if (DLY3) __builtin_amdgcn_s_sleep(DLY3);
    if (UNLIKELY(!MLOK())) {
      unsigned it = 0;
      RETRY(6); WAITREC(rlo);
      do { CBAR(); ML_LOADS(); } while (!MLOK() && ++it < SPIN_CAP);
    }
    HOP3_LOADS();
    SB();
    STAMP(18);
    const float mm = max2(max2(__uint_as_float(ml[0][0]), __uint_as_float(ml[1][0])), max2(__uint_as_float(ml[2][0]), __uint_as_float(ml[3][0])));
    float wk[4], o[8];
#pragma unroll
    for (int k = 0; k < 4; ++k) wk[k] = __builtin_amdgcn_exp2f(__uint_as_float(ml[k][0]) - mm);
    const float inv = __builtin_amdgcn_rcpf((wk[0] * __uint_as_float(ml[0][2]) + wk[1] * __uint_as_float(ml[1][2])) + (wk[2] * __uint_as_float(ml[2][2]) + wk[3] * __uint_as_float(ml[3][2])));
#pragma unroll
    for (int k = 0; k < 4; ++k) wk[k] *= inv;
#pragma unroll
    for (int e = 0; e < 4; ++e) { o[2 * e] = opq(fmaf(wk[0], bflo(pp[0][e]), bflo(xr[e]))); o[2 * e + 1] = opq(fmaf(wk[0], bfhi(pp[0][e]), bfhi(xr[e]))); }
    SB();
    if (UNLIKELY(OBAD())) {
      unsigned it = 0;
      do { RETRY(5); WAITREC(rlo); HOP3_LOADS(); } while (OBAD() && ++it < SPIN_CAP);
    }
    STAMP(1);
    v4u ov;
#pragma unroll
    for (int e = 0; e < 4; ++e) {
#pragma unroll
      for (int k = 1; k < 4; ++k) { o[2 * e] = fmaf(wk[k], bflo(pp[k][e]), o[2 * e]); o[2 * e + 1] = fmaf(wk[k], bfhi(pp[k][e]), o[2 * e + 1]); }
      ov[e] = cvtpk(o[2 * e], o[2 * e + 1]);
    }
    *(v4u*)((unsigned char*)out + ooff) = ov;
  } else if (UNLIKELY(bid == 255 && w == 7)) {
#pragma unroll 1
    for (unsigned it = 0; it < SPIN_CAP; ++it) {
      CBAR();
      unsigned bad = 0;
#pragma unroll
      for (int k = 0; k < 4; ++k) bad |= LD32(O_CL + (k * 64 + lane) * 128) ^ tag;
      if (__all(bad == 0)) break;
    }
    if (lane == 0) __hip_atomic_store((gu32*)(ws + O_EPOCH), tag, __ATOMIC_RELAXED, __HIP_MEMORY_SCOPE_AGENT);
  }
  STAMP(19);
  STFLUSH();
}

extern "C" void kernel_launch(void* const* d_in, const int* in_sizes, int n_in, void* d_out, int out_size, void* d_ws, size_t ws_size,
                              hipStream_t stream) {
  (void)hipFuncSetAttribute((const void*)dit_kernel, hipFuncAttributeMaxDynamicSharedMemorySize, L_TOTAL);
  dit_kernel<<<256, 512, L_TOTAL, stream>>>((const u16*)d_in[0], (const u16*)d_in[1], (const u16*)d_in[2], (const u16*)d_in[3],
                                            (const u16*)d_in[4], (unsigned char*)d_ws, (u16*)d_out);
}
```

```cpp
#include <hip/hip_runtime.h>
#include <hip/hip_bf16.h>

typedef short bf16x8 __attribute__((ext_vector_type(8)));
typedef short s16x4 __attribute__((ext_vector_type(4)));
typedef float f32x4 __attribute__((ext_vector_type(4)));
typedef float f32x2 __attribute__((ext_vector_type(2)));
typedef unsigned v4u __attribute__((ext_vector_type(4)));
typedef unsigned v2u __attribute__((ext_vector_type(2)));
typedef __bf16 bf2 __attribute__((ext_vector_type(2)));
typedef unsigned short u16;
#define DI __device__ __forceinline__
#define LAS __attribute__((address_space(3)))
#define GAS __attribute__((address_space(1)))
typedef GAS unsigned gu32;
typedef LAS unsigned char* lp;
#define STDECL
#define STAMP(i)
#define STFLUSH()
#define RETRY(k)
#define DLY0 30
#define DLY1 8
#define DLY2 2
#define PRE 1
#define HP 4
#define MP 1
#define TOUCH 1
#define CODEPF 15360
#define XS 0
#define DLY3 4
#define PD 4
#define SLP 2
#define ROT 0
#define NX1 14
#define NEARLY 0
#ifndef NV0
#define NV0 0
#endif
#define AUXO 16
#define NH 0
#ifndef NV2
#define NV2 16
#endif
#define HL 8
#define MC 8
#define DLY4 18
#define DLY5 8
#define BA 4
#define GATE 2
#define RL 8
#define MLP 12
#define PREWR 1
#define VR 9
#define VC 15
#define NH2 12
#ifndef NVS
#define NVS 12
#endif

constexpr int O_EPOCH = 0;
constexpr int O_MOD = 4096;
constexpr int O_CL = 16384;
constexpr int O_MT = 131072;
constexpr int O_NF = O_MT + 589824;
constexpr int O_QF = O_NF + 1572864;
constexpr int O_VF = O_QF + 1572864;
constexpr int O_OF = O_VF + 1572864;
constexpr int O_ML = O_OF + 6291456;
constexpr int O_RM = O_ML + 131072;
constexpr int O_RN = O_RM + 1152 * 128;
constexpr int O_RQ = O_RN + 512 * 128;
constexpr int O_RV = O_RQ + 1536 * 128;
constexpr int O_RO = O_RV + 1536 * 128;
constexpr int WS_BYTES = O_RO + 6144 * 128;
constexpr int OOB = 0x40000000;

constexpr int L_P0 = 0;
constexpr int L_MS = 61440, L_QS = L_MS + 24576, L_CS = L_QS + 24576, L_MX = L_CS + 512, L_LS = L_MX + 1024, L_BN = L_LS + 1024;
constexpr int L_SCR = L_BN + 6144, L_TAG = L_SCR + 256;
constexpr int L_TOTAL = L_TAG + 32;

static_assert(NV2 == 16 && NV0 + NVS <= NV2 && NV2 <= 24, "");
static_assert(BA < NX1 && MC + 3 < NX1 && MP < MC && HP < NX1 && NX1 >= 9 && NX1 <= 21, "");
constexpr int AUX_SC1 = 16;
constexpr unsigned SPIN_CAP = 1u << 16;

DI float bflo(unsigned v) { return __uint_as_float(v << 16); }
DI float bfhi(unsigned v) { return __uint_as_float(v & 0xffff0000u); }
DI unsigned cvtpk(float lo, float hi) {
  f32x2 x = {lo, hi};
  return __builtin_bit_cast(unsigned, __builtin_convertvector(x, bf2));
}
DI float bfround(float x) { return bflo(cvtpk(x, 0.f)); }
DI unsigned pkhi(unsigned lo, unsigned hi) { return __builtin_amdgcn_perm(hi, lo, 0x07060302u); }
DI unsigned tagged(float v, unsigned tag16) { return (cvtpk(0.f, v) & 0xffff0000u) | tag16; }
DI unsigned tchk(unsigned a, v4u v, unsigned tag) { return a | (v[0] ^ tag) | (v[1] ^ tag) | (v[2] ^ tag) | (v[3] ^ tag); }
typedef unsigned short u16x2 __attribute__((ext_vector_type(2)));
DI unsigned sum2(unsigned a, unsigned x, unsigned y) { return __builtin_amdgcn_udot2(__builtin_bit_cast(u16x2, x), __builtin_bit_cast(u16x2, y), a, false); }
DI v2u sum4(v2u a, v4u v) { return v2u{sum2(a[0], v[0], v[1]), sum2(a[1], v[2], v[3])}; }
DI v2u sum4(unsigned a, v4u v) { return sum4(v2u{a, 0u}, v); }
DI unsigned scan63(unsigned v) {
  v += __builtin_amdgcn_update_dpp(0u, v, 0x111, 0xf, 0xf, true);
  v += __builtin_amdgcn_update_dpp(0u, v, 0x112, 0xf, 0xf, true);
  v += __builtin_amdgcn_update_dpp(0u, v, 0x114, 0xf, 0xf, true);
  v += __builtin_amdgcn_update_dpp(0u, v, 0x118, 0xf, 0xf, true);
  v += __builtin_amdgcn_update_dpp(0u, v, 0x142, 0xa, 0xf, true);
  v += __builtin_amdgcn_update_dpp(0u, v, 0x143, 0xc, 0xf, true);
  return v;
}
DI float sum_g(float v);
DI float qsumf(float v) {
#define FSTEP(ctl) v += __uint_as_float(__builtin_amdgcn_update_dpp(0u, __float_as_uint(v), ctl, 0xf, 0xf, true))
  FSTEP(0x128); FSTEP(0x124);
#undef FSTEP
  return sum_g(v);
}
DI unsigned wsum(unsigned v) { return __builtin_amdgcn_readlane(scan63(v), 63); }
DI unsigned wsum(v2u v, unsigned minus = 0u) { return wsum(v[0] + v[1] - minus); }
DI float max3(float a, float b, float c) { float r; asm("v_max3_f32 %0, %1, %2, %3" : "=v"(r) : "v"(a), "v"(b), "v"(c)); return r; }
DI float max2(float a, float b) { float r; asm("v_max_f32 %0, %1, %2" : "=v"(r) : "v"(a), "v"(b)); return r; }
DI float dot2(unsigned a, unsigned b, float c) { return __builtin_amdgcn_fdot2_f32_bf16(__builtin_bit_cast(bf2, a), __builtin_bit_cast(bf2, b), c, false); }
DI f32x4 mfma(v4u a, v4u b, f32x4 c) {
  return __builtin_amdgcn_mfma_f32_16x16x32_bf16(__builtin_bit_cast(bf16x8, a), __builtin_bit_cast(bf16x8, b), c, 0, 0, 0);
}
DI float sum_g(float v) {
  auto r = __builtin_amdgcn_permlane16_swap(__float_as_uint(v), __float_as_uint(v), false, false);
  v = __uint_as_float(r[0]) + __uint_as_float(r[1]);
  auto q = __builtin_amdgcn_permlane32_swap(__float_as_uint(v), __float_as_uint(v), false, false);
  return __uint_as_float(q[0]) + __uint_as_float(q[1]);
}
DI float max_g(float v) {
  auto r = __builtin_amdgcn_permlane16_swap(__float_as_uint(v), __float_as_uint(v), false, false);
  v = max2(__uint_as_float(r[0]), __uint_as_float(r[1]));
  auto q = __builtin_amdgcn_permlane32_swap(__float_as_uint(v), __float_as_uint(v), false, false);
  return max2(__uint_as_float(q[0]), __uint_as_float(q[1]));
}
DI int sopq(int v) { v = __builtin_amdgcn_readfirstlane(v); asm volatile("" : "+s"(v)); return v; }
template <typename T> DI T opq(T v) { asm volatile("" : "+v"(v)); return v; }
#define BAR() do { asm volatile("s_waitcnt lgkmcnt(0)" ::: "memory"); __builtin_amdgcn_s_barrier(); asm volatile("" ::: "memory"); } while (0)
#define VMWAIT(n) asm volatile("s_waitcnt vmcnt(%0)" :: "i"(n) : "memory")
#define SB() __builtin_amdgcn_sched_barrier(0)
#define CBAR() asm volatile("" ::: "memory")
#define LD128(off, so) __builtin_amdgcn_raw_buffer_load_b128(rws, (off), (so), AUX_SC1)
#define LD64(off) __builtin_amdgcn_raw_buffer_load_b64(rws, (off), 0, AUX_SC1)
#define LD32(off) __builtin_amdgcn_raw_buffer_load_b32(rws, (off), 0, AUX_SC1)
#define ST128(v, off) __builtin_amdgcn_raw_buffer_store_b128((v), rws, (off), 0, AUX_SC1)
#define ST128S(v, off, so) __builtin_amdgcn_raw_buffer_store_b128((v), rws, (off), (so), AUX_SC1)
#define ST64(v, off) __builtin_amdgcn_raw_buffer_store_b64((v), rws, (off), 0, AUX_SC1)
#define ST32(v, off) __builtin_amdgcn_raw_buffer_store_b32((v), rws, (off), 0, AUX_SC1)
#define ST16(v, off) __builtin_amdgcn_raw_buffer_store_b16((v), rws, (off), 0, AUX_SC1)
#define RST(sum, off) ST128((v4u{(sum) + tag, tag, tag, tag}), (off))
#define RECOK(v, off) __all((off) >= OOB || (v) == tag)
#define WAITREC(off) do { unsigned w_ = 0; CBAR(); while (!RECOK(LD32((off) + 4), (off)) && ++w_ < SPIN_CAP) { __builtin_amdgcn_s_sleep(2); CBAR(); } CBAR(); } while (0)
#define LDS128(p, off) (*(LAS v4u*)((p) + (off)))
#define LDSF4(p, off) (*(LAS f32x4*)((p) + (off)))
DI void dma(unsigned ldsaddr, int voff, __amdgpu_buffer_rsrc_t rs, int soff) {
  asm volatile("s_mov_b32 m0, %0\n\ts_nop 0\n\tbuffer_load_dwordx4 %1, %2, %3 offen lds" :: "s"(ldsaddr), "v"(voff), "s"(rs), "s"(soff) : "memory");
}

DI void touch(unsigned ldsaddr, int voff, __amdgpu_buffer_rsrc_t rs) {
  asm volatile("s_mov_b32 m0, %0\n\ts_nop 0\n\tbuffer_load_dword %1, %2, 0 offen lds" :: "s"(ldsaddr), "v"(voff), "s"(rs) : "memory");
}
#define UNLIKELY(c) __builtin_expect(!!(c), 0)

__global__ __launch_bounds__(512, 2) void dit_kernel(const u16* __restrict__ x, const u16* __restrict__ c, const u16* __restrict__ wqkv,
                                                     const u16* __restrict__ wo, const u16* __restrict__ wmod, unsigned char* ws,
                                                     u16* __restrict__ out) {
  extern __shared__ __attribute__((aligned(16))) unsigned char lds_[];
  lp lds = (lp)lds_;
  const unsigned ldsb = (unsigned)(size_t)lds;
  STDECL;
  STAMP(0);
  const unsigned long long pc0 = __builtin_amdgcn_s_getpc();
  const int tid = threadIdx.x, lane = tid & 63, w = __builtin_amdgcn_readfirstlane(tid >> 6);
  const int n16 = lane & 15, g = lane >> 4;
  const int bid = blockIdx.x, xg = bid & 7, r = bid >> 3;
  const int b = xg >> 2, ql = r & 7, kb = r >> 3;
  const int qt = b * 32 + (xg & 3) * 8 + ql;
  const int qt3 = b * 32 + (((xg & 3) + XS) & 3) * 8 + ql;
  const bool act1 = ql < 6;
  const int cb = (xg & 3) * 6 + (act1 ? ql : ql - 2);
  const __amdgpu_buffer_rsrc_t rws = __builtin_amdgcn_make_buffer_rsrc(ws, 0, WS_BYTES, 0x00020000);
  const __amdgpu_buffer_rsrc_t rx = __builtin_amdgcn_make_buffer_rsrc((void*)x, 0, 2048 * 768, 0x00020000);
  const int dch = 16 * ((lane & 3) ^ ((lane & 32) ? 3 : 0));
  const int rslot = (4 * n16 + (g ^ ((n16 & 8) ? 3 : 0))) * 16;
  const int l63 = lane < RL ? lane * 16 : OOB;

  const int mu = (((r - 24) >> 1) * 8 + xg) * 2 + (r & 1);
  const int role = r < 12 ? 0 : ((r >= 24 && mu < 36) ? 2 : 1);
  const int dead0 = (r >= 24 && mu >= 36) ? OOB : 0;
  const int ru = r < 12 ? r : (r < 24 ? r - 12 : r - 24);
  const int u12 = 6 * (xg & 1) + ru % 6, u8 = 2 * (xg >> 1) + ru / 6;
  unsigned ep;
  asm volatile("s_nop 4\n\tbuffer_load_dword %0, off, %1, 0 sc1" : "=v"(ep) : "s"(rws) : "memory");
  if (w < 6) {
    unsigned cv = 0;
    if (role == 2) cv = ((const unsigned*)c)[tid];
    if (role < 2) {
      const __amdgpu_buffer_rsrc_t rq = __builtin_amdgcn_make_buffer_rsrc((void*)wqkv, 0, 384 * 2304, 0x00020000);
      const int v = (lane >> 2) * 2304 + dch;
      const int s3 = 48 * u8 * 2304 + (role ? 1536 : 768) + 128 * w;
#pragma unroll
      for (int t = 0; t < 3; ++t) {
        dma(ldsb + L_P0 + (t * 12 + 2 * w) * 1024, v, rq, s3 + 16 * t * 2304);
        dma(ldsb + L_P0 + (t * 12 + 2 * w + 1) * 1024, v, rq, s3 + 16 * t * 2304 + 64);
      }
      if (role == 0) {
        const int s2 = 32 * u12 * 2304 + 128 * w;
#pragma unroll
        for (int t = 0; t < 2; ++t) {
          dma(ldsb + L_P0 + ((3 + t) * 12 + 2 * w) * 1024, v, rq, s2 + 16 * t * 2304);
          dma(ldsb + L_P0 + ((3 + t) * 12 + 2 * w + 1) * 1024, v, rq, s2 + 16 * t * 2304 + 64);
        }
      }
    }
    if (role == 1 || role == 2) {
      const int ld = role == 1 ? 768 : 4608;
      const __amdgpu_buffer_rsrc_t rc = __builtin_amdgcn_make_buffer_rsrc((void*)(role == 1 ? wo : wmod), 0, 384 * ld, 0x00020000);
      const int v = (lane >> 2) * ld + dch;
#pragma unroll
      for (int j = 0; j < 4; ++j) dma(ldsb + L_P0 + (36 + 4 * w + j) * 1024, v, rc, (role == 1 ? u12 : mu) * 64 + (4 * w + j) * 16 * ld);
    }
    if (role == 2) {
      const float f0 = bflo(cv), f1 = bfhi(cv);
      const int bb = tid >= 192, i2 = 2 * (tid - 192 * bb);
      *(LAS unsigned*)(lds + L_P0 + (i2 >> 5) * 1024 + (bb * 4 + ((i2 >> 3) & 3)) * 16 + (i2 & 7) * 2) =
          cvtpk(f0 * __builtin_amdgcn_rcpf(1.f + __expf(-f0)), f1 * __builtin_amdgcn_rcpf(1.f + __expf(-f1)));
    }
    VMWAIT(0);
  } else {
    __builtin_amdgcn_s_setprio(3);
    v4u xs[3];
    asm volatile("buffer_load_dwordx4 %0, %3, %4, 0 offen\n\tbuffer_load_dwordx4 %1, %3, %4, 0 offen offset:256\n\tbuffer_load_dwordx4 %2, %3, %4, 0 offen offset:512"
                 : "=&v"(xs[0]), "=&v"(xs[1]), "=&v"(xs[2]) : "v"((32 * (bid >> 2) + 16 * (bid & 1) + 8 * (w & 1) + 2 * (bid & 2) + (lane & 3)) * 768 + (lane >> 2) * 16), "s"(rx) : "memory");
    {
      const unsigned scr = ldsb + L_SCR;
      const int dead = ((TOUCH & 1) && lane < 36) ? 0 : OOB;
      const __amdgpu_buffer_rsrc_t rq = __builtin_amdgcn_make_buffer_rsrc((void*)wqkv, 0, 384 * 2304, 0x00020000);
      touch(scr, dead + (w == 6 ? (96 * (xg >> 1) + 3 * r + lane / 12) * 2304 + 768 + (lane % 12) * 128 : (192 * (xg & 1) + 6 * r + lane / 6) * 2304 + (lane % 6) * 128), rq);
      const __amdgpu_buffer_rsrc_t r2 = __builtin_amdgcn_make_buffer_rsrc(w == 6 ? (void*)wo : (void*)(pc0 & ~127ull), 0, w == 6 ? 384 * 768 : CODEPF, 0x00020000);
      touch(scr, w == 6 ? dead + (12 * r + lane / 3) * 768 + 384 * (xg & 1) + (lane % 3) * 128 : (lane < 5 ? (r * 5 + lane) * 128 : OOB), r2);
    }
    if (w == 7) {
      const __amdgpu_buffer_rsrc_t r3 = __builtin_amdgcn_make_buffer_rsrc((void*)wmod, 0, 384 * 4608, 0x00020000);
      touch(ldsb + L_SCR, (lane < (xg < 2 ? 36 : 24) ? 0 : OOB) + (12 * r + lane % 12) * 4608 + ((lane / 12) * 8 + xg) * 128, r3);
    }
    else touch(ldsb + L_SCR, OOB, rx);
    asm volatile("s_waitcnt vmcnt(3)" : "+v"(xs[0]), "+v"(xs[1]), "+v"(xs[2]) :: "memory");
    if (PREWR & 2) ST128((v4u{0u, 0u, 0u, 0u}), l63 + O_RN + (bid * 2 + (w & 1)) * 128);
    float f[24], sm = 0.f, q = 0.f;
#pragma unroll
    for (int i = 0; i < 12; ++i) { f[2 * i] = bflo(xs[i >> 2][i & 3]); f[2 * i + 1] = bfhi(xs[i >> 2][i & 3]); sm += f[2 * i] + f[2 * i + 1]; }
    sm = qsumf(sm) * (1.f / 384.f);
#pragma unroll
    for (int i = 0; i < 24; ++i) { f[i] -= sm; q = fmaf(f[i], f[i], q); }
    const float rs = __builtin_amdgcn_rsqf(qsumf(q) * (1.f / 384.f) + 1e-5f);
#pragma unroll
    for (int i = 0; i < 3; ++i)
      LDS128(lds, L_BN + ((lane >> 2) + 16 * i) * 128 + ((w & 1) * 4 + (lane & 3)) * 16) =
          v4u{cvtpk(f[8 * i] * rs, f[8 * i + 1] * rs), cvtpk(f[8 * i + 2] * rs, f[8 * i + 3] * rs), cvtpk(f[8 * i + 4] * rs, f[8 * i + 5] * rs), cvtpk(f[8 * i + 6] * rs, f[8 * i + 7] * rs)};
  }
  asm volatile("" : "+v"(ep));
  const unsigned tag = __builtin_amdgcn_readfirstlane(ep) + 1u;
  STAMP(2);
  BAR();
  STAMP(3);
  if (w >= 6) {
    v4u nv[3];
#pragma unroll
    for (int i = 0; i < 3; ++i) nv[i] = LDS128(lds, L_BN + (w & 1) * 3072 + i * 1024 + lane * 16);
    const int no = O_NF + (bid >> 1) * 12288 + ((w & 1) * 24 + (lane >> 3)) * 256 + (bid & 1) * 128 + (lane & 7) * 16;
#pragma unroll
    for (int i = 0; i < 3; ++i) ST128(nv[i], no + i * 2048);
    VMWAIT(0);
    RST(0u, l63 + O_RN + (bid * 2 + (w & 1)) * 128);
    __builtin_amdgcn_s_setprio(0);
  }
  const int rg = b * 32 + kb * 8 + w;
  const int nro = lane < 8 ? O_RN + (rg * 8 + lane) * 128 : OOB;
  if (w < (role == 2 ? 2 : 6)) {
    const int t3 = w >> 1, t2 = w & 1;
    f32x4 acc = {};
    v4u a[12], bq[12];
    if (role == 0) {
      lp ap = opq(lds + L_P0 + (3 + t2) * 12288 + rslot), bp = opq(lds + L_P0 + t3 * 12288 + rslot);
#pragma unroll
      for (int k = 0; k < 12; ++k) {
        a[k] = LDS128(ap, k * 1024); bq[k] = LDS128(bp, k * 1024);
        if (k >= 5) acc = mfma(a[k - 5], bq[k - 5], acc);
        SB();
      }
    } else {
      lp ap = opq(lds + L_P0 + (role == 2 ? ((n16 & 1) * 4 + g) * 16 : t3 * 12288 + rslot));
      lp tb = opq(lds + L_P0 + 36864 + (8 * g + (n16 >> 2)) * 64 + 16 * ((2 * t2 + ((n16 >> 1) & 1)) ^ ((g & 1) ? 3 : 0)) + 8 * (n16 & 1));
#pragma unroll
      for (int k = 0; k < 12; ++k) {
        a[k] = LDS128(ap, k * 1024);
        bq[k] = __builtin_bit_cast(v4u, __builtin_shufflevector(__builtin_amdgcn_ds_read_tr16_b64_v4i16((LAS s16x4*)(tb + k * 2048)),
                                                                __builtin_amdgcn_ds_read_tr16_b64_v4i16((LAS s16x4*)(tb + k * 2048 + 256)), 0, 1, 2, 3, 4, 5, 6, 7));
        if (k >= 5) acc = mfma(a[k - 5], bq[k - 5], acc);
        SB();
      }
    }
#pragma unroll
    for (int k = 7; k < 12; ++k) acc = mfma(a[k], bq[k], acc);
    if (role != 2) {
      const int blk = role * 576 + (role == 0 ? (3 * u8 + t3) * 24 + 2 * u12 + t2 : (2 * u12 + t2) * 24 + 3 * u8 + t3);
      const unsigned o0 = cvtpk(acc[0], acc[1]), o1 = cvtpk(acc[2], acc[3]);
      auto r0 = __builtin_amdgcn_permlane16_swap(o0, o0, false, false);
      auto r1 = __builtin_amdgcn_permlane16_swap(o1, o1, false, false);
      ST128((v4u{r0[0], r1[0], r0[1], r1[1]}), (((g & 1) ? OOB : 0) | dead0) + O_MT + blk * 512 + ((g >> 1) * 16 + n16) * 16);
      RST(wsum(sum2(0u, o0, o1)), (l63 | dead0) + O_RM + blk * 128);
    } else if (g < 2) {
      float v = bfround(g ? acc[1] : acc[0]);
      if (mu >= 12 && mu < 24) v += 1.f;
      ST32(tagged(v, tag & 0xffffu), O_MOD + (2 * mu + t2) * 128 + lane * 4);
    }
  }
  STAMP(4);
  __builtin_amdgcn_s_barrier();
  if (DLY1) __builtin_amdgcn_s_sleep(DLY1);
  v2u nr = LD64(nro);
  SB();
  const int l16 = opq(lane * 16);
  const int s_n = O_NF + rg * 24576;
  const int rot = ROT ? (ql * 3) >> 1 : 0;
  const int l16b = opq(lane * 16 + 12288);
  v4u xf[2][12];
#define XF(i) xf[(i) & 1][(i) >> 1]
#define XLOAD(i) XF(i) = LD128(((i) & 1) ? l16b : l16, s_n + (((i) >> 1) + rot - (((i) >> 1) + rot >= 12 ? 12 : 0)) * 1024)
  lp fl = opq(lds + L_MS + lane * 16);
  v4u qf[3]; unsigned qr, vsum; int rvo;
  const int qro = opq(lane < 3 ? O_RQ + (qt * 24 + 3 * w + lane) * 128 : OOB);
  const int s_ql = sopq(O_QF + (qt * 24 + 3 * w) * 1024);
#define HOP2_LOADS() do { _Pragma("unroll") for (int i = 0; i < 3; ++i) qf[i] = LD128(l16 + i * 1024, s_ql); qr = LD32(qro); } while (0)
  {
    const int s0 = 3 * (w & 3);
    const int mc4 = lane < 24 ? 96 + 8 * s0 + lane : (lane < 48 ? 8 * s0 + lane - 24 : (w < 4 ? 96 : 192) + 4 * cb + (lane & 3));
    const int mo = opq(O_MOD + (mc4 >> 2) * 128 + b * 64 + (mc4 & 3) * 16);
    const int fb = (w >> 2) * 576 + cb * 24 + 2 * s0;
    const int ro = opq(lane < 6 ? O_RM + (fb + lane) * 128 : OOB);
    const int s_f = sopq(O_MT + fb * 512);
    const int blk1 = rg * 24 + cb, dead1 = act1 ? 0 : OOB;
    const int qso = opq(dead1 + O_QF + blk1 * 1024 + ((g & 1) * 32 + (g >> 1) * 16 + n16) * 16);
    const int vso = opq(dead1 + O_VF + blk1 * 1024 + lane * 16);
    const int rso = opq((dead1 | l63) + O_RQ + blk1 * 128);
    rvo = opq((dead1 | l63) + O_RV + (((b * 4 + kb) * 24 + cb) * 8 + w) * 128);
    lp cg = opq(lds + L_CS + g * 16), cn = opq(lds + L_CS + 256 + n16 * 4), csw = opq(lds + L_CS + w * 64 + n16 * 4);
    lp bn = lds + L_BN + w * 768, bg = opq(bn + g * 32), msw[3];
#pragma unroll
    for (int i = 0; i < 3; ++i) { int sl = s0 + i - rot; if (sl < 0) sl += 12; msw[i] = opq(fl + (w >> 2) * 12288 + sl * 1024); }
    SB();
    if (UNLIKELY(!__all(lane >= 8 || nr[1] == tag))) {
      unsigned it = 0;
      do { RETRY(0); __builtin_amdgcn_s_sleep(2); CBAR(); nr = LD64(nro); } while (!__all(lane >= 8 || nr[1] == tag) && ++it < SPIN_CAP);
    }
    CBAR();
    if (GATE == 2) __builtin_amdgcn_s_barrier();
    STAMP(5);
    v4u mf[3], mv4; unsigned mr;
    f32x4 sa[3], sb[3]; v4u tf[3]; float oc_;
#define MODOK() __all((tchk(0u, mv4, tag) & 0xffffu) == 0u)
#define HOP1_LOADS() do { _Pragma("unroll") for (int i = 0; i < 3; ++i) mf[i] = LD128(l16 + i * 1024, s_f); mr = LD32(ro); } while (0)
#pragma unroll
    for (int i = 0; i < NX1; ++i) {
      XLOAD(i);
      if (i == MP) mv4 = LD128(mo, 0);
      if (i == HP) HOP1_LOADS();
      if (i == BA && w >= 4) __builtin_amdgcn_s_barrier();
      if (i == MC) {
        if (UNLIKELY(!MODOK())) {
          unsigned it = 0;
          do { RETRY(1); __builtin_amdgcn_s_sleep(8); CBAR(); mv4 = LD128(mo, 0); } while (!MODOK() && ++it < SPIN_CAP);
        }
        if (lane >= 24 && lane < 48) *(LAS v2u*)(bn + 512 + (lane - 24) * 8) = v2u{pkhi(mv4[0], mv4[1]), pkhi(mv4[2], mv4[3])};
        else LDS128(bn, (lane < 24 ? lane : (lane & 3) + 24) * 16) = mv4 & 0xffff0000u;
        CBAR();
#pragma unroll
        for (int k = 0; k < 3; ++k) { sa[k] = LDSF4(bg, 128 * k); sb[k] = LDSF4(bg, 128 * k + 16); tf[k] = LDS128(bn, 512 + 64 * k + 16 * g); }
        oc_ = *(LAS float*)(bn + 384 + n16 * 4);
      }
      if (i == MC + 1) { if (w < 4) oc_ *= 0.05103103630798288f * 1.4426950408889634f; sa[0] = opq(sa[0] * oc_); sb[0] = opq(sb[0] * oc_); }
      if (i == MC + 2) { sa[1] = opq(sa[1] * oc_); sb[1] = opq(sb[1] * oc_); }
      if (i == MC + 3) { sa[2] = opq(sa[2] * oc_); sb[2] = opq(sb[2] * oc_); }
      SB();
    }
    STAMP(6);
#define FBAD() (wsum(sum4(sum4(sum4(-mr, mf[0]), mf[1]), mf[2])) + 6u * tag != 0u)
    if (UNLIKELY(FBAD())) {
      unsigned it = 0;
      do { RETRY(9); __builtin_amdgcn_s_sleep(6); CBAR(); HOP1_LOADS(); } while (FBAD() && ++it < SPIN_CAP);
    }
    STAMP(7);
    {
      f32x4 au = {};
#pragma unroll
      for (int i = 0; i < 3; ++i) {
        au = mfma(tf[i], mf[i], au);
        const v4u o = {cvtpk(bflo(mf[i][0]) * sa[i][0], bfhi(mf[i][0]) * sa[i][1]), cvtpk(bflo(mf[i][1]) * sa[i][2], bfhi(mf[i][1]) * sa[i][3]),
                       cvtpk(bflo(mf[i][2]) * sb[i][0], bfhi(mf[i][2]) * sb[i][1]), cvtpk(bflo(mf[i][3]) * sb[i][2], bfhi(mf[i][3]) * sb[i][3])};
        LDS128(msw[i], 0) = o;
        if (NX1 + i < 24) XLOAD(NX1 + i);
        SB();
      }
      *(LAS float*)(csw) = au[0] * oc_;
    }
    STAMP(9);
    __builtin_amdgcn_s_setprio(2);
    BAR();
    v4u a[24];
    f32x4 uu[4]; float zz[4], zs;
#define AOP(k) do { if ((k) + PD < ((k) < 12 ? 12 : 24)) a[(k) + PD] = LDS128(fl, ((k) + PD) * 1024); \
      if ((k) < 12) { aq[0] = mfma(a[k], xf[0][(k) % 12], aq[0]); aq[1] = mfma(a[k], xf[1][(k) % 12], aq[1]); } \
      else { av[0] = mfma(xf[0][(k) % 12], a[k], av[0]); av[1] = mfma(xf[1][(k) % 12], a[k], av[1]); } } while (0)
#pragma unroll
    for (int k = 0; k < PD; ++k) a[k] = LDS128(fl, k * 1024);
    SB();
    f32x4 aq[2] = {}, av[2] = {};
#pragma unroll
    for (int k = 0; k < 12; ++k) {
      if (NX1 + 3 + k < 24) XLOAD(NX1 + 3 + k);
      if (k >= 2 && k < 6) uu[k - 2] = LDSF4(cg, (k - 2) * 64);
      AOP(k);
      if (k == 9) { uu[0] = uu[0] + uu[1]; uu[2] = uu[2] + uu[3]; }
      if (k == 10) uu[0] = uu[0] + uu[2];
      SB();
    }
    {
      const f32x4 v0 = aq[0] + uu[0], v1 = aq[1] + uu[0];
      auto r0 = __builtin_amdgcn_permlane16_swap(cvtpk(v0[0], v0[1]), cvtpk(v1[0], v1[1]), false, false);
      auto r1 = __builtin_amdgcn_permlane16_swap(cvtpk(v0[2], v0[3]), cvtpk(v1[2], v1[3]), false, false);
      const v4u ov = {r0[0], r1[0], r0[1], r1[1]};
      ST128(ov, qso);
      RST(wsum(sum4(0u, ov)), rso);
      __builtin_amdgcn_s_setprio(0);
      STAMP(12);
      if (w < 4) BAR();
      CBAR();
#pragma unroll
      for (int k = 12; k < 12 + PD; ++k) a[k] = LDS128(fl, k * 1024);
      SB();
#pragma unroll
      for (int k = 12; k < 24; ++k) {
        if (k >= 14 && k < 18) zz[k - 14] = *(LAS float*)(cn + (k - 14) * 64);
        AOP(k);
        if (k == 21) zs = (zz[0] + zz[1]) + (zz[2] + zz[3]);
        SB();
      }
      const f32x4 w0 = av[0] + zs, w1 = av[1] + zs;
      const v4u o = {cvtpk(w0[0], w0[1]), cvtpk(w0[2], w0[3]), cvtpk(w1[0], w1[1]), cvtpk(w1[2], w1[3])};
      ST128(o, vso);
      vsum = wsum(sum4(0u, o));
    }
    STAMP(10);
    SB();
  }
  STAMP(11);
  if (w == 7) ST128((v4u{tag, tag, tag, tag}), l63 + O_CL + bid * 128);
  const int vro = opq(lane < 24 ? O_RV + (((b * 4 + kb) * 8 + w) * 24 + lane) * 128 : OOB);
  const int mso = opq((w == 0 && g < 2) ? O_ML + ((qt * 4 + kb) * 32 + lane) * 16 : OOB);
  const int s_vl = sopq(O_VF + ((b * 32 + 8 * kb) * 24 + 3 * w) * 1024);
  const bool own = (w >> 1) == kb;
  const int oso = opq((own ? OOB : 0) + O_OF + ((qt * 24 + 3 * w) * 4 + kb) * 1024 + ((g & 1) * 32 + (g >> 1) * 16 + n16) * 16);
  const int roo = opq(((own ? OOB : 0) | l63) + O_RO + ((qt * 24 + 3 * w) * 4 + kb) * 128);
  lp mxr = opq(lds + L_MX + n16 * 32), mxw = opq(lds + L_MX + n16 * 32 + w * 4);
  lp plw = opq(lds + lane * 16 + w * 2048);
  lp pl = opq(lds + lane * 16);
  const int ooff = opq(((qt * 32 + 8 * (n16 >> 2) + 4 * (lane >> 5) + (n16 & 3)) * 384 + (6 * kb + w) * 16 + 8 * (g & 1)) * 2);
  const v4u xr = __builtin_amdgcn_raw_buffer_load_b128(rx, w < 6 ? ooff : OOB, 0, 0);
  const int mlo = opq(O_ML + (qt * 128 + (lane >> 5) * 16 + n16) * 16);
  const int s_ol = sopq(O_OF + (qt * 24 + 6 * kb + w) * 4096), rlo = opq((lane < 4 && lane != kb) ? O_RO + ((qt * 24 + 6 * kb + w) * 4 + lane) * 128 : OOB);
  if (PREWR & 1) {
    const unsigned nt_ = ~tag;
    ST128((v4u{nt_, nt_, nt_, nt_}), lane < 24 ? O_RO + ((qt * 24 + 3 * w + (lane >> 3)) * 4 + kb) * 128 + (lane & 7) * 16 : ((w == 0 && lane >= 32) ? O_ML + ((qt * 4 + kb) * 32 + lane - 32) * 16 : OOB));
  }
  v4u vf[8][3];
#define VF(i) vf[(i) & 7][(i) >> 3]
#define VLOAD(i) VF(i) = LD128(l16 + ((i) >> 3) * 1024, s_vl + ((i) & 7) * 24576)
  {
    lp qw = opq(lds + L_QS + (lane >> 5) * 12288 + 3 * w * 512 + (lane & 31) * 16);
    SB();
    __builtin_amdgcn_s_barrier();
    if (DLY2) __builtin_amdgcn_s_sleep(DLY2);
    HOP2_LOADS();
#pragma unroll
    for (int i = 0; i < NV0; ++i) VLOAD(i);
    SB();
#define QBAD() (wsum(sum4(sum4(sum4(-qr, qf[0]), qf[1]), qf[2])) + 3u * tag != 0u)
#pragma unroll
    for (int i = 0; i < 3; ++i) LDS128(qw, i * 512) = qf[i];
    if (UNLIKELY(QBAD())) {
      unsigned it = 0;
      do { RETRY(2); __builtin_amdgcn_s_sleep(3); CBAR(); HOP2_LOADS(); } while (QBAD() && ++it < SPIN_CAP);
#pragma unroll
      for (int i = 0; i < 3; ++i) LDS128(qw, i * 512) = qf[i];
    }
  }
  VMWAIT(0);
  RST(vsum, rvo);
  STAMP(13);
  BAR();
  float m[2]; v2u vr, hv0 = {0u, 0u};
  {
    v4u q[24];
#define QREAD(i) q[i] = LDS128(fl, 24576 + (((i) & 1) * 12 + ((i) >> 1)) * 1024)
#pragma unroll
    for (int i = 0; i < PD; ++i) QREAD(i);
    SB();
    f32x4 acc[2][2] = {};
#pragma unroll
    for (int i = 0; i < 24; ++i) {
      if (i + PD < 24) QREAD(i + PD);
      acc[i & 1][0] = mfma(xf[0][i >> 1], q[i], acc[i & 1][0]); acc[i & 1][1] = mfma(xf[1][i >> 1], q[i], acc[i & 1][1]);
      if ((i * NVS) / 24 != ((i + 1) * NVS) / 24) VLOAD((i * NVS) / 24);
      if (i >= 24 - NH) hv0 = opq(sum4(hv0, VF(i - (24 - NH))));
      SB();
    }
#pragma unroll
    for (int u = 0; u < 2; ++u) {
      float mx = max3(max3(acc[u][0][0], acc[u][0][1], acc[u][0][2]), acc[u][0][3], acc[u][1][0]);
      mx = max_g(max2(max3(mx, acc[u][1][1], acc[u][1][2]), acc[u][1][3]));
      *(LAS float*)(mxw + u * 512) = mx;
    }
    STAMP(14);
    BAR();
    f32x4 ma[2], mb[2];
#pragma unroll
    for (int u = 0; u < 2; ++u) { ma[u] = LDSF4(mxr, u * 512); mb[u] = LDSF4(mxr, u * 512 + 16); }
    vr = LD64(vro);
    SB();
#pragma unroll
    for (int u = 0; u < 2; ++u) {
      m[u] = max2(max3(max3(ma[u][0], ma[u][1], ma[u][2]), ma[u][3], mb[u][0]), max3(mb[u][1], mb[u][2], mb[u][3]));
      float p[8];
#pragma unroll
      for (int i = 0; i < 8; ++i) p[i] = __builtin_amdgcn_exp2f(acc[u][i >> 2][i & 3] - m[u]);
      LDS128(plw, u * 1024) = v4u{cvtpk(p[0], p[1]), cvtpk(p[2], p[3]), cvtpk(p[4], p[5]), cvtpk(p[6], p[7])};
      if (NV0 + NVS + u < NV2) VLOAD(NV0 + NVS + u);
      SB();
    }
#pragma unroll
    for (int i = NV0 + NVS + 2; i < NV2; ++i) VLOAD(i);
  }
#pragma unroll
  for (int i = NH; i < NH2; ++i) hv0 = sum4(hv0, VF(i));
  hv0 = opq(hv0);
  if (UNLIKELY(!RECOK(vr[1], vro))) { RETRY(7); WAITREC(vro); vr = LD64(vro); }
  STAMP(15);
  BAR();
  v4u ml[4];
#define ML_LOADS() do { _Pragma("unroll") for (int k = 0; k < 4; ++k) ml[k] = LD128(mlo, ((kb + k) & 3) * 512); } while (0)
  {
    v4u pf[16];
    f32x4 acc[3][2] = {};
    v4u ov[3];
#define OSTORE(et) do { const f32x4 v0 = acc[et][0], v1 = acc[et][1]; \
      auto r0 = __builtin_amdgcn_permlane16_swap(cvtpk(v0[0], v0[1]), cvtpk(v1[0], v1[1]), false, false); \
      auto r1 = __builtin_amdgcn_permlane16_swap(cvtpk(v0[2], v0[3]), cvtpk(v1[2], v1[3]), false, false); \
      ov[et] = v4u{r0[0], r1[0], r0[1], r1[1]}; ST128S(ov[et], oso, (et) * 4096); \
      if (own) LDS128(lds + 16384 + ((w & 1) * 3 + (et)) * 1024, ((g & 1) * 32 + (g >> 1) * 16 + n16) * 16) = ov[et]; } while (0)
#define ORECORD(et) RST(wsum(sum4(0u, ov[et])), roo + (et) * 512)
#pragma unroll
    for (int i = 0; i < 8; ++i) pf[i] = LDS128(pl, i * 1024);
    SB();
#pragma unroll
    for (int i = 0; i < 16; ++i) {
      if (i + 8 < 16) pf[i + 8] = LDS128(pl, (i + 8) * 1024);
      if ((i & 1) && NV2 + (i >> 1) < 24) VLOAD(NV2 + (i >> 1));
      acc[0][i & 1] = mfma(vf[i >> 1][0], pf[i], acc[0][i & 1]);
      if (i >= NH2) hv0 = opq(sum4(hv0, VF(i)));
      SB();
    }
    if (w == 0) {
      const v4u ones = {0x3f803f80u, 0x3f803f80u, 0x3f803f80u, 0x3f803f80u};
      f32x4 ls[2] = {};
#pragma unroll
      for (int i = 0; i < 16; ++i) ls[i & 1] = mfma(ones, pf[i], ls[i & 1]);
      ST128((v4u{__float_as_uint(g ? m[1] : m[0]), tag, __float_as_uint(g ? ls[1][0] : ls[0][0]), tag}), mso);
    }
    if (UNLIKELY(wsum(hv0, lane < 16 ? vr[0] : 0u) + 16u * tag != 0u)) {
      RETRY(3); CBAR();
#pragma unroll
      for (int i = 0; i < 16; ++i) VLOAD(i);
      acc[0][0] = acc[0][1] = f32x4{0.f, 0.f, 0.f, 0.f};
#pragma unroll
      for (int i = 0; i < 16; ++i) acc[0][i & 1] = mfma(vf[i >> 1][0], pf[i], acc[0][i & 1]);
    }
#pragma unroll
    for (int i = 0; i < 16; ++i) {
      acc[1][i & 1] = mfma(vf[i >> 1][1], pf[i], acc[1][i & 1]);
      if (i == 0) OSTORE(0);
      if (i == 6) ORECORD(0);
      if ((i & 3) == 3) SB();
    }
#pragma unroll
    for (int i = 0; i < 16; ++i) {
      acc[2][i & 1] = mfma(vf[i >> 1][2], pf[i], acc[2][i & 1]);
      if (i == 0) OSTORE(1);
      if (i == 6) ORECORD(1);
      if (i == MLP) ML_LOADS();
      if ((i & 3) == 3) SB();
    }
    STAMP(16);
    OSTORE(2);
    ORECORD(2);
  }
  STAMP(17);
  BAR();
  if (w < 6) {
    v4u pp[4]; unsigned orr;
#define MLOK() __all(((ml[0][1] ^ tag) | (ml[1][1] ^ tag) | (ml[2][1] ^ tag) | (ml[3][1] ^ tag) | (ml[0][3] ^ tag) | (ml[1][3] ^ tag) | (ml[2][3] ^ tag) | (ml[3][3] ^ tag)) == 0u)
#define HOP3_LOADS() do { _Pragma("unroll") for (int k = 1; k < 4; ++k) pp[k] = LD128(l16, s_ol + ((kb + k) & 3) * 1024); orr = LD32(rlo); } while (0)
#define OBAD() (wsum(sum4(sum4(sum4(-orr, pp[1]), pp[2]), pp[3])) + 3u * tag != 0u)
    pp[0] = LDS128(pl, 16384 + w * 1024);
    SB();
    if (DLY3) __builtin_amdgcn_s_sleep(DLY3);
    if (UNLIKELY(!MLOK())) {
      unsigned it = 0;
      RETRY(6); WAITREC(rlo);
      do { CBAR(); ML_LOADS(); } while (!MLOK() && ++it < SPIN_CAP);
    }
    HOP3_LOADS();
    SB();
    STAMP(18);
    const float mm = max2(max2(__uint_as_float(ml[0][0]), __uint_as_float(ml[1][0])), max2(__uint_as_float(ml[2][0]), __uint_as_float(ml[3][0])));
    float wk[4], o[8];
#pragma unroll
    for (int k = 0; k < 4; ++k) wk[k] = __builtin_amdgcn_exp2f(__uint_as_float(ml[k][0]) - mm);
    const float inv = __builtin_amdgcn_rcpf((wk[0] * __uint_as_float(ml[0][2]) + wk[1] * __uint_as_float(ml[1][2])) + (wk[2] * __uint_as_float(ml[2][2]) + wk[3] * __uint_as_float(ml[3][2])));
#pragma unroll
    for (int k = 0; k < 4; ++k) wk[k] *= inv;
#pragma unroll
    for (int e = 0; e < 4; ++e) { o[2 * e] = opq(fmaf(wk[0], bflo(pp[0][e]), bflo(xr[e]))); o[2 * e + 1] = opq(fmaf(wk[0], bfhi(pp[0][e]), bfhi(xr[e]))); }
    SB();
    if (UNLIKELY(OBAD())) {
      unsigned it = 0;
      do { RETRY(5); WAITREC(rlo); HOP3_LOADS(); } while (OBAD() && ++it < SPIN_CAP);
    }
    STAMP(1);
    v4u ov;
#pragma unroll
    for (int e = 0; e < 4; ++e) {
#pragma unroll
      for (int k = 1; k < 4; ++k) { o[2 * e] = fmaf(wk[k], bflo(pp[k][e]), o[2 * e]); o[2 * e + 1] = fmaf(wk[k], bfhi(pp[k][e]), o[2 * e + 1]); }
      ov[e] = cvtpk(o[2 * e], o[2 * e + 1]);
    }
    *(v4u*)((unsigned char*)out + ooff) = ov;
  } else if (UNLIKELY(bid == 255 && w == 7)) {
#pragma unroll 1
    for (unsigned it = 0; it < SPIN_CAP; ++it) {
      CBAR();
      unsigned bad = 0;
#pragma unroll
      for (int k = 0; k < 4; ++k) bad |= LD32(O_CL + (k * 64 + lane) * 128) ^ tag;
      if (__all(bad == 0)) break;
    }
    if (lane == 0) __hip_atomic_store((gu32*)(ws + O_EPOCH), tag, __ATOMIC_RELAXED, __HIP_MEMORY_SCOPE_AGENT);
  }
  STAMP(19);
  STFLUSH();
}

extern "C" void kernel_launch(void* const* d_in, const int* in_sizes, int n_in, void* d_out, int out_size, void* d_ws, size_t ws_size,
                              hipStream_t stream) {
  (void)hipFuncSetAttribute((const void*)dit_kernel, hipFuncAttributeMaxDynamicSharedMemorySize, L_TOTAL);
  dit_kernel<<<256, 512, L_TOTAL, stream>>>((const u16*)d_in[0], (const u16*)d_in[1], (const u16*)d_in[2], (const u16*)d_in[3],
                                            (const u16*)d_in[4], (unsigned char*)d_ws, (u16*)d_out);
}
```

```cpp
#include <hip/hip_runtime.h>
#include <hip/hip_bf16.h>

typedef short bf16x8 __attribute__((ext_vector_type(8)));
typedef short s16x4 __attribute__((ext_vector_type(4)));
typedef float f32x4 __attribute__((ext_vector_type(4)));
typedef float f32x2 __attribute__((ext_vector_type(2)));
typedef unsigned v4u __attribute__((ext_vector_type(4)));
typedef unsigned v2u __attribute__((ext_vector_type(2)));
typedef __bf16 bf2 __attribute__((ext_vector_type(2)));
typedef unsigned short u16;
#define DI __device__ __forceinline__
#define LAS __attribute__((address_space(3)))
#define GAS __attribute__((address_space(1)))
typedef GAS unsigned gu32;
typedef LAS unsigned char* lp;
#define STDECL
#define STAMP(i)
#define STFLUSH()
#define RETRY(k)
#define DLY0 3
#define DLY1 6
#define DLY2 2
#define PRE 1
#define HP 4
#define MP 1
#define TOUCH 1
#define CODEPF 14848
#define XS 0
#define DLY3 4
#define PD 4
#define SLP 2
#define ROT 0
#define NX1 10
#define NEARLY 0
#ifndef NV0
#define NV0 0
#endif
#define AUXO 16
#define NH 0
#ifndef NV2
#define NV2 16
#endif
#define HL 8
#define MC 1
#define DLY4 18
#define DLY5 8
#define BA 2
#define GATE 2
#define RL 8
#define MLP 12
#define PREWR 1
#define VR 9
#define VC 15
#define NH2 12
#define XL 2
#define SPLITB 1
#define XS0 2
#define XE 6
#ifndef NVS
#define NVS 12
#endif

constexpr int O_EPOCH = 0;
constexpr int O_MOD = 4096;
constexpr int O_CL = 16384;
constexpr int O_MT = 131072;
constexpr int O_NF = O_MT + 589824;
constexpr int O_QF = O_NF + 1572864;
constexpr int O_VF = O_QF + 1572864;
constexpr int O_OF = O_VF + 1572864;
constexpr int O_ML = O_OF + 6291456;
constexpr int O_RM = O_ML + 131072;
constexpr int O_RN = O_RM + 1152 * 128;
constexpr int O_RQ = O_RN + 512 * 128;
constexpr int O_RV = O_RQ + 1536 * 128;
constexpr int O_RO = O_RV + 1536 * 128;
constexpr int WS_BYTES = O_RO + 6144 * 128;
constexpr int OOB = 0x40000000;

constexpr int L_P0 = 0;
constexpr int L_MS = 61440, L_QS = L_MS + 24576, L_CS = L_QS + 24576, L_MX = L_CS + 512, L_LS = L_MX + 1024, L_BN = L_LS + 1024;
constexpr int L_SCR = L_BN + 6144, L_TAG = L_SCR + 256;
constexpr int L_TOTAL = L_TAG + 32;

static_assert(NV2 == 16 && NV0 + NVS <= NV2 && NV2 <= 24, "");
static_assert(XE + 3 * XS0 <= NX1 + 3 && NX1 <= 21 && (XL >= 2 || NX1 >= 9), "");
constexpr int AUX_SC1 = 16;
constexpr unsigned SPIN_CAP = 1u << 16;

DI float bflo(unsigned v) { return __uint_as_float(v << 16); }
DI float bfhi(unsigned v) { return __uint_as_float(v & 0xffff0000u); }
DI unsigned cvtpk(float lo, float hi) {
  f32x2 x = {lo, hi};
  return __builtin_bit_cast(unsigned, __builtin_convertvector(x, bf2));
}
DI float bfround(float x) { return bflo(cvtpk(x, 0.f)); }
DI unsigned pkhi(unsigned lo, unsigned hi) { return __builtin_amdgcn_perm(hi, lo, 0x07060302u); }
DI unsigned tagged(float v, unsigned tag16) { return (cvtpk(0.f, v) & 0xffff0000u) | tag16; }
DI unsigned tchk(unsigned a, v4u v, unsigned tag) { return a | (v[0] ^ tag) | (v[1] ^ tag) | (v[2] ^ tag) | (v[3] ^ tag); }
typedef unsigned short u16x2 __attribute__((ext_vector_type(2)));
DI unsigned sum2(unsigned a, unsigned x, unsigned y) { return __builtin_amdgcn_udot2(__builtin_bit_cast(u16x2, x), __builtin_bit_cast(u16x2, y), a, false); }
DI v2u sum4(v2u a, v4u v) { return v2u{sum2(a[0], v[0], v[1]), sum2(a[1], v[2], v[3])}; }
DI v2u sum4(unsigned a, v4u v) { return sum4(v2u{a, 0u}, v); }
DI unsigned scan63(unsigned v) {
  v += __builtin_amdgcn_update_dpp(0u, v, 0x111, 0xf, 0xf, true);
  v += __builtin_amdgcn_update_dpp(0u, v, 0x112, 0xf, 0xf, true);
  v += __builtin_amdgcn_update_dpp(0u, v, 0x114, 0xf, 0xf, true);
  v += __builtin_amdgcn_update_dpp(0u, v, 0x118, 0xf, 0xf, true);
  v += __builtin_amdgcn_update_dpp(0u, v, 0x142, 0xa, 0xf, true);
  v += __builtin_amdgcn_update_dpp(0u, v, 0x143, 0xc, 0xf, true);
  return v;
}
DI float sum_g(float v);
DI float qsumf(float v) {
#define FSTEP(ctl) v += __uint_as_float(__builtin_amdgcn_update_dpp(0u, __float_as_uint(v), ctl, 0xf, 0xf, true))
  FSTEP(0x128); FSTEP(0x124);
#undef FSTEP
  return sum_g(v);
}
DI unsigned wsum(unsigned v) { return __builtin_amdgcn_readlane(scan63(v), 63); }
DI unsigned wsum(v2u v, unsigned minus = 0u) { return wsum(v[0] + v[1] - minus); }
DI float max3(float a, float b, float c) { float r; asm("v_max3_f32 %0, %1, %2, %3" : "=v"(r) : "v"(a), "v"(b), "v"(c)); return r; }
DI float max2(float a, float b) { float r; asm("v_max_f32 %0, %1, %2" : "=v"(r) : "v"(a), "v"(b)); return r; }
DI float dot2(unsigned a, unsigned b, float c) { return __builtin_amdgcn_fdot2_f32_bf16(__builtin_bit_cast(bf2, a), __builtin_bit_cast(bf2, b), c, false); }
DI f32x4 mfma(v4u a, v4u b, f32x4 c) {
  return __builtin_amdgcn_mfma_f32_16x16x32_bf16(__builtin_bit_cast(bf16x8, a), __builtin_bit_cast(bf16x8, b), c, 0, 0, 0);
}
DI float sum_g(float v) {
  auto r = __builtin_amdgcn_permlane16_swap(__float_as_uint(v), __float_as_uint(v), false, false);
  v = __uint_as_float(r[0]) + __uint_as_float(r[1]);
  auto q = __builtin_amdgcn_permlane32_swap(__float_as_uint(v), __float_as_uint(v), false, false);
  return __uint_as_float(q[0]) + __uint_as_float(q[1]);
}
DI float max_g(float v) {
  auto r = __builtin_amdgcn_permlane16_swap(__float_as_uint(v), __float_as_uint(v), false, false);
  v = max2(__uint_as_float(r[0]), __uint_as_float(r[1]));
  auto q = __builtin_amdgcn_permlane32_swap(__float_as_uint(v), __float_as_uint(v), false, false);
  return max2(__uint_as_float(q[0]), __uint_as_float(q[1]));
}
DI int sopq(int v) { v = __builtin_amdgcn_readfirstlane(v); asm volatile("" : "+s"(v)); return v; }
template <typename T> DI T opq(T v) { asm volatile("" : "+v"(v)); return v; }
#define BAR() do { asm volatile("s_waitcnt lgkmcnt(0)" ::: "memory"); __builtin_amdgcn_s_barrier(); asm volatile("" ::: "memory"); } while (0)
#define VMWAIT(n) asm volatile("s_waitcnt vmcnt(%0)" :: "i"(n) : "memory")
#define SB() __builtin_amdgcn_sched_barrier(0)
#define CBAR() asm volatile("" ::: "memory")
#define LD128(off, so) __builtin_amdgcn_raw_buffer_load_b128(rws, (off), (so), AUX_SC1)
#define LD64(off) __builtin_amdgcn_raw_buffer_load_b64(rws, (off), 0, AUX_SC1)
#define LD32(off) __builtin_amdgcn_raw_buffer_load_b32(rws, (off), 0, AUX_SC1)
#define ST128(v, off) __builtin_amdgcn_raw_buffer_store_b128((v), rws, (off), 0, AUX_SC1)
#define ST128S(v, off, so) __builtin_amdgcn_raw_buffer_store_b128((v), rws, (off), (so), AUX_SC1)
#define ST64(v, off) __builtin_amdgcn_raw_buffer_store_b64((v), rws, (off), 0, AUX_SC1)
#define ST32(v, off) __builtin_amdgcn_raw_buffer_store_b32((v), rws, (off), 0, AUX_SC1)
#define ST16(v, off) __builtin_amdgcn_raw_buffer_store_b16((v), rws, (off), 0, AUX_SC1)
#define RST(sum, off) ST128((v4u{(sum) + tag, tag, tag, tag}), (off))
#define RECOK(v, off) __all((off) >= OOB || (v) == tag)
#define WAITREC(off) do { unsigned w_ = 0; CBAR(); while (!RECOK(LD32((off) + 4), (off)) && ++w_ < SPIN_CAP) { __builtin_amdgcn_s_sleep(2); CBAR(); } CBAR(); } while (0)
#define LDS128(p, off) (*(LAS v4u*)((p) + (off)))
#define LDSF4(p, off) (*(LAS f32x4*)((p) + (off)))
DI void dma(unsigned ldsaddr, int voff, __amdgpu_buffer_rsrc_t rs, int soff) {
  asm volatile("s_mov_b32 m0, %0\n\ts_nop 0\n\tbuffer_load_dwordx4 %1, %2, %3 offen lds" :: "s"(ldsaddr), "v"(voff), "s"(rs), "s"(soff) : "memory");
}

DI void touch(unsigned ldsaddr, int voff, __amdgpu_buffer_rsrc_t rs) {
  asm volatile("s_mov_b32 m0, %0\n\ts_nop 0\n\tbuffer_load_dword %1, %2, 0 offen lds" :: "s"(ldsaddr), "v"(voff), "s"(rs) : "memory");
}
#define UNLIKELY(c) __builtin_expect(!!(c), 0)

__global__ __launch_bounds__(512, 2) void dit_kernel(const u16* __restrict__ x, const u16* __restrict__ c, const u16* __restrict__ wqkv,
                                                     const u16* __restrict__ wo, const u16* __restrict__ wmod, unsigned char* ws,
                                                     u16* __restrict__ out) {
  extern __shared__ __attribute__((aligned(16))) unsigned char lds_[];
  lp lds = (lp)lds_;
  const unsigned ldsb = (unsigned)(size_t)lds;
  STDECL;
  STAMP(0);
  const unsigned long long pc0 = __builtin_amdgcn_s_getpc();
  const int tid = threadIdx.x, lane = tid & 63, w = __builtin_amdgcn_readfirstlane(tid >> 6);
  const int n16 = lane & 15, g = lane >> 4;
  const int bid = blockIdx.x, xg = bid & 7, r = bid >> 3;
  const int b = xg >> 2, ql = r & 7, kb = r >> 3;
  const int qt = b * 32 + (xg & 3) * 8 + ql;
  const int qt3 = b * 32 + (((xg & 3) + XS) & 3) * 8 + ql;
  const bool act1 = ql < 6;
  const int cb = (xg & 3) * 6 + (act1 ? ql : ql - 2);
  const __amdgpu_buffer_rsrc_t rws = __builtin_amdgcn_make_buffer_rsrc(ws, 0, WS_BYTES, 0x00020000);
  const __amdgpu_buffer_rsrc_t rx = __builtin_amdgcn_make_buffer_rsrc((void*)x, 0, 2048 * 768, 0x00020000);
  const int dch = 16 * ((lane & 3) ^ ((lane & 32) ? 3 : 0));
  const int rslot = (4 * n16 + (g ^ ((n16 & 8) ? 3 : 0))) * 16;
  const int l63 = lane < RL ? lane * 16 : OOB;

  const int mu = (((r - 24) >> 1) * 8 + xg) * 2 + (r & 1);
  const int role = r < 12 ? 0 : ((r >= 24 && mu < 36) ? 2 : 1);
  const int dead0 = (r >= 24 && mu >= 36) ? OOB : 0;
  const int ru = r < 12 ? r : (r < 24 ? r - 12 : r - 24);
  const int u12 = 6 * (xg & 1) + ru % 6, u8 = 2 * (xg >> 1) + ru / 6;
  unsigned ep;
  asm volatile("s_nop 4\n\tbuffer_load_dword %0, off, %1, 0 sc1" : "=v"(ep) : "s"(rws) : "memory");
  if (w < 6) {
    unsigned cv = 0;
    if (role == 2) cv = ((const unsigned*)c)[tid];
    if (role < 2) {
      const __amdgpu_buffer_rsrc_t rq = __builtin_amdgcn_make_buffer_rsrc((void*)wqkv, 0, 384 * 2304, 0x00020000);
      const int v = (lane >> 2) * 2304 + dch;
      const int s3 = 48 * u8 * 2304 + (role ? 1536 : 768) + 128 * w;
#pragma unroll
      for (int t = 0; t < 3; ++t) {
        dma(ldsb + L_P0 + (t * 12 + 2 * w) * 1024, v, rq, s3 + 16 * t * 2304);
        dma(ldsb + L_P0 + (t * 12 + 2 * w + 1) * 1024, v, rq, s3 + 16 * t * 2304 + 64);
      }
      if (role == 0) {
        const int s2 = 32 * u12 * 2304 + 128 * w;
#pragma unroll
        for (int t = 0; t < 2; ++t) {
          dma(ldsb + L_P0 + ((3 + t) * 12 + 2 * w) * 1024, v, rq, s2 + 16 * t * 2304);
          dma(ldsb + L_P0 + ((3 + t) * 12 + 2 * w + 1) * 1024, v, rq, s2 + 16 * t * 2304 + 64);
        }
      }
    }
    if (role == 1 || role == 2) {
      const int ld = role == 1 ? 768 : 4608;
      const __amdgpu_buffer_rsrc_t rc = __builtin_amdgcn_make_buffer_rsrc((void*)(role == 1 ? wo : wmod), 0, 384 * ld, 0x00020000);
      const int v = (lane >> 2) * ld + dch;
#pragma unroll
      for (int j = 0; j < 4; ++j) dma(ldsb + L_P0 + (36 + 4 * w + j) * 1024, v, rc, (role == 1 ? u12 : mu) * 64 + (4 * w + j) * 16 * ld);
    }
    if (role == 2) {
      const float f0 = bflo(cv), f1 = bfhi(cv);
      const int bb = tid >= 192, i2 = 2 * (tid - 192 * bb);
      *(LAS unsigned*)(lds + L_P0 + (i2 >> 5) * 1024 + (bb * 4 + ((i2 >> 3) & 3)) * 16 + (i2 & 7) * 2) =
          cvtpk(f0 * __builtin_amdgcn_rcpf(1.f + __expf(-f0)), f1 * __builtin_amdgcn_rcpf(1.f + __expf(-f1)));
    }
    VMWAIT(0);
    __builtin_amdgcn_s_waitcnt(0x0F70);
  } else {
    __builtin_amdgcn_s_setprio(3);
    v4u xs[3];
    asm volatile("buffer_load_dwordx4 %0, %3, %4, 0 offen\n\tbuffer_load_dwordx4 %1, %3, %4, 0 offen offset:256\n\tbuffer_load_dwordx4 %2, %3, %4, 0 offen offset:512"
                 : "=&v"(xs[0]), "=&v"(xs[1]), "=&v"(xs[2]) : "v"((32 * (bid >> 2) + 16 * (bid & 1) + 8 * (w & 1) + 2 * (bid & 2) + (lane & 3)) * 768 + (lane >> 2) * 16), "s"(rx) : "memory");
    {
      const unsigned scr = ldsb + L_SCR;
      const int dead = ((TOUCH & 1) && lane < 36) ? 0 : OOB;
      const __amdgpu_buffer_rsrc_t rq = __builtin_amdgcn_make_buffer_rsrc((void*)wqkv, 0, 384 * 2304, 0x00020000);
      touch(scr, dead + (w == 6 ? (96 * (xg >> 1) + 3 * r + lane / 12) * 2304 + 768 + (lane % 12) * 128 : (192 * (xg & 1) + 6 * r + lane / 6) * 2304 + (lane % 6) * 128), rq);
      const __amdgpu_buffer_rsrc_t r2 = __builtin_amdgcn_make_buffer_rsrc(w == 6 ? (void*)wo : (void*)(pc0 & ~127ull), 0, w == 6 ? 384 * 768 : CODEPF, 0x00020000);
      touch(scr, w == 6 ? dead + (12 * r + lane / 3) * 768 + 384 * (xg & 1) + (lane % 3) * 128 : (lane < 5 ? (r * 5 + lane) * 128 : OOB), r2);
    }
    if (w == 7) {
      const __amdgpu_buffer_rsrc_t r3 = __builtin_amdgcn_make_buffer_rsrc((void*)wmod, 0, 384 * 4608, 0x00020000);
      touch(ldsb + L_SCR, (lane < (xg < 2 ? 36 : 24) ? 0 : OOB) + (12 * r + lane % 12) * 4608 + ((lane / 12) * 8 + xg) * 128, r3);
    }
    else touch(ldsb + L_SCR, OOB, rx);
    asm volatile("s_waitcnt vmcnt(3)" : "+v"(xs[0]), "+v"(xs[1]), "+v"(xs[2]) :: "memory");
    if (PREWR & 2) ST128((v4u{0u, 0u, 0u, 0u}), l63 + O_RN + (bid * 2 + (w & 1)) * 128);
    float f[24], sm = 0.f, q = 0.f;
#pragma unroll
    for (int i = 0; i < 12; ++i) { f[2 * i] = bflo(xs[i >> 2][i & 3]); f[2 * i + 1] = bfhi(xs[i >> 2][i & 3]); sm += f[2 * i] + f[2 * i + 1]; }
    sm = qsumf(sm) * (1.f / 384.f);
#pragma unroll
    for (int i = 0; i < 24; ++i) { f[i] -= sm; q = fmaf(f[i], f[i], q); }
    const float rs = __builtin_amdgcn_rsqf(qsumf(q) * (1.f / 384.f) + 1e-5f);
#pragma unroll
    for (int i = 0; i < 3; ++i)
      LDS128(lds, L_BN + ((lane >> 2) + 16 * i) * 128 + ((w & 1) * 4 + (lane & 3)) * 16) =
          v4u{cvtpk(f[8 * i] * rs, f[8 * i + 1] * rs), cvtpk(f[8 * i + 2] * rs, f[8 * i + 3] * rs), cvtpk(f[8 * i + 4] * rs, f[8 * i + 5] * rs), cvtpk(f[8 * i + 6] * rs, f[8 * i + 7] * rs)};
  }
  asm volatile("" : "+v"(ep));
  const unsigned tag = __builtin_amdgcn_readfirstlane(ep) + 1u;
  STAMP(2);
  BAR();
  STAMP(3);
  if (w >= 6) {
    v4u nv[3];
#pragma unroll
    for (int i = 0; i < 3; ++i) nv[i] = LDS128(lds, L_BN + (w & 1) * 3072 + i * 1024 + lane * 16);
    const int no = O_NF + (bid >> 1) * 12288 + ((w & 1) * 24 + (lane >> 3)) * 256 + (bid & 1) * 128 + (lane & 7) * 16;
#pragma unroll
    for (int i = 0; i < 3; ++i) ST128(nv[i], no + i * 2048);
    VMWAIT(0);
    RST(0u, l63 + O_RN + (bid * 2 + (w & 1)) * 128);
    __builtin_amdgcn_s_setprio(0);
  }
  const int rg = b * 32 + kb * 8 + w;
  const int nro = lane < 8 ? O_RN + (rg * 8 + lane) * 128 : OOB;
  if (w < (role == 2 ? 2 : 6)) {
    const int t3 = w >> 1, t2 = w & 1;
    f32x4 acc = {};
    v4u a[12], bq[12];
    if (role == 0) {
      lp ap = opq(lds + L_P0 + (3 + t2) * 12288 + rslot), bp = opq(lds + L_P0 + t3 * 12288 + rslot);
#pragma unroll
      for (int k = 0; k < 12; ++k) {
        a[k] = LDS128(ap, k * 1024); bq[k] = LDS128(bp, k * 1024);
        if (k >= 5) acc = mfma(a[k - 5], bq[k - 5], acc);
        SB();
      }
    } else {
      lp ap = opq(lds + L_P0 + (role == 2 ? ((n16 & 1) * 4 + g) * 16 : t3 * 12288 + rslot));
      lp tb = opq(lds + L_P0 + 36864 + (8 * g + (n16 >> 2)) * 64 + 16 * ((2 * t2 + ((n16 >> 1) & 1)) ^ ((g & 1) ? 3 : 0)) + 8 * (n16 & 1));
#pragma unroll
      for (int k = 0; k < 12; ++k) {
        a[k] = LDS128(ap, k * 1024);
        bq[k] = __builtin_bit_cast(v4u, __builtin_shufflevector(__builtin_amdgcn_ds_read_tr16_b64_v4i16((LAS s16x4*)(tb + k * 2048)),
                                                                __builtin_amdgcn_ds_read_tr16_b64_v4i16((LAS s16x4*)(tb + k * 2048 + 256)), 0, 1, 2, 3, 4, 5, 6, 7));
        if (k >= 5) acc = mfma(a[k - 5], bq[k - 5], acc);
        SB();
      }
    }
#pragma unroll
    for (int k = 7; k < 12; ++k) acc = mfma(a[k], bq[k], acc);
    if (role != 2) {
      const int blk = role * 576 + (role == 0 ? (3 * u8 + t3) * 24 + 2 * u12 + t2 : (2 * u12 + t2) * 24 + 3 * u8 + t3);
      const unsigned o0 = cvtpk(acc[0], acc[1]), o1 = cvtpk(acc[2], acc[3]);
      auto r0 = __builtin_amdgcn_permlane16_swap(o0, o0, false, false);
      auto r1 = __builtin_amdgcn_permlane16_swap(o1, o1, false, false);
      ST128((v4u{r0[0], r1[0], r0[1], r1[1]}), (((g & 1) ? OOB : 0) | dead0) + O_MT + blk * 512 + ((g >> 1) * 16 + n16) * 16);
      RST(wsum(sum2(0u, o0, o1)), (l63 | dead0) + O_RM + blk * 128);
    } else if (g < 2) {
      float v = bfround(g ? acc[1] : acc[0]);
      if (mu >= 12 && mu < 24) v += 1.f;
      ST32(tagged(v, tag & 0xffffu), O_MOD + (2 * mu + t2) * 128 + lane * 4);
    }
  }
  STAMP(4);
  __builtin_amdgcn_s_barrier();
  const int l16 = opq(lane * 16);
  const int s_n = O_NF + rg * 24576;
  const int rot = ROT ? (ql * 3) >> 1 : 0;
  const int l16b = opq(lane * 16 + 12288);
  v4u xf[2][12];
#define XF(i) xf[(i) & 1][(i) >> 1]
#define XLOAD(i) XF(i) = LD128(((i) & 1) ? l16b : l16, s_n + (((i) >> 1) + rot - (((i) >> 1) + rot >= 12 ? 12 : 0)) * 1024)
  lp fl = opq(lds + L_MS + lane * 16);
  v4u qf[3]; unsigned qr, vsum; int rvo;
  const int qro = opq(lane < 3 ? O_RQ + (qt * 24 + 3 * w + lane) * 128 : OOB);
  const int s_ql = sopq(O_QF + (qt * 24 + 3 * w) * 1024);
#define HOP2_LOADS() do { _Pragma("unroll") for (int i = 0; i < 3; ++i) qf[i] = LD128(l16 + i * 1024, s_ql); qr = LD32(qro); } while (0)
  {
    const int s0 = 3 * (w & 3);
    const int mc4 = lane < 24 ? 96 + 8 * s0 + lane : (lane < 48 ? 8 * s0 + lane - 24 : (w < 4 ? 96 : 192) + 4 * cb + (lane & 3));
    const int mo = opq(O_MOD + (mc4 >> 2) * 128 + b * 64 + (mc4 & 3) * 16);
    const int fb = (w >> 2) * 576 + cb * 24 + 2 * s0;
    const int ro = opq(lane < 6 ? O_RM + (fb + lane) * 128 : OOB);
    const int s_f = sopq(O_MT + fb * 512);
    const int blk1 = rg * 24 + cb, dead1 = act1 ? 0 : OOB;
    const int qso = opq(dead1 + O_QF + blk1 * 1024 + ((g & 1) * 32 + (g >> 1) * 16 + n16) * 16);
    const int vso = opq(dead1 + O_VF + blk1 * 1024 + lane * 16);
    const int rso = opq((dead1 | l63) + O_RQ + blk1 * 128);
    rvo = opq((dead1 | l63) + O_RV + (((b * 4 + kb) * 24 + cb) * 8 + w) * 128);
    lp cg = opq(lds + L_CS + g * 16), cn = opq(lds + L_CS + 256 + n16 * 4), csw = opq(lds + L_CS + w * 64 + n16 * 4);
    lp bn = lds + L_BN + w * 768, bg = opq(bn + g * 32), msw[3];
#pragma unroll
    for (int i = 0; i < 3; ++i) { int sl = s0 + i - rot; if (sl < 0) sl += 12; msw[i] = opq(fl + (w >> 2) * 12288 + sl * 1024); }
    SB();
    v4u mf[3], mv4; unsigned mr;
#define HOP1_LOADS() do { _Pragma("unroll") for (int i = 0; i < 3; ++i) mf[i] = LD128(l16 + i * 1024, s_f); mr = LD32(ro); } while (0)
    if (DLY1) __builtin_amdgcn_s_sleep(DLY1);
    mv4 = LD128(mo, 0);
    HOP1_LOADS();
    if (DLY0) __builtin_amdgcn_s_sleep(DLY0);
    const unsigned nf = LD32(nro + 4);
    SB();
    f32x4 sa[3], sb[3]; v4u tf[3]; float oc_;
#define MODOK() __all((tchk(0u, mv4, tag) & 0xffffu) == 0u)
    if (UNLIKELY(!MODOK())) {
      unsigned it = 0;
      do { RETRY(1); __builtin_amdgcn_s_sleep(3); CBAR(); mv4 = LD128(mo, 0); } while (!MODOK() && ++it < SPIN_CAP);
    }
    if (lane >= 24 && lane < 48) *(LAS v2u*)(bn + 512 + (lane - 24) * 8) = v2u{pkhi(mv4[0], mv4[1]), pkhi(mv4[2], mv4[3])};
    else LDS128(bn, (lane < 24 ? lane : (lane & 3) + 24) * 16) = mv4 & 0xffff0000u;
    CBAR();
#pragma unroll
    for (int k = 0; k < 3; ++k) { sa[k] = LDSF4(bg, 128 * k); sb[k] = LDSF4(bg, 128 * k + 16); tf[k] = LDS128(bn, 512 + 64 * k + 16 * g); }
    oc_ = *(LAS float*)(bn + 384 + n16 * 4);
    if (UNLIKELY(!RECOK(nf, nro))) { RETRY(0); WAITREC(nro); }
    CBAR();
    STAMP(5);
#pragma unroll
    for (int i = 0; i < XE; ++i) XLOAD(i);
    SB();
#define FBAD() (wsum(sum4(sum4(sum4(-mr, mf[0]), mf[1]), mf[2])) + 6u * tag != 0u)
    if (UNLIKELY(FBAD())) {
      unsigned it = 0;
      do { RETRY(9); __builtin_amdgcn_s_sleep(3); CBAR(); HOP1_LOADS(); } while (FBAD() && ++it < SPIN_CAP);
    }
    if (w < 4) oc_ *= 0.05103103630798288f * 1.4426950408889634f;
    {
      f32x4 au = {};
#pragma unroll
      for (int i = 0; i < 3; ++i) {
#pragma unroll
        for (int x_ = 0; x_ < XS0; ++x_) XLOAD(XE + XS0 * i + x_);
        au = mfma(tf[i], mf[i], au);
        const f32x4 a_ = sa[i] * oc_, b_ = sb[i] * oc_;
        const v4u o = {cvtpk(bflo(mf[i][0]) * a_[0], bfhi(mf[i][0]) * a_[1]), cvtpk(bflo(mf[i][1]) * a_[2], bfhi(mf[i][1]) * a_[3]),
                       cvtpk(bflo(mf[i][2]) * b_[0], bfhi(mf[i][2]) * b_[1]), cvtpk(bflo(mf[i][3]) * b_[2], bfhi(mf[i][3]) * b_[3])};
        LDS128(msw[i], 0) = o;
        SB();
      }
      *(LAS float*)(csw) = au[0] * oc_;
    }
#pragma unroll
    for (int i = XE + 3 * XS0; i < NX1 + 3; ++i) XLOAD(i);
    STAMP(9);
    __builtin_amdgcn_s_setprio(2);
    BAR();
    v4u a[24];
    f32x4 uu[4]; float zz[4], zs;
#define AOP(k) do { if ((k) + PD < ((k) < 12 ? 12 : 24)) a[(k) + PD] = LDS128(fl, ((k) + PD) * 1024); \
      if ((k) < 12) { aq[0] = mfma(a[k], xf[0][(k) % 12], aq[0]); aq[1] = mfma(a[k], xf[1][(k) % 12], aq[1]); } \
      else { av[0] = mfma(xf[0][(k) % 12], a[k], av[0]); av[1] = mfma(xf[1][(k) % 12], a[k], av[1]); } } while (0)
#pragma unroll
    for (int k = 0; k < PD; ++k) a[k] = LDS128(fl, k * 1024);
    SB();
    f32x4 aq[2] = {}, av[2] = {};
#pragma unroll
    for (int k = 0; k < 12; ++k) {
#pragma unroll
      for (int x_ = 0; x_ < XL; ++x_) if (NX1 + 3 + XL * k + x_ < 24) XLOAD(NX1 + 3 + XL * k + x_);
      if (k >= 2 && k < 6) uu[k - 2] = LDSF4(cg, (k - 2) * 64);
      AOP(k);
      if (k == 9) { uu[0] = uu[0] + uu[1]; uu[2] = uu[2] + uu[3]; }
      if (k == 10) uu[0] = uu[0] + uu[2];
      SB();
    }
    {
      const f32x4 v0 = aq[0] + uu[0], v1 = aq[1] + uu[0];
      auto r0 = __builtin_amdgcn_permlane16_swap(cvtpk(v0[0], v0[1]), cvtpk(v1[0], v1[1]), false, false);
      auto r1 = __builtin_amdgcn_permlane16_swap(cvtpk(v0[2], v0[3]), cvtpk(v1[2], v1[3]), false, false);
      const v4u ov = {r0[0], r1[0], r0[1], r1[1]};
      ST128(ov, qso);
      RST(wsum(sum4(0u, ov)), rso);
      __builtin_amdgcn_s_setprio(0);
      STAMP(12);
#pragma unroll
      for (int k = 12; k < 12 + PD; ++k) a[k] = LDS128(fl, k * 1024);
      SB();
#pragma unroll
      for (int k = 12; k < 24; ++k) {
        if (k >= 14 && k < 18) zz[k - 14] = *(LAS float*)(cn + (k - 14) * 64);
        AOP(k);
        if (k == 21) zs = (zz[0] + zz[1]) + (zz[2] + zz[3]);
        SB();
      }
      const f32x4 w0 = av[0] + zs, w1 = av[1] + zs;
      const v4u o = {cvtpk(w0[0], w0[1]), cvtpk(w0[2], w0[3]), cvtpk(w1[0], w1[1]), cvtpk(w1[2], w1[3])};
      ST128(o, vso);
      vsum = wsum(sum4(0u, o));
    }
    STAMP(10);
    SB();
  }
  STAMP(11);
  if (w == 7) ST128((v4u{tag, tag, tag, tag}), l63 + O_CL + bid * 128);
  const int vro = opq(lane < 24 ? O_RV + (((b * 4 + kb) * 8 + w) * 24 + lane) * 128 : OOB);
  const int mso = opq((w == 0 && g < 2) ? O_ML + ((qt * 4 + kb) * 32 + lane) * 16 : OOB);
  const int s_vl = sopq(O_VF + ((b * 32 + 8 * kb) * 24 + 3 * w) * 1024);
  const bool own = (w >> 1) == kb;
  const int oso = opq((own ? OOB : 0) + O_OF + ((qt * 24 + 3 * w) * 4 + kb) * 1024 + ((g & 1) * 32 + (g >> 1) * 16 + n16) * 16);
  const int roo = opq(((own ? OOB : 0) | l63) + O_RO + ((qt * 24 + 3 * w) * 4 + kb) * 128);
  lp mxr = opq(lds + L_MX + n16 * 32), mxw = opq(lds + L_MX + n16 * 32 + w * 4);
  lp plw = opq(lds + lane * 16 + w * 2048);
  lp pl = opq(lds + lane * 16);
  const int ooff = opq(((qt * 32 + 8 * (n16 >> 2) + 4 * (lane >> 5) + (n16 & 3)) * 384 + (6 * kb + w) * 16 + 8 * (g & 1)) * 2);
  const v4u xr = __builtin_amdgcn_raw_buffer_load_b128(rx, w < 6 ? ooff : OOB, 0, 0);
  const int mlo = opq(O_ML + (qt * 128 + (lane >> 5) * 16 + n16) * 16);
  const int s_ol = sopq(O_OF + (qt * 24 + 6 * kb + w) * 4096), rlo = opq((lane < 4 && lane != kb) ? O_RO + ((qt * 24 + 6 * kb + w) * 4 + lane) * 128 : OOB);
  if (PREWR & 1) {
    const unsigned nt_ = ~tag;
    ST128((v4u{nt_, nt_, nt_, nt_}), lane < 24 ? O_RO + ((qt * 24 + 3 * w + (lane >> 3)) * 4 + kb) * 128 + (lane & 7) * 16 : ((w == 0 && lane >= 32) ? O_ML + ((qt * 4 + kb) * 32 + lane - 32) * 16 : OOB));
  }
  v4u vf[8][3];
#define VF(i) vf[(i) & 7][(i) >> 3]
#define VLOAD(i) VF(i) = LD128(l16 + ((i) >> 3) * 1024, s_vl + ((i) & 7) * 24576)
  {
    lp qw = opq(lds + L_QS + (lane >> 5) * 12288 + 3 * w * 512 + (lane & 31) * 16);
    SB();
    __builtin_amdgcn_s_barrier();
    if (DLY2) __builtin_amdgcn_s_sleep(DLY2);
    HOP2_LOADS();
#pragma unroll
    for (int i = 0; i < NV0; ++i) VLOAD(i);
    SB();
#define QBAD() (wsum(sum4(sum4(sum4(-qr, qf[0]), qf[1]), qf[2])) + 3u * tag != 0u)
#pragma unroll
    for (int i = 0; i < 3; ++i) LDS128(qw, i * 512) = qf[i];
    if (UNLIKELY(QBAD())) {
      unsigned it = 0;
      do { RETRY(2); __builtin_amdgcn_s_sleep(3); CBAR(); HOP2_LOADS(); } while (QBAD() && ++it < SPIN_CAP);
#pragma unroll
      for (int i = 0; i < 3; ++i) LDS128(qw, i * 512) = qf[i];
    }
  }
  VMWAIT(0);
  RST(vsum, rvo);
  STAMP(13);
  BAR();
  float m[2]; v2u vr, hv0 = {0u, 0u};
  {
    v4u q[24];
#define QREAD(i) q[i] = LDS128(fl, 24576 + (((i) & 1) * 12 + ((i) >> 1)) * 1024)
#pragma unroll
    for (int i = 0; i < PD; ++i) QREAD(i);
    SB();
    f32x4 acc[2][2] = {};
#pragma unroll
    for (int i = 0; i < 24; ++i) {
      if (i + PD < 24) QREAD(i + PD);
      acc[i & 1][0] = mfma(xf[0][i >> 1], q[i], acc[i & 1][0]); acc[i & 1][1] = mfma(xf[1][i >> 1], q[i], acc[i & 1][1]);
      if ((i * NVS) / 24 != ((i + 1) * NVS) / 24) VLOAD((i * NVS) / 24);
      if (i >= 24 - NH) hv0 = opq(sum4(hv0, VF(i - (24 - NH))));
      SB();
    }
#pragma unroll
    for (int u = 0; u < 2; ++u) {
      float mx = max3(max3(acc[u][0][0], acc[u][0][1], acc[u][0][2]), acc[u][0][3], acc[u][1][0]);
      mx = max_g(max2(max3(mx, acc[u][1][1], acc[u][1][2]), acc[u][1][3]));
      *(LAS float*)(mxw + u * 512) = mx;
    }
    STAMP(14);
    BAR();
    f32x4 ma[2], mb[2];
#pragma unroll
    for (int u = 0; u < 2; ++u) { ma[u] = LDSF4(mxr, u * 512); mb[u] = LDSF4(mxr, u * 512 + 16); }
    vr = LD64(vro);
    SB();
#pragma unroll
    for (int u = 0; u < 2; ++u) {
      m[u] = max2(max3(max3(ma[u][0], ma[u][1], ma[u][2]), ma[u][3], mb[u][0]), max3(mb[u][1], mb[u][2], mb[u][3]));
      float p[8];
#pragma unroll
      for (int i = 0; i < 8; ++i) p[i] = __builtin_amdgcn_exp2f(acc[u][i >> 2][i & 3] - m[u]);
      LDS128(plw, u * 1024) = v4u{cvtpk(p[0], p[1]), cvtpk(p[2], p[3]), cvtpk(p[4], p[5]), cvtpk(p[6], p[7])};
      if (NV0 + NVS + u < NV2) VLOAD(NV0 + NVS + u);
      SB();
    }
#pragma unroll
    for (int i = NV0 + NVS + 2; i < NV2; ++i) VLOAD(i);
  }
#pragma unroll
  for (int i = NH; i < NH2; ++i) hv0 = sum4(hv0, VF(i));
  hv0 = opq(hv0);
  if (UNLIKELY(!RECOK(vr[1], vro))) { RETRY(7); WAITREC(vro); vr = LD64(vro); }
  STAMP(15);
  BAR();
  v4u ml[4];
#define ML_LOADS() do { _Pragma("unroll") for (int k = 0; k < 4; ++k) ml[k] = LD128(mlo, ((kb + k) & 3) * 512); } while (0)
  {
    v4u pf[16];
    f32x4 acc[3][2] = {};
    v4u ov[3];
#define OSTORE(et) do { const f32x4 v0 = acc[et][0], v1 = acc[et][1]; \
      auto r0 = __builtin_amdgcn_permlane16_swap(cvtpk(v0[0], v0[1]), cvtpk(v1[0], v1[1]), false, false); \
      auto r1 = __builtin_amdgcn_permlane16_swap(cvtpk(v0[2], v0[3]), cvtpk(v1[2], v1[3]), false, false); \
      ov[et] = v4u{r0[0], r1[0], r0[1], r1[1]}; ST128S(ov[et], oso, (et) * 4096); \
      if (own) LDS128(lds + 16384 + ((w & 1) * 3 + (et)) * 1024, ((g & 1) * 32 + (g >> 1) * 16 + n16) * 16) = ov[et]; } while (0)
#define ORECORD(et) RST(wsum(sum4(0u, ov[et])), roo + (et) * 512)
#pragma unroll
    for (int i = 0; i < 8; ++i) pf[i] = LDS128(pl, i * 1024);
    SB();
#pragma unroll
    for (int i = 0; i < 16; ++i) {
      if (i + 8 < 16) pf[i + 8] = LDS128(pl, (i + 8) * 1024);
      if ((i & 1) && NV2 + (i >> 1) < 24) VLOAD(NV2 + (i >> 1));
      acc[0][i & 1] = mfma(vf[i >> 1][0], pf[i], acc[0][i & 1]);
      if (i >= NH2) hv0 = opq(sum4(hv0, VF(i)));
      SB();
    }
    if (w == 0) {
      const v4u ones = {0x3f803f80u, 0x3f803f80u, 0x3f803f80u, 0x3f803f80u};
      f32x4 ls[2] = {};
#pragma unroll
      for (int i = 0; i < 16; ++i) ls[i & 1] = mfma(ones, pf[i], ls[i & 1]);
      ST128((v4u{__float_as_uint(g ? m[1] : m[0]), tag, __float_as_uint(g ? ls[1][0] : ls[0][0]), tag}), mso);
    }
    if (UNLIKELY(wsum(hv0, lane < 16 ? vr[0] : 0u) + 16u * tag != 0u)) {
      RETRY(3); CBAR();
#pragma unroll
      for (int i = 0; i < 16; ++i) VLOAD(i);
      acc[0][0] = acc[0][1] = f32x4{0.f, 0.f, 0.f, 0.f};
#pragma unroll
      for (int i = 0; i < 16; ++i) acc[0][i & 1] = mfma(vf[i >> 1][0], pf[i], acc[0][i & 1]);
    }
#pragma unroll
    for (int i = 0; i < 16; ++i) {
      acc[1][i & 1] = mfma(vf[i >> 1][1], pf[i], acc[1][i & 1]);
      if (i == 0) OSTORE(0);
      if (i == 6) ORECORD(0);
      if ((i & 3) == 3) SB();
    }
#pragma unroll
    for (int i = 0; i < 16; ++i) {
      acc[2][i & 1] = mfma(vf[i >> 1][2], pf[i], acc[2][i & 1]);
      if (i == 0) OSTORE(1);
      if (i == 6) ORECORD(1);
      if (i == MLP) ML_LOADS();
      if ((i & 3) == 3) SB();
    }
    STAMP(16);
    OSTORE(2);
    ORECORD(2);
  }
  STAMP(17);
  BAR();
  if (w < 6) {
    v4u pp[4]; unsigned orr;
#define MLOK() __all(((ml[0][1] ^ tag) | (ml[1][1] ^ tag) | (ml[2][1] ^ tag) | (ml[3][1] ^ tag) | (ml[0][3] ^ tag) | (ml[1][3] ^ tag) | (ml[2][3] ^ tag) | (ml[3][3] ^ tag)) == 0u)
#define HOP3_LOADS() do { _Pragma("unroll") for (int k = 1; k < 4; ++k) pp[k] = LD128(l16, s_ol + ((kb + k) & 3) * 1024); orr = LD32(rlo); } while (0)
#define OBAD() (wsum(sum4(sum4(sum4(-orr, pp[1]), pp[2]), pp[3])) + 3u * tag != 0u)
    pp[0] = LDS128(pl, 16384 + w * 1024);
    SB();
    if (DLY3) __builtin_amdgcn_s_sleep(DLY3);
    if (UNLIKELY(!MLOK())) {
      unsigned it = 0;
      RETRY(6); WAITREC(rlo);
      do { CBAR(); ML_LOADS(); } while (!MLOK() && ++it < SPIN_CAP);
    }
    HOP3_LOADS();
    SB();
    STAMP(18);
    const float mm = max2(max2(__uint_as_float(ml[0][0]), __uint_as_float(ml[1][0])), max2(__uint_as_float(ml[2][0]), __uint_as_float(ml[3][0])));
    float wk[4], o[8];
#pragma unroll
    for (int k = 0; k < 4; ++k) wk[k] = __builtin_amdgcn_exp2f(__uint_as_float(ml[k][0]) - mm);
    const float inv = __builtin_amdgcn_rcpf((wk[0] * __uint_as_float(ml[0][2]) + wk[1] * __uint_as_float(ml[1][2])) + (wk[2] * __uint_as_float(ml[2][2]) + wk[3] * __uint_as_float(ml[3][2])));
#pragma unroll
    for (int k = 0; k < 4; ++k) wk[k] *= inv;
#pragma unroll
    for (int e = 0; e < 4; ++e) { o[2 * e] = opq(fmaf(wk[0], bflo(pp[0][e]), bflo(xr[e]))); o[2 * e + 1] = opq(fmaf(wk[0], bfhi(pp[0][e]), bfhi(xr[e]))); }
    SB();
    if (UNLIKELY(OBAD())) {
      unsigned it = 0;
      do { RETRY(5); WAITREC(rlo); HOP3_LOADS(); } while (OBAD() && ++it < SPIN_CAP);
    }
    STAMP(1);
    v4u ov;
#pragma unroll
    for (int e = 0; e < 4; ++e) {
#pragma unroll
      for (int k = 1; k < 4; ++k) { o[2 * e] = fmaf(wk[k], bflo(pp[k][e]), o[2 * e]); o[2 * e + 1] = fmaf(wk[k], bfhi(pp[k][e]), o[2 * e + 1]); }
      ov[e] = cvtpk(o[2 * e], o[2 * e + 1]);
    }
    *(v4u*)((unsigned char*)out + ooff) = ov;
  } else if (UNLIKELY(bid == 255 && w == 7)) {
#pragma unroll 1
    for (unsigned it = 0; it < SPIN_CAP; ++it) {
      CBAR();
      unsigned bad = 0;
#pragma unroll
      for (int k = 0; k < 4; ++k) bad |= LD32(O_CL + (k * 64 + lane) * 128) ^ tag;
      if (__all(bad == 0)) break;
    }
    if (lane == 0) __hip_atomic_store((gu32*)(ws + O_EPOCH), tag, __ATOMIC_RELAXED, __HIP_MEMORY_SCOPE_AGENT);
  }
  STAMP(19);
  STFLUSH();
}

extern "C" void kernel_launch(void* const* d_in, const int* in_sizes, int n_in, void* d_out, int out_size, void* d_ws, size_t ws_size,
                              hipStream_t stream) {
  (void)hipFuncSetAttribute((const void*)dit_kernel, hipFuncAttributeMaxDynamicSharedMemorySize, L_TOTAL);
  dit_kernel<<<256, 512, L_TOTAL, stream>>>((const u16*)d_in[0], (const u16*)d_in[1], (const u16*)d_in[2], (const u16*)d_in[3],
                                            (const u16*)d_in[4], (unsigned char*)d_ws, (u16*)d_out);
}
```

```cpp
#include <hip/hip_runtime.h>
#include <hip/hip_bf16.h>

typedef short bf16x8 __attribute__((ext_vector_type(8)));
typedef short s16x4 __attribute__((ext_vector_type(4)));
typedef float f32x4 __attribute__((ext_vector_type(4)));
typedef float f32x2 __attribute__((ext_vector_type(2)));
typedef unsigned v4u __attribute__((ext_vector_type(4)));
typedef unsigned v2u __attribute__((ext_vector_type(2)));
typedef __bf16 bf2 __attribute__((ext_vector_type(2)));
typedef unsigned short u16;
#define DI __device__ __forceinline__
#define LAS __attribute__((address_space(3)))
#define GAS __attribute__((address_space(1)))
typedef GAS unsigned gu32;
typedef LAS unsigned char* lp;

constexpr int DLY0 = 3, DLY1 = 6, DLY2 = 2, DLY3 = 4;
constexpr int PD = 4;
constexpr int XE = 6, XS0 = 2, NX1 = 13, XL = 2;
constexpr int NVS = 12, NH2 = 12, MLP = 12;
constexpr int CODEPF = 14848;

constexpr int O_EPOCH = 0;
constexpr int O_MOD = 4096;
constexpr int O_CL = 16384;
constexpr int O_MT = 131072;
constexpr int O_NF = O_MT + 589824;
constexpr int O_QF = O_NF + 1572864;
constexpr int O_VF = O_QF + 1572864;
constexpr int O_OF = O_VF + 1572864;
constexpr int O_ML = O_OF + 6291456;
constexpr int O_RM = O_ML + 131072;
constexpr int O_RN = O_RM + 1152 * 128;
constexpr int O_RQ = O_RN + 512 * 128;
constexpr int O_RV = O_RQ + 1536 * 128;
constexpr int O_RO = O_RV + 1536 * 128;
constexpr int WS_BYTES = O_RO + 6144 * 128;
constexpr int OOB = 0x40000000;

constexpr int L_P0 = 0;
constexpr int L_MS = 61440, L_QS = L_MS + 24576, L_CS = L_QS + 24576, L_MX = L_CS + 512, L_BN = L_MX + 1024;
constexpr int L_SCR = L_BN + 6144;
constexpr int L_TOTAL = L_SCR + 256;

static_assert(NH2 <= NVS && NVS <= 16, "");
static_assert(XE + 3 * XS0 <= NX1 && NX1 <= 24 && XL >= 2, "");
constexpr int AUX_SC1 = 16;
constexpr unsigned SPIN_CAP = 1u << 18;

DI float bflo(unsigned v) { return __uint_as_float(v << 16); }
DI float bfhi(unsigned v) { return __uint_as_float(v & 0xffff0000u); }
DI unsigned cvtpk(float lo, float hi) {
  f32x2 x = {lo, hi};
  return __builtin_bit_cast(unsigned, __builtin_convertvector(x, bf2));
}
DI float bfround(float x) { return bflo(cvtpk(x, 0.f)); }
DI unsigned pkhi(unsigned lo, unsigned hi) { return __builtin_amdgcn_perm(hi, lo, 0x07060302u); }
DI unsigned tagged(float v, unsigned tag16) { return (cvtpk(0.f, v) & 0xffff0000u) | tag16; }
DI unsigned tchk(unsigned a, v4u v, unsigned tag) { return a | (v[0] ^ tag) | (v[1] ^ tag) | (v[2] ^ tag) | (v[3] ^ tag); }
typedef unsigned short u16x2 __attribute__((ext_vector_type(2)));
DI unsigned sum2(unsigned a, unsigned x, unsigned y) { return __builtin_amdgcn_udot2(__builtin_bit_cast(u16x2, x), __builtin_bit_cast(u16x2, y), a, false); }
DI v2u sum4(v2u a, v4u v) { return v2u{sum2(a[0], v[0], v[1]), sum2(a[1], v[2], v[3])}; }
DI v2u sum4(unsigned a, v4u v) { return sum4(v2u{a, 0u}, v); }
DI unsigned scan63(unsigned v) {
  v += __builtin_amdgcn_update_dpp(0u, v, 0x111, 0xf, 0xf, true);
  v += __builtin_amdgcn_update_dpp(0u, v, 0x112, 0xf, 0xf, true);
  v += __builtin_amdgcn_update_dpp(0u, v, 0x114, 0xf, 0xf, true);
  v += __builtin_amdgcn_update_dpp(0u, v, 0x118, 0xf, 0xf, true);
  v += __builtin_amdgcn_update_dpp(0u, v, 0x142, 0xa, 0xf, true);
  v += __builtin_amdgcn_update_dpp(0u, v, 0x143, 0xc, 0xf, true);
  return v;
}
DI float sum_g(float v);
DI float qsumf(float v) {
#define FSTEP(ctl) v += __uint_as_float(__builtin_amdgcn_update_dpp(0u, __float_as_uint(v), ctl, 0xf, 0xf, true))
  FSTEP(0x128); FSTEP(0x124);
#undef FSTEP
  return sum_g(v);
}
DI unsigned wsum(unsigned v) { return __builtin_amdgcn_readlane(scan63(v), 63); }
DI unsigned wsum(v2u v, unsigned minus = 0u) { return wsum(v[0] + v[1] - minus); }
DI float max3(float a, float b, float c) { float r; asm("v_max3_f32 %0, %1, %2, %3" : "=v"(r) : "v"(a), "v"(b), "v"(c)); return r; }
DI float max2(float a, float b) { float r; asm("v_max_f32 %0, %1, %2" : "=v"(r) : "v"(a), "v"(b)); return r; }
DI f32x4 mfma(v4u a, v4u b, f32x4 c) {
  return __builtin_amdgcn_mfma_f32_16x16x32_bf16(__builtin_bit_cast(bf16x8, a), __builtin_bit_cast(bf16x8, b), c, 0, 0, 0);
}
DI float sum_g(float v) {
  auto r = __builtin_amdgcn_permlane16_swap(__float_as_uint(v), __float_as_uint(v), false, false);
  v = __uint_as_float(r[0]) + __uint_as_float(r[1]);
  auto q = __builtin_amdgcn_permlane32_swap(__float_as_uint(v), __float_as_uint(v), false, false);
  return __uint_as_float(q[0]) + __uint_as_float(q[1]);
}
DI float max_g(float v) {
  auto r = __builtin_amdgcn_permlane16_swap(__float_as_uint(v), __float_as_uint(v), false, false);
  v = max2(__uint_as_float(r[0]), __uint_as_float(r[1]));
  auto q = __builtin_amdgcn_permlane32_swap(__float_as_uint(v), __float_as_uint(v), false, false);
  return max2(__uint_as_float(q[0]), __uint_as_float(q[1]));
}
DI int sopq(int v) { v = __builtin_amdgcn_readfirstlane(v); asm volatile("" : "+s"(v)); return v; }
template <typename T> DI T opq(T v) { asm volatile("" : "+v"(v)); return v; }
#define BAR() do { asm volatile("s_waitcnt lgkmcnt(0)" ::: "memory"); __builtin_amdgcn_s_barrier(); asm volatile("" ::: "memory"); } while (0)
#define VMWAIT(n) asm volatile("s_waitcnt vmcnt(%0)" :: "i"(n) : "memory")
#define SB() __builtin_amdgcn_sched_barrier(0)
#define CBAR() asm volatile("" ::: "memory")
#define LD128(off, so) __builtin_amdgcn_raw_buffer_load_b128(rws, (off), (so), AUX_SC1)
#define LD64(off) __builtin_amdgcn_raw_buffer_load_b64(rws, (off), 0, AUX_SC1)
#define LD32(off) __builtin_amdgcn_raw_buffer_load_b32(rws, (off), 0, AUX_SC1)
#define ST128(v, off) __builtin_amdgcn_raw_buffer_store_b128((v), rws, (off), 0, AUX_SC1)
#define ST128S(v, off, so) __builtin_amdgcn_raw_buffer_store_b128((v), rws, (off), (so), AUX_SC1)
#define ST32(v, off) __builtin_amdgcn_raw_buffer_store_b32((v), rws, (off), 0, AUX_SC1)
#define RST(sum, off) ST128((v4u{(sum) + tag, tag, tag, tag}), (off))
#define RECOK(v, off) __all((off) >= OOB || (v) == tag)
#define WAITREC(off) do { unsigned w_ = 0; CBAR(); while (!RECOK(LD32((off) + 4), (off)) && ++w_ < SPIN_CAP) { __builtin_amdgcn_s_sleep(2); CBAR(); } CBAR(); } while (0)
#define LDS128(p, off) (*(LAS v4u*)((p) + (off)))
#define LDSF4(p, off) (*(LAS f32x4*)((p) + (off)))
DI void dma(unsigned ldsaddr, int voff, __amdgpu_buffer_rsrc_t rs, int soff) {
  asm volatile("s_mov_b32 m0, %0\n\ts_nop 0\n\tbuffer_load_dwordx4 %1, %2, %3 offen lds" :: "s"(ldsaddr), "v"(voff), "s"(rs), "s"(soff) : "memory");
}

DI void touch(unsigned ldsaddr, int voff, __amdgpu_buffer_rsrc_t rs) {
  asm volatile("s_mov_b32 m0, %0\n\ts_nop 0\n\tbuffer_load_dword %1, %2, 0 offen lds" :: "s"(ldsaddr), "v"(voff), "s"(rs) : "memory");
}
#define UNLIKELY(c) __builtin_expect(!!(c), 0)

__global__ __launch_bounds__(512, 2) void dit_kernel(const u16* __restrict__ x, const u16* __restrict__ c, const u16* __restrict__ wqkv,
                                                     const u16* __restrict__ wo, const u16* __restrict__ wmod, unsigned char* ws,
                                                     u16* __restrict__ out) {
  extern __shared__ __attribute__((aligned(16))) unsigned char lds_[];
  lp lds = (lp)lds_;
  const unsigned ldsb = (unsigned)(size_t)lds;
  const unsigned long long pc0 = __builtin_amdgcn_s_getpc();
  const int tid = threadIdx.x, lane = tid & 63, w = __builtin_amdgcn_readfirstlane(tid >> 6);
  const int n16 = lane & 15, g = lane >> 4;
  const int bid = blockIdx.x, xg = bid & 7, r = bid >> 3;
  const int b = xg >> 2, ql = r & 7, kb = r >> 3;
  const int qt = b * 32 + (xg & 3) * 8 + ql;
  const bool act1 = ql < 6;
  const int cb = (xg & 3) * 6 + (act1 ? ql : ql - 2);
  const __amdgpu_buffer_rsrc_t rws = __builtin_amdgcn_make_buffer_rsrc(ws, 0, WS_BYTES, 0x00020000);
  const __amdgpu_buffer_rsrc_t rx = __builtin_amdgcn_make_buffer_rsrc((void*)x, 0, 2048 * 768, 0x00020000);
  const int dch = 16 * ((lane & 3) ^ ((lane & 32) ? 3 : 0));
  const int rslot = (4 * n16 + (g ^ ((n16 & 8) ? 3 : 0))) * 16;
  const int l63 = lane < 8 ? lane * 16 : OOB;

  const int mu = (((r - 24) >> 1) * 8 + xg) * 2 + (r & 1);
  const int role = r < 12 ? 0 : ((r >= 24 && mu < 36) ? 2 : 1);
  const int dead0 = (r >= 24 && mu >= 36) ? OOB : 0;
  const int ru = r < 12 ? r : (r < 24 ? r - 12 : r - 24);
  const int u12 = 6 * (xg & 1) + ru % 6, u8 = 2 * (xg >> 1) + ru / 6;
  unsigned ep;
  asm volatile("s_nop 4\n\tbuffer_load_dword %0, off, %1, 0 sc1" : "=v"(ep) : "s"(rws) : "memory");
  if (w < 6) {
    unsigned cv = 0;
    if (role == 2) cv = ((const unsigned*)c)[tid];
    if (role < 2) {
      const __amdgpu_buffer_rsrc_t rq = __builtin_amdgcn_make_buffer_rsrc((void*)wqkv, 0, 384 * 2304, 0x00020000);
      const int v = (lane >> 2) * 2304 + dch;
      const int s3 = 48 * u8 * 2304 + (role ? 1536 : 768) + 128 * w;
#pragma unroll
      for (int t = 0; t < 3; ++t) {
        dma(ldsb + L_P0 + (t * 12 + 2 * w) * 1024, v, rq, s3 + 16 * t * 2304);
        dma(ldsb + L_P0 + (t * 12 + 2 * w + 1) * 1024, v, rq, s3 + 16 * t * 2304 + 64);
      }
      if (role == 0) {
        const int s2 = 32 * u12 * 2304 + 128 * w;
#pragma unroll
        for (int t = 0; t < 2; ++t) {
          dma(ldsb + L_P0 + ((3 + t) * 12 + 2 * w) * 1024, v, rq, s2 + 16 * t * 2304);
          dma(ldsb + L_P0 + ((3 + t) * 12 + 2 * w + 1) * 1024, v, rq, s2 + 16 * t * 2304 + 64);
        }
      }
    }
    if (role == 1 || role == 2) {
      const int ld = role == 1 ? 768 : 4608;
      const __amdgpu_buffer_rsrc_t rc = __builtin_amdgcn_make_buffer_rsrc((void*)(role == 1 ? wo : wmod), 0, 384 * ld, 0x00020000);
      const int v = (lane >> 2) * ld + dch;
#pragma unroll
      for (int j = 0; j < 4; ++j) dma(ldsb + L_P0 + (36 + 4 * w + j) * 1024, v, rc, (role == 1 ? u12 : mu) * 64 + (4 * w + j) * 16 * ld);
    }
    if (role == 2) {
      const float f0 = bflo(cv), f1 = bfhi(cv);
      const int bb = tid >= 192, i2 = 2 * (tid - 192 * bb);
      *(LAS unsigned*)(lds + L_P0 + (i2 >> 5) * 1024 + (bb * 4 + ((i2 >> 3) & 3)) * 16 + (i2 & 7) * 2) =
          cvtpk(f0 * __builtin_amdgcn_rcpf(1.f + __expf(-f0)), f1 * __builtin_amdgcn_rcpf(1.f + __expf(-f1)));
    }
    VMWAIT(0);
    __builtin_amdgcn_s_waitcnt(0x0F70);
  } else {
    __builtin_amdgcn_s_setprio(3);
    v4u xs[3];
    asm volatile("buffer_load_dwordx4 %0, %3, %4, 0 offen\n\tbuffer_load_dwordx4 %1, %3, %4, 0 offen offset:256\n\tbuffer_load_dwordx4 %2, %3, %4, 0 offen offset:512"
                 : "=&v"(xs[0]), "=&v"(xs[1]), "=&v"(xs[2]) : "v"((32 * (bid >> 2) + 16 * (bid & 1) + 8 * (w & 1) + 2 * (bid & 2) + (lane & 3)) * 768 + (lane >> 2) * 16), "s"(rx) : "memory");
    {
      const unsigned scr = ldsb + L_SCR;
      const int dead = lane < 36 ? 0 : OOB;
      const __amdgpu_buffer_rsrc_t rq = __builtin_amdgcn_make_buffer_rsrc((void*)wqkv, 0, 384 * 2304, 0x00020000);
      touch(scr, dead + (w == 6 ? (96 * (xg >> 1) + 3 * r + lane / 12) * 2304 + 768 + (lane % 12) * 128 : (192 * (xg & 1) + 6 * r + lane / 6) * 2304 + (lane % 6) * 128), rq);
      const __amdgpu_buffer_rsrc_t r2 = __builtin_amdgcn_make_buffer_rsrc(w == 6 ? (void*)wo : (void*)(pc0 & ~127ull), 0, w == 6 ? 384 * 768 : CODEPF, 0x00020000);
      touch(scr, w == 6 ? dead + (12 * r + lane / 3) * 768 + 384 * (xg & 1) + (lane % 3) * 128 : (lane < 5 ? (r * 5 + lane) * 128 : OOB), r2);
    }
    if (w == 7) {
      const __amdgpu_buffer_rsrc_t r3 = __builtin_amdgcn_make_buffer_rsrc((void*)wmod, 0, 384 * 4608, 0x00020000);
      touch(ldsb + L_SCR, (lane < (xg < 2 ? 36 : 24) ? 0 : OOB) + (12 * r + lane % 12) * 4608 + ((lane / 12) * 8 + xg) * 128, r3);
    }
    else touch(ldsb + L_SCR, OOB, rx);
    asm volatile("s_waitcnt vmcnt(3)" : "+v"(xs[0]), "+v"(xs[1]), "+v"(xs[2]) :: "memory");
    float f[24], sm = 0.f, q = 0.f;
#pragma unroll
    for (int i = 0; i < 12; ++i) { f[2 * i] = bflo(xs[i >> 2][i & 3]); f[2 * i + 1] = bfhi(xs[i >> 2][i & 3]); sm += f[2 * i] + f[2 * i + 1]; }
    sm = qsumf(sm) * (1.f / 384.f);
#pragma unroll
    for (int i = 0; i < 24; ++i) { f[i] -= sm; q = fmaf(f[i], f[i], q); }
    const float rs = __builtin_amdgcn_rsqf(qsumf(q) * (1.f / 384.f) + 1e-5f);
#pragma unroll
    for (int i = 0; i < 3; ++i)
      LDS128(lds, L_BN + ((lane >> 2) + 16 * i) * 128 + ((w & 1) * 4 + (lane & 3)) * 16) =
          v4u{cvtpk(f[8 * i] * rs, f[8 * i + 1] * rs), cvtpk(f[8 * i + 2] * rs, f[8 * i + 3] * rs), cvtpk(f[8 * i + 4] * rs, f[8 * i + 5] * rs), cvtpk(f[8 * i + 6] * rs, f[8 * i + 7] * rs)};
  }
  asm volatile("" : "+v"(ep));
  const unsigned tag = __builtin_amdgcn_readfirstlane(ep) + 1u;
  BAR();
  if (w >= 6) {
    v4u nv[3];
#pragma unroll
    for (int i = 0; i < 3; ++i) nv[i] = LDS128(lds, L_BN + (w & 1) * 3072 + i * 1024 + lane * 16);
    const int no = O_NF + (bid >> 1) * 12288 + ((w & 1) * 24 + (lane >> 3)) * 256 + (bid & 1) * 128 + (lane & 7) * 16;
#pragma unroll
    for (int i = 0; i < 3; ++i) ST128(nv[i], no + i * 2048);
    VMWAIT(0);
    RST(0u, l63 + O_RN + (bid * 2 + (w & 1)) * 128);
    __builtin_amdgcn_s_setprio(0);
  }
  const int rg = b * 32 + kb * 8 + w;
  const int nro = lane < 8 ? O_RN + (rg * 8 + lane) * 128 : OOB;
  if (w < (role == 2 ? 2 : 6)) {
    const int t3 = w >> 1, t2 = w & 1;
    f32x4 acc = {};
    v4u a[12], bq[12];
    if (role == 0) {
      lp ap = opq(lds + L_P0 + (3 + t2) * 12288 + rslot), bp = opq(lds + L_P0 + t3 * 12288 + rslot);
#pragma unroll
      for (int k = 0; k < 12; ++k) {
        a[k] = LDS128(ap, k * 1024); bq[k] = LDS128(bp, k * 1024);
        if (k >= 5) acc = mfma(a[k - 5], bq[k - 5], acc);
        SB();
      }
    } else {
      lp ap = opq(lds + L_P0 + (role == 2 ? ((n16 & 1) * 4 + g) * 16 : t3 * 12288 + rslot));
      lp tb = opq(lds + L_P0 + 36864 + (8 * g + (n16 >> 2)) * 64 + 16 * ((2 * t2 + ((n16 >> 1) & 1)) ^ ((g & 1) ? 3 : 0)) + 8 * (n16 & 1));
#pragma unroll
      for (int k = 0; k < 12; ++k) {
        a[k] = LDS128(ap, k * 1024);
        bq[k] = __builtin_bit_cast(v4u, __builtin_shufflevector(__builtin_amdgcn_ds_read_tr16_b64_v4i16((LAS s16x4*)(tb + k * 2048)),
                                                                __builtin_amdgcn_ds_read_tr16_b64_v4i16((LAS s16x4*)(tb + k * 2048 + 256)), 0, 1, 2, 3, 4, 5, 6, 7));
        if (k >= 5) acc = mfma(a[k - 5], bq[k - 5], acc);
        SB();
      }
    }
#pragma unroll
    for (int k = 7; k < 12; ++k) acc = mfma(a[k], bq[k], acc);
    if (role != 2) {
      const int blk = role * 576 + (role == 0 ? (3 * u8 + t3) * 24 + 2 * u12 + t2 : (2 * u12 + t2) * 24 + 3 * u8 + t3);
      const unsigned o0 = cvtpk(acc[0], acc[1]), o1 = cvtpk(acc[2], acc[3]);
      auto r0 = __builtin_amdgcn_permlane16_swap(o0, o0, false, false);
      auto r1 = __builtin_amdgcn_permlane16_swap(o1, o1, false, false);
      ST128((v4u{r0[0], r1[0], r0[1], r1[1]}), (((g & 1) ? OOB : 0) | dead0) + O_MT + blk * 512 + ((g >> 1) * 16 + n16) * 16);
      RST(wsum(sum2(0u, o0, o1)), (l63 | dead0) + O_RM + blk * 128);
    } else if (g < 2) {
      float v = bfround(g ? acc[1] : acc[0]);
      if (mu >= 12 && mu < 24) v += 1.f;
      ST32(tagged(v, tag & 0xffffu), O_MOD + (2 * mu + t2) * 128 + lane * 4);
    }
  }
  __builtin_amdgcn_s_barrier();
  const int l16 = opq(lane * 16);
  const int s_n = O_NF + rg * 24576;
  const int l16b = opq(lane * 16 + 12288);
  v4u xf[2][12];
#define XF(i) xf[(i) & 1][(i) >> 1]
#define XLOAD(i) XF(i) = LD128(((i) & 1) ? l16b : l16, s_n + ((i) >> 1) * 1024)
  lp fl = opq(lds + L_MS + lane * 16);
  v4u qf[3]; unsigned qr, vsum; int rvo;
  const int qro = opq(lane < 3 ? O_RQ + (qt * 24 + 3 * w + lane) * 128 : OOB);
  const int s_ql = sopq(O_QF + (qt * 24 + 3 * w) * 1024);
#define HOP2_LOADS() do { _Pragma("unroll") for (int i = 0; i < 3; ++i) qf[i] = LD128(l16 + i * 1024, s_ql); qr = LD32(qro); } while (0)
  {
    const int s0 = 3 * (w & 3);
    const int mc4 = lane < 24 ? 96 + 8 * s0 + lane : (lane < 48 ? 8 * s0 + lane - 24 : (w < 4 ? 96 : 192) + 4 * cb + (lane & 3));
    const int mo = opq(O_MOD + (mc4 >> 2) * 128 + b * 64 + (mc4 & 3) * 16);
    const int fb = (w >> 2) * 576 + cb * 24 + 2 * s0;
    const int ro = opq(lane < 6 ? O_RM + (fb + lane) * 128 : OOB);
    const int s_f = sopq(O_MT + fb * 512);
    const int blk1 = rg * 24 + cb, dead1 = act1 ? 0 : OOB;
    const int qso = opq(dead1 + O_QF + blk1 * 1024 + ((g & 1) * 32 + (g >> 1) * 16 + n16) * 16);
    const int vso = opq(dead1 + O_VF + blk1 * 1024 + lane * 16);
    const int rso = opq((dead1 | l63) + O_RQ + blk1 * 128);
    rvo = opq((dead1 | l63) + O_RV + (((b * 4 + kb) * 24 + cb) * 8 + w) * 128);
    lp cg = opq(lds + L_CS + g * 16), cn = opq(lds + L_CS + 256 + n16 * 4), csw = opq(lds + L_CS + w * 64 + n16 * 4);
    lp bn = lds + L_BN + w * 768, bg = opq(bn + g * 32), msw = opq(fl + 3 * w * 1024);
    SB();
    v4u mf[3], mv4; unsigned mr;
#define HOP1_LOADS() do { _Pragma("unroll") for (int i = 0; i < 3; ++i) mf[i] = LD128(l16 + i * 1024, s_f); mr = LD32(ro); } while (0)
    __builtin_amdgcn_s_sleep(DLY1);
    mv4 = LD128(mo, 0);
    HOP1_LOADS();
    __builtin_amdgcn_s_sleep(DLY0);
    const unsigned nf = LD32(nro + 4);
    SB();
    f32x4 sa[3], sb[3]; v4u tf[3]; float oc_;
#define MODOK() __all((tchk(0u, mv4, tag) & 0xffffu) == 0u)
    if (UNLIKELY(!MODOK())) {
      unsigned it = 0;
      do { __builtin_amdgcn_s_sleep(3); CBAR(); mv4 = LD128(mo, 0); } while (!MODOK() && ++it < SPIN_CAP);
    }
    if (lane >= 24 && lane < 48) *(LAS v2u*)(bn + 512 + (lane - 24) * 8) = v2u{pkhi(mv4[0], mv4[1]), pkhi(mv4[2], mv4[3])};
    else LDS128(bn, (lane < 24 ? lane : (lane & 3) + 24) * 16) = mv4 & 0xffff0000u;
    CBAR();
#pragma unroll
    for (int k = 0; k < 3; ++k) { sa[k] = LDSF4(bg, 128 * k); sb[k] = LDSF4(bg, 128 * k + 16); tf[k] = LDS128(bn, 512 + 64 * k + 16 * g); }
    oc_ = *(LAS float*)(bn + 384 + n16 * 4);
    if (UNLIKELY(!RECOK(nf, nro))) WAITREC(nro);
    CBAR();
#pragma unroll
    for (int i = 0; i < XE; ++i) XLOAD(i);
    SB();
#define FBAD() (wsum(sum4(sum4(sum4(-mr, mf[0]), mf[1]), mf[2])) + 6u * tag != 0u)
    if (UNLIKELY(FBAD())) {
      unsigned it = 0;
      do { __builtin_amdgcn_s_sleep(3); CBAR(); HOP1_LOADS(); } while (FBAD() && ++it < SPIN_CAP);
    }
    if (w < 4) oc_ *= 0.05103103630798288f * 1.4426950408889634f;
    {
      f32x4 au = {};
#pragma unroll
      for (int i = 0; i < 3; ++i) {
#pragma unroll
        for (int x_ = 0; x_ < XS0; ++x_) XLOAD(XE + XS0 * i + x_);
        au = mfma(tf[i], mf[i], au);
        const f32x4 a_ = sa[i] * oc_, b_ = sb[i] * oc_;
        const v4u o = {cvtpk(bflo(mf[i][0]) * a_[0], bfhi(mf[i][0]) * a_[1]), cvtpk(bflo(mf[i][1]) * a_[2], bfhi(mf[i][1]) * a_[3]),
                       cvtpk(bflo(mf[i][2]) * b_[0], bfhi(mf[i][2]) * b_[1]), cvtpk(bflo(mf[i][3]) * b_[2], bfhi(mf[i][3]) * b_[3])};
        LDS128(msw, i * 1024) = o;
        SB();
      }
      *(LAS float*)(csw) = au[0] * oc_;
    }
#pragma unroll
    for (int i = XE + 3 * XS0; i < NX1; ++i) XLOAD(i);
    __builtin_amdgcn_s_setprio(2);
    BAR();
    v4u a[24];
    f32x4 uu[4]; float zz[4], zs;
#define AOP(k) do { if ((k) + PD < ((k) < 12 ? 12 : 24)) a[(k) + PD] = LDS128(fl, ((k) + PD) * 1024); \
      if ((k) < 12) { aq[0] = mfma(a[k], xf[0][(k) % 12], aq[0]); aq[1] = mfma(a[k], xf[1][(k) % 12], aq[1]); } \
      else { av[0] = mfma(xf[0][(k) % 12], a[k], av[0]); av[1] = mfma(xf[1][(k) % 12], a[k], av[1]); } } while (0)
#pragma unroll
    for (int k = 0; k < PD; ++k) a[k] = LDS128(fl, k * 1024);
    SB();
    f32x4 aq[2] = {}, av[2] = {};
#pragma unroll
    for (int k = 0; k < 12; ++k) {
#pragma unroll
      for (int x_ = 0; x_ < XL; ++x_) if (NX1 + XL * k + x_ < 24) XLOAD(NX1 + XL * k + x_);
      if (k >= 2 && k < 6) uu[k - 2] = LDSF4(cg, (k - 2) * 64);
      AOP(k);
      if (k == 9) { uu[0] = uu[0] + uu[1]; uu[2] = uu[2] + uu[3]; }
      if (k == 10) uu[0] = uu[0] + uu[2];
      SB();
    }
    {
      const f32x4 v0 = aq[0] + uu[0], v1 = aq[1] + uu[0];
      auto r0 = __builtin_amdgcn_permlane16_swap(cvtpk(v0[0], v0[1]), cvtpk(v1[0], v1[1]), false, false);
      auto r1 = __builtin_amdgcn_permlane16_swap(cvtpk(v0[2], v0[3]), cvtpk(v1[2], v1[3]), false, false);
      const v4u ov = {r0[0], r1[0], r0[1], r1[1]};
      ST128(ov, qso);
      RST(wsum(sum4(0u, ov)), rso);
      __builtin_amdgcn_s_setprio(0);
#pragma unroll
      for (int k = 12; k < 12 + PD; ++k) a[k] = LDS128(fl, k * 1024);
      SB();
#pragma unroll
      for (int k = 12; k < 24; ++k) {
        if (k >= 14 && k < 18) zz[k - 14] = *(LAS float*)(cn + (k - 14) * 64);
        AOP(k);
        if (k == 21) zs = (zz[0] + zz[1]) + (zz[2] + zz[3]);
        SB();
      }
      const f32x4 w0 = av[0] + zs, w1 = av[1] + zs;
      const v4u o = {cvtpk(w0[0], w0[1]), cvtpk(w0[2], w0[3]), cvtpk(w1[0], w1[1]), cvtpk(w1[2], w1[3])};
      ST128(o, vso);
      vsum = wsum(sum4(0u, o));
    }
    SB();
  }
  if (w == 7) ST128((v4u{tag, tag, tag, tag}), l63 + O_CL + bid * 128);
  const int vro = opq(lane < 24 ? O_RV + (((b * 4 + kb) * 8 + w) * 24 + lane) * 128 : OOB);
  const int mso = opq((w == 0 && g < 2) ? O_ML + ((qt * 4 + kb) * 32 + lane) * 16 : OOB);
  const int s_vl = sopq(O_VF + ((b * 32 + 8 * kb) * 24 + 3 * w) * 1024);
  const bool own = (w >> 1) == kb;
  const int oso = opq((own ? OOB : 0) + O_OF + ((qt * 24 + 3 * w) * 4 + kb) * 1024 + ((g & 1) * 32 + (g >> 1) * 16 + n16) * 16);
  const int roo = opq(((own ? OOB : 0) | l63) + O_RO + ((qt * 24 + 3 * w) * 4 + kb) * 128);
  lp mxr = opq(lds + L_MX + n16 * 32), mxw = opq(lds + L_MX + n16 * 32 + w * 4);
  lp plw = opq(lds + lane * 16 + w * 2048);
  lp pl = opq(lds + lane * 16);
  const int ooff = opq(((qt * 32 + 8 * (n16 >> 2) + 4 * (lane >> 5) + (n16 & 3)) * 384 + (6 * kb + w) * 16 + 8 * (g & 1)) * 2);
  const v4u xr = __builtin_amdgcn_raw_buffer_load_b128(rx, w < 6 ? ooff : OOB, 0, 0);
  const int mlo = opq(O_ML + (qt * 128 + (lane >> 5) * 16 + n16) * 16);
  const int s_ol = sopq(O_OF + (qt * 24 + 6 * kb + w) * 4096), rlo = opq((lane < 4 && lane != kb) ? O_RO + ((qt * 24 + 6 * kb + w) * 4 + lane) * 128 : OOB);
  {
    const unsigned nt_ = ~tag;
    ST128((v4u{nt_, nt_, nt_, nt_}), lane < 24 ? O_RO + ((qt * 24 + 3 * w + (lane >> 3)) * 4 + kb) * 128 + (lane & 7) * 16 : ((w == 0 && lane >= 32) ? O_ML + ((qt * 4 + kb) * 32 + lane - 32) * 16 : OOB));
  }
  v4u vf[8][3];
#define VF(i) vf[(i) & 7][(i) >> 3]
#define VLOAD(i) VF(i) = LD128(l16 + ((i) >> 3) * 1024, s_vl + ((i) & 7) * 24576)
  {
    lp qw = opq(lds + L_QS + (lane >> 5) * 12288 + 3 * w * 512 + (lane & 31) * 16);
    SB();
    __builtin_amdgcn_s_barrier();
    __builtin_amdgcn_s_sleep(DLY2);
    HOP2_LOADS();
    SB();
#define QBAD() (wsum(sum4(sum4(sum4(-qr, qf[0]), qf[1]), qf[2])) + 3u * tag != 0u)
#pragma unroll
    for (int i = 0; i < 3; ++i) LDS128(qw, i * 512) = qf[i];
    if (UNLIKELY(QBAD())) {
      unsigned it = 0;
      do { __builtin_amdgcn_s_sleep(3); CBAR(); HOP2_LOADS(); } while (QBAD() && ++it < SPIN_CAP);
#pragma unroll
      for (int i = 0; i < 3; ++i) LDS128(qw, i * 512) = qf[i];
    }
  }
  VMWAIT(0);
  RST(vsum, rvo);
  BAR();
  float m[2]; v2u vr, hv0 = {0u, 0u};
  {
    v4u q[24];
#define QREAD(i) q[i] = LDS128(fl, 24576 + (((i) & 1) * 12 + ((i) >> 1)) * 1024)
#pragma unroll
    for (int i = 0; i < PD; ++i) QREAD(i);
    SB();
    f32x4 acc[2][2] = {};
#pragma unroll
    for (int i = 0; i < 24; ++i) {
      if (i + PD < 24) QREAD(i + PD);
      acc[i & 1][0] = mfma(xf[0][i >> 1], q[i], acc[i & 1][0]); acc[i & 1][1] = mfma(xf[1][i >> 1], q[i], acc[i & 1][1]);
      if ((i * NVS) / 24 != ((i + 1) * NVS) / 24) VLOAD((i * NVS) / 24);
      SB();
    }
    asm volatile("s_nop 7" : "+v"(acc[0][0]), "+v"(acc[0][1]), "+v"(acc[1][0]), "+v"(acc[1][1]));
#pragma unroll
    for (int u = 0; u < 2; ++u) {
      float mx = max3(max3(acc[u][0][0], acc[u][0][1], acc[u][0][2]), acc[u][0][3], acc[u][1][0]);
      mx = max_g(max2(max3(mx, acc[u][1][1], acc[u][1][2]), acc[u][1][3]));
      *(LAS float*)(mxw + u * 512) = mx;
    }
    BAR();
    f32x4 ma[2], mb[2];
#pragma unroll
    for (int u = 0; u < 2; ++u) { ma[u] = LDSF4(mxr, u * 512); mb[u] = LDSF4(mxr, u * 512 + 16); }
    vr = LD64(vro);
    SB();
#pragma unroll
    for (int u = 0; u < 2; ++u) {
      m[u] = max2(max3(max3(ma[u][0], ma[u][1], ma[u][2]), ma[u][3], mb[u][0]), max3(mb[u][1], mb[u][2], mb[u][3]));
      float p[8];
#pragma unroll
      for (int i = 0; i < 8; ++i) p[i] = __builtin_amdgcn_exp2f(acc[u][i >> 2][i & 3] - m[u]);
      LDS128(plw, u * 1024) = v4u{cvtpk(p[0], p[1]), cvtpk(p[2], p[3]), cvtpk(p[4], p[5]), cvtpk(p[6], p[7])};
      if (NVS + u < 16) VLOAD(NVS + u);
      SB();
    }
#pragma unroll
    for (int i = NVS + 2; i < 16; ++i) VLOAD(i);
  }
#pragma unroll
  for (int i = 0; i < NH2; ++i) hv0 = sum4(hv0, VF(i));
  hv0 = opq(hv0);
  if (UNLIKELY(!RECOK(vr[1], vro))) { WAITREC(vro); vr = LD64(vro); }
  CBAR();
  BAR();
  v4u ml[4];
#define ML_LOADS() do { _Pragma("unroll") for (int k = 0; k < 4; ++k) ml[k] = LD128(mlo, ((kb + k) & 3) * 512); } while (0)
  {
    v4u pf[16];
    f32x4 acc[3][2] = {};
    v4u ov[3];
#define OSTORE(et) do { const f32x4 v0 = acc[et][0], v1 = acc[et][1]; \
      auto r0 = __builtin_amdgcn_permlane16_swap(cvtpk(v0[0], v0[1]), cvtpk(v1[0], v1[1]), false, false); \
      auto r1 = __builtin_amdgcn_permlane16_swap(cvtpk(v0[2], v0[3]), cvtpk(v1[2], v1[3]), false, false); \
      ov[et] = v4u{r0[0], r1[0], r0[1], r1[1]}; ST128S(ov[et], oso, (et) * 4096); \
      if (own) LDS128(lds + 16384 + ((w & 1) * 3 + (et)) * 1024, ((g & 1) * 32 + (g >> 1) * 16 + n16) * 16) = ov[et]; } while (0)
#define ORECORD(et) RST(wsum(sum4(0u, ov[et])), roo + (et) * 512)
#pragma unroll
    for (int i = 0; i < 8; ++i) pf[i] = LDS128(pl, i * 1024);
    SB();
#pragma unroll
    for (int i = 0; i < 16; ++i) {
      if (i + 8 < 16) pf[i + 8] = LDS128(pl, (i + 8) * 1024);
      if ((i & 1) && 16 + (i >> 1) < 24) VLOAD(16 + (i >> 1));
      acc[0][i & 1] = mfma(vf[i >> 1][0], pf[i], acc[0][i & 1]);
      if (i >= NH2) hv0 = opq(sum4(hv0, VF(i)));
      SB();
    }
    if (w == 0) {
      const v4u ones = {0x3f803f80u, 0x3f803f80u, 0x3f803f80u, 0x3f803f80u};
      f32x4 ls[2] = {};
#pragma unroll
      for (int i = 0; i < 16; ++i) ls[i & 1] = mfma(ones, pf[i], ls[i & 1]);
      ST128((v4u{__float_as_uint(g ? m[1] : m[0]), tag, __float_as_uint(g ? ls[1][0] : ls[0][0]), tag}), mso);
    }
    if (UNLIKELY(wsum(hv0, lane < 16 ? vr[0] : 0u) + 16u * tag != 0u)) {
      CBAR();
#pragma unroll
      for (int i = 0; i < 16; ++i) VLOAD(i);
      acc[0][0] = acc[0][1] = f32x4{0.f, 0.f, 0.f, 0.f};
#pragma unroll
      for (int i = 0; i < 16; ++i) acc[0][i & 1] = mfma(vf[i >> 1][0], pf[i], acc[0][i & 1]);
    }
#pragma unroll
    for (int i = 0; i < 16; ++i) {
      acc[1][i & 1] = mfma(vf[i >> 1][1], pf[i], acc[1][i & 1]);
      if (i == 0) OSTORE(0);
      if (i == 6) ORECORD(0);
      if ((i & 3) == 3) SB();
    }
#pragma unroll
    for (int i = 0; i < 16; ++i) {
      acc[2][i & 1] = mfma(vf[i >> 1][2], pf[i], acc[2][i & 1]);
      if (i == 0) OSTORE(1);
      if (i == 6) ORECORD(1);
      if (i == MLP) ML_LOADS();
      if ((i & 3) == 3) SB();
    }
    OSTORE(2);
    ORECORD(2);
  }
  BAR();
  if (w < 6) {
    v4u pp[4]; unsigned orr;
#define MLOK() __all(((ml[0][1] ^ tag) | (ml[1][1] ^ tag) | (ml[2][1] ^ tag) | (ml[3][1] ^ tag) | (ml[0][3] ^ tag) | (ml[1][3] ^ tag) | (ml[2][3] ^ tag) | (ml[3][3] ^ tag)) == 0u)
#define HOP3_LOADS() do { _Pragma("unroll") for (int k = 1; k < 4; ++k) pp[k] = LD128(l16, s_ol + ((kb + k) & 3) * 1024); orr = LD32(rlo); } while (0)
#define OBAD() (wsum(sum4(sum4(sum4(-orr, pp[1]), pp[2]), pp[3])) + 3u * tag != 0u)
    pp[0] = LDS128(pl, 16384 + w * 1024);
    SB();
    __builtin_amdgcn_s_sleep(DLY3);
    if (UNLIKELY(!MLOK())) {
      unsigned it = 0;
      WAITREC(rlo);
      do { CBAR(); ML_LOADS(); } while (!MLOK() && ++it < SPIN_CAP);
    }
    HOP3_LOADS();
    SB();
    const float mm = max2(max2(__uint_as_float(ml[0][0]), __uint_as_float(ml[1][0])), max2(__uint_as_float(ml[2][0]), __uint_as_float(ml[3][0])));
    float wk[4], o[8];
#pragma unroll
    for (int k = 0; k < 4; ++k) wk[k] = __builtin_amdgcn_exp2f(__uint_as_float(ml[k][0]) - mm);
    const float inv = __builtin_amdgcn_rcpf((wk[0] * __uint_as_float(ml[0][2]) + wk[1] * __uint_as_float(ml[1][2])) + (wk[2] * __uint_as_float(ml[2][2]) + wk[3] * __uint_as_float(ml[3][2])));
#pragma unroll
    for (int k = 0; k < 4; ++k) wk[k] *= inv;
#pragma unroll
    for (int e = 0; e < 4; ++e) { o[2 * e] = opq(fmaf(wk[0], bflo(pp[0][e]), bflo(xr[e]))); o[2 * e + 1] = opq(fmaf(wk[0], bfhi(pp[0][e]), bfhi(xr[e]))); }
    SB();
    if (UNLIKELY(OBAD())) {
      unsigned it = 0;
      do { WAITREC(rlo); HOP3_LOADS(); } while (OBAD() && ++it < SPIN_CAP);
    }
    v4u ov;
#pragma unroll
    for (int e = 0; e < 4; ++e) {
#pragma unroll
      for (int k = 1; k < 4; ++k) { o[2 * e] = fmaf(wk[k], bflo(pp[k][e]), o[2 * e]); o[2 * e + 1] = fmaf(wk[k], bfhi(pp[k][e]), o[2 * e + 1]); }
      ov[e] = cvtpk(o[2 * e], o[2 * e + 1]);
    }
    *(v4u*)((unsigned char*)out + ooff) = ov;
  } else if (UNLIKELY(bid == 255 && w == 7)) {
#pragma unroll 1
    for (unsigned it = 0; it < SPIN_CAP; ++it) {
      CBAR();
      unsigned bad = 0;
#pragma unroll
      for (int k = 0; k < 4; ++k) bad |= LD32(O_CL + (k * 64 + lane) * 128) ^ tag;
      if (__all(bad == 0)) break;
    }
    if (lane == 0) __hip_atomic_store((gu32*)(ws + O_EPOCH), tag, __ATOMIC_RELAXED, __HIP_MEMORY_SCOPE_AGENT);
  }
}

extern "C" void kernel_launch(void* const* d_in, const int* in_sizes, int n_in, void* d_out, int out_size, void* d_ws, size_t ws_size,
                              hipStream_t stream) {
  (void)hipFuncSetAttribute((const void*)dit_kernel, hipFuncAttributeMaxDynamicSharedMemorySize, L_TOTAL);
  dit_kernel<<<256, 512, L_TOTAL, stream>>>((const u16*)d_in[0], (const u16*)d_in[1], (const u16*)d_in[2], (const u16*)d_in[3],
                                            (const u16*)d_in[4], (unsigned char*)d_ws, (u16*)d_out);
}
```

```cpp
#include <hip/hip_runtime.h>
#include <hip/hip_bf16.h>

typedef short bf16x8 __attribute__((ext_vector_type(8)));
typedef short s16x4 __attribute__((ext_vector_type(4)));
typedef float f32x4 __attribute__((ext_vector_type(4)));
typedef float f32x2 __attribute__((ext_vector_type(2)));
typedef unsigned v4u __attribute__((ext_vector_type(4)));
typedef unsigned v2u __attribute__((ext_vector_type(2)));
typedef __bf16 bf2 __attribute__((ext_vector_type(2)));
typedef unsigned short u16;
#define DI __device__ __forceinline__
#define LAS __attribute__((address_space(3)))
#define GAS __attribute__((address_space(1)))
typedef GAS unsigned gu32;
typedef LAS unsigned char* lp;
#define STDECL
#define STAMP(i)
#define STFLUSH()
#define RETRY(k)
#define DLY0 0
#define DLY1 0
#define DLY2 5
#define PRE 1
#define HP 4
#define MP 1
#define TOUCH 1
#define CODEPF 15360
#define XS 0
#define DLY3 4
#define PD 4
#define SLP 2
#define ROT 0
#define NX1 17
#define NEARLY 0
#ifndef NV0
#define NV0 0
#endif
#define AUXO 16
#define NH 0
#ifndef NV2
#define NV2 16
#endif
#define HL 8
#define MC 1
#define DLY4 6
#define DLY5 8
#define BA 2
#define GATE 2
#define RL 8
#define MLP 12
#define PREWR 1
#define VR 9
#define VC 15
#define NH2 12
#define XL 2
#define SPLITB 1
#define XS0 2
#define XE 14
#define E1 5
#define E2 5
#define H2E 2
#define VS0 0
#ifndef NVS
#define NVS 12
#endif

constexpr int O_EPOCH = 0;
constexpr int O_MOD = 4096;
constexpr int O_CL = 16384;
constexpr int O_MT = 131072;
constexpr int O_NF = O_MT + 589824;
constexpr int O_QF = O_NF + 1572864;
constexpr int O_VF = O_QF + 1572864;
constexpr int O_OF = O_VF + 1572864;
constexpr int O_ML = O_OF + 6291456;
constexpr int O_RM = O_ML + 131072;
constexpr int O_RN = O_RM + 1152 * 128;
constexpr int O_RQ = O_RN + 512 * 128;
constexpr int O_RV = O_RQ + 1536 * 128;
constexpr int O_RO = O_RV + 1536 * 128;
constexpr int WS_BYTES = O_RO + 6144 * 128;
constexpr int OOB = 0x40000000;

constexpr int L_P0 = 0;
constexpr int L_MS = 61440, L_QS = L_MS + 24576, L_CS = L_QS + 24576, L_MX = L_CS + 512, L_LS = L_MX + 1024, L_BN = L_LS + 1024;
constexpr int L_SCR = L_BN + 6144, L_TAG = L_SCR + 256;
constexpr int L_TOTAL = L_TAG + 32;

static_assert(NV2 == 16 && NV0 + NVS <= NV2 && NV2 <= 24, "");
static_assert(XE + 3 * XS0 <= NX1 + 3 && NX1 <= 21 && (XL >= 2 || NX1 >= 9), "");
constexpr int AUX_SC1 = 16;
constexpr unsigned SPIN_CAP = 1u << 16;

DI float bflo(unsigned v) { return __uint_as_float(v << 16); }
DI float bfhi(unsigned v) { return __uint_as_float(v & 0xffff0000u); }
DI unsigned cvtpk(float lo, float hi) {
  f32x2 x = {lo, hi};
  return __builtin_bit_cast(unsigned, __builtin_convertvector(x, bf2));
}
DI float bfround(float x) { return bflo(cvtpk(x, 0.f)); }
DI unsigned pkhi(unsigned lo, unsigned hi) { return __builtin_amdgcn_perm(hi, lo, 0x07060302u); }
DI unsigned tagged(float v, unsigned tag16) { return (cvtpk(0.f, v) & 0xffff0000u) | tag16; }
DI unsigned tchk(unsigned a, v4u v, unsigned tag) { return a | (v[0] ^ tag) | (v[1] ^ tag) | (v[2] ^ tag) | (v[3] ^ tag); }
typedef unsigned short u16x2 __attribute__((ext_vector_type(2)));
DI unsigned sum2(unsigned a, unsigned x, unsigned y) { return __builtin_amdgcn_udot2(__builtin_bit_cast(u16x2, x), __builtin_bit_cast(u16x2, y), a, false); }
DI v2u sum4(v2u a, v4u v) { return v2u{sum2(a[0], v[0], v[1]), sum2(a[1], v[2], v[3])}; }
DI v2u sum4(unsigned a, v4u v) { return sum4(v2u{a, 0u}, v); }
DI unsigned scan63(unsigned v) {
  v += __builtin_amdgcn_update_dpp(0u, v, 0x111, 0xf, 0xf, true);
  v += __builtin_amdgcn_update_dpp(0u, v, 0x112, 0xf, 0xf, true);
  v += __builtin_amdgcn_update_dpp(0u, v, 0x114, 0xf, 0xf, true);
  v += __builtin_amdgcn_update_dpp(0u, v, 0x118, 0xf, 0xf, true);
  v += __builtin_amdgcn_update_dpp(0u, v, 0x142, 0xa, 0xf, true);
  v += __builtin_amdgcn_update_dpp(0u, v, 0x143, 0xc, 0xf, true);
  return v;
}
DI float sum_g(float v);
DI float qsumf(float v) {
#define FSTEP(ctl) v += __uint_as_float(__builtin_amdgcn_update_dpp(0u, __float_as_uint(v), ctl, 0xf, 0xf, true))
  FSTEP(0x128); FSTEP(0x124);
#undef FSTEP
  return sum_g(v);
}
DI unsigned wsum(unsigned v) { return __builtin_amdgcn_readlane(scan63(v), 63); }
DI unsigned wsum(v2u v, unsigned minus = 0u) { return wsum(v[0] + v[1] - minus); }
DI float max3(float a, float b, float c) { float r; asm("v_max3_f32 %0, %1, %2, %3" : "=v"(r) : "v"(a), "v"(b), "v"(c)); return r; }
DI float max2(float a, float b) { float r; asm("v_max_f32 %0, %1, %2" : "=v"(r) : "v"(a), "v"(b)); return r; }
DI float dot2(unsigned a, unsigned b, float c) { return __builtin_amdgcn_fdot2_f32_bf16(__builtin_bit_cast(bf2, a), __builtin_bit_cast(bf2, b), c, false); }
DI f32x4 mfma(v4u a, v4u b, f32x4 c) {
  return __builtin_amdgcn_mfma_f32_16x16x32_bf16(__builtin_bit_cast(bf16x8, a), __builtin_bit_cast(bf16x8, b), c, 0, 0, 0);
}
DI float sum_g(float v) {
  auto r = __builtin_amdgcn_permlane16_swap(__float_as_uint(v), __float_as_uint(v), false, false);
  v = __uint_as_float(r[0]) + __uint_as_float(r[1]);
  auto q = __builtin_amdgcn_permlane32_swap(__float_as_uint(v), __float_as_uint(v), false, false);
  return __uint_as_float(q[0]) + __uint_as_float(q[1]);
}
DI float max_g(float v) {
  auto r = __builtin_amdgcn_permlane16_swap(__float_as_uint(v), __float_as_uint(v), false, false);
  v = max2(__uint_as_float(r[0]), __uint_as_float(r[1]));
  auto q = __builtin_amdgcn_permlane32_swap(__float_as_uint(v), __float_as_uint(v), false, false);
  return max2(__uint_as_float(q[0]), __uint_as_float(q[1]));
}
DI int sopq(int v) { v = __builtin_amdgcn_readfirstlane(v); asm volatile("" : "+s"(v)); return v; }
template <typename T> DI T opq(T v) { asm volatile("" : "+v"(v)); return v; }
#define BAR() do { asm volatile("s_waitcnt lgkmcnt(0)" ::: "memory"); __builtin_amdgcn_s_barrier(); asm volatile("" ::: "memory"); } while (0)
#define VMWAIT(n) asm volatile("s_waitcnt vmcnt(%0)" :: "i"(n) : "memory")
#define SB() __builtin_amdgcn_sched_barrier(0)
#define CBAR() asm volatile("" ::: "memory")
#define LD128(off, so) __builtin_amdgcn_raw_buffer_load_b128(rws, (off), (so), AUX_SC1)
#define LD64(off) __builtin_amdgcn_raw_buffer_load_b64(rws, (off), 0, AUX_SC1)
#define LD32(off) __builtin_amdgcn_raw_buffer_load_b32(rws, (off), 0, AUX_SC1)
#define ST128(v, off) __builtin_amdgcn_raw_buffer_store_b128((v), rws, (off), 0, AUX_SC1)
#define ST128S(v, off, so) __builtin_amdgcn_raw_buffer_store_b128((v), rws, (off), (so), AUX_SC1)
#define ST64(v, off) __builtin_amdgcn_raw_buffer_store_b64((v), rws, (off), 0, AUX_SC1)
#define ST32(v, off) __builtin_amdgcn_raw_buffer_store_b32((v), rws, (off), 0, AUX_SC1)
#define ST16(v, off) __builtin_amdgcn_raw_buffer_store_b16((v), rws, (off), 0, AUX_SC1)
#define RST(sum, off) ST128((v4u{(sum) + tag, tag, tag, tag}), (off))
#define RECOK(v, off) __all((off) >= OOB || (v) == tag)
#define WAITREC(off) do { unsigned w_ = 0; CBAR(); while (!RECOK(LD32((off) + 4), (off)) && ++w_ < SPIN_CAP) { __builtin_amdgcn_s_sleep(2); CBAR(); } CBAR(); } while (0)
#define LDS128(p, off) (*(LAS v4u*)((p) + (off)))
#define LDSF4(p, off) (*(LAS f32x4*)((p) + (off)))
DI void dma(unsigned ldsaddr, int voff, __amdgpu_buffer_rsrc_t rs, int soff) {
  asm volatile("s_mov_b32 m0, %0\n\ts_nop 0\n\tbuffer_load_dwordx4 %1, %2, %3 offen lds" :: "s"(ldsaddr), "v"(voff), "s"(rs), "s"(soff) : "memory");
}

DI void touch(unsigned ldsaddr, int voff, __amdgpu_buffer_rsrc_t rs) {
  asm volatile("s_mov_b32 m0, %0\n\ts_nop 0\n\tbuffer_load_dword %1, %2, 0 offen lds" :: "s"(ldsaddr), "v"(voff), "s"(rs) : "memory");
}
#define UNLIKELY(c) __builtin_expect(!!(c), 0)

__global__ __launch_bounds__(512, 2) void dit_kernel(const u16* __restrict__ x, const u16* __restrict__ c, const u16* __restrict__ wqkv,
                                                     const u16* __restrict__ wo, const u16* __restrict__ wmod, unsigned char* ws,
                                                     u16* __restrict__ out) {
  extern __shared__ __attribute__((aligned(16))) unsigned char lds_[];
  lp lds = (lp)lds_;
  const unsigned ldsb = (unsigned)(size_t)lds;
  STDECL;
  STAMP(0);
  const unsigned long long pc0 = __builtin_amdgcn_s_getpc();
  const int tid = threadIdx.x, lane = tid & 63, w = __builtin_amdgcn_readfirstlane(tid >> 6);
  const int n16 = lane & 15, g = lane >> 4;
  const int bid = blockIdx.x, xg = bid & 7, r = bid >> 3;
  const int b = xg >> 2, ql = r & 7, kb = r >> 3;
  const int qt = b * 32 + (xg & 3) * 8 + ql;
  const int qt3 = b * 32 + (((xg & 3) + XS) & 3) * 8 + ql;
  const bool act1 = ql < 6;
  const int cb = (xg & 3) * 6 + (act1 ? ql : ql - 2);
  const __amdgpu_buffer_rsrc_t rws = __builtin_amdgcn_make_buffer_rsrc(ws, 0, WS_BYTES, 0x00020000);
  const __amdgpu_buffer_rsrc_t rx = __builtin_amdgcn_make_buffer_rsrc((void*)x, 0, 2048 * 768, 0x00020000);
  const int dch = 16 * ((lane & 3) ^ ((lane & 32) ? 3 : 0));
  const int rslot = (4 * n16 + (g ^ ((n16 & 8) ? 3 : 0))) * 16;
  const int l63 = lane < RL ? lane * 16 : OOB;

  const int mu = (((r - 24) >> 1) * 8 + xg) * 2 + (r & 1);
  const int role = r < 12 ? 0 : ((r >= 24 && mu < 36) ? 2 : 1);
  const int dead0 = (r >= 24 && mu >= 36) ? OOB : 0;
  const int ru = r < 12 ? r : (r < 24 ? r - 12 : r - 24);
  const int u12 = 6 * (xg & 1) + ru % 6, u8 = 2 * (xg >> 1) + ru / 6;
  unsigned ep;
  asm volatile("s_nop 4\n\tbuffer_load_dword %0, off, %1, 0 sc1" : "=v"(ep) : "s"(rws) : "memory");
  if (w < 6) {
    unsigned cv = 0;
    if (role == 2) cv = ((const unsigned*)c)[tid];
    if (role < 2) {
      const __amdgpu_buffer_rsrc_t rq = __builtin_amdgcn_make_buffer_rsrc((void*)wqkv, 0, 384 * 2304, 0x00020000);
      const int v = (lane >> 2) * 2304 + dch;
      const int s3 = 48 * u8 * 2304 + (role ? 1536 : 768) + 128 * w;
#pragma unroll
      for (int t = 0; t < 3; ++t) {
        dma(ldsb + L_P0 + (t * 12 + 2 * w) * 1024, v, rq, s3 + 16 * t * 2304);
        dma(ldsb + L_P0 + (t * 12 + 2 * w + 1) * 1024, v, rq, s3 + 16 * t * 2304 + 64);
      }
      if (role == 0) {
        const int s2 = 32 * u12 * 2304 + 128 * w;
#pragma unroll
        for (int t = 0; t < 2; ++t) {
          dma(ldsb + L_P0 + ((3 + t) * 12 + 2 * w) * 1024, v, rq, s2 + 16 * t * 2304);
          dma(ldsb + L_P0 + ((3 + t) * 12 + 2 * w + 1) * 1024, v, rq, s2 + 16 * t * 2304 + 64);
        }
      }
    }
    if (role == 1 || role == 2) {
      const int ld = role == 1 ? 768 : 4608;
      const __amdgpu_buffer_rsrc_t rc = __builtin_amdgcn_make_buffer_rsrc((void*)(role == 1 ? wo : wmod), 0, 384 * ld, 0x00020000);
      const int v = (lane >> 2) * ld + dch;
#pragma unroll
      for (int j = 0; j < 4; ++j) dma(ldsb + L_P0 + (36 + 4 * w + j) * 1024, v, rc, (role == 1 ? u12 : mu) * 64 + (4 * w + j) * 16 * ld);
    }
    if (role == 2) {
      const float f0 = bflo(cv), f1 = bfhi(cv);
      const int bb = tid >= 192, i2 = 2 * (tid - 192 * bb);
      *(LAS unsigned*)(lds + L_P0 + (i2 >> 5) * 1024 + (bb * 4 + ((i2 >> 3) & 3)) * 16 + (i2 & 7) * 2) =
          cvtpk(f0 * __builtin_amdgcn_rcpf(1.f + __expf(-f0)), f1 * __builtin_amdgcn_rcpf(1.f + __expf(-f1)));
    }
    VMWAIT(0);
    __builtin_amdgcn_s_waitcnt(0x0F70);
  } else {
    __builtin_amdgcn_s_setprio(3);
    v4u xs[3];
    asm volatile("buffer_load_dwordx4 %0, %3, %4, 0 offen\n\tbuffer_load_dwordx4 %1, %3, %4, 0 offen offset:256\n\tbuffer_load_dwordx4 %2, %3, %4, 0 offen offset:512"
                 : "=&v"(xs[0]), "=&v"(xs[1]), "=&v"(xs[2]) : "v"((32 * (bid >> 2) + 16 * (bid & 1) + 8 * (w & 1) + 2 * (bid & 2) + (lane & 3)) * 768 + (lane >> 2) * 16), "s"(rx) : "memory");
    {
      const unsigned scr = ldsb + L_SCR;
      const int dead = ((TOUCH & 1) && lane < 36) ? 0 : OOB;
      const __amdgpu_buffer_rsrc_t rq = __builtin_amdgcn_make_buffer_rsrc((void*)wqkv, 0, 384 * 2304, 0x00020000);
      touch(scr, dead + (w == 6 ? (96 * (xg >> 1) + 3 * r + lane / 12) * 2304 + 768 + (lane % 12) * 128 : (192 * (xg & 1) + 6 * r + lane / 6) * 2304 + (lane % 6) * 128), rq);
      const __amdgpu_buffer_rsrc_t r2 = __builtin_amdgcn_make_buffer_rsrc(w == 6 ? (void*)wo : (void*)(pc0 & ~127ull), 0, w == 6 ? 384 * 768 : CODEPF, 0x00020000);
      touch(scr, w == 6 ? dead + (12 * r + lane / 3) * 768 + 384 * (xg & 1) + (lane % 3) * 128 : (lane < 5 ? (r * 5 + lane) * 128 : OOB), r2);
    }
    if (w == 7) {
      const __amdgpu_buffer_rsrc_t r3 = __builtin_amdgcn_make_buffer_rsrc((void*)wmod, 0, 384 * 4608, 0x00020000);
      touch(ldsb + L_SCR, (lane < (xg < 2 ? 36 : 24) ? 0 : OOB) + (12 * r + lane % 12) * 4608 + ((lane / 12) * 8 + xg) * 128, r3);
    }
    else touch(ldsb + L_SCR, OOB, rx);
    asm volatile("s_waitcnt vmcnt(3)" : "+v"(xs[0]), "+v"(xs[1]), "+v"(xs[2]) :: "memory");
    if (PREWR & 2) ST128((v4u{0u, 0u, 0u, 0u}), l63 + O_RN + (bid * 2 + (w & 1)) * 128);
    float f[24], sm = 0.f, q = 0.f;
#pragma unroll
    for (int i = 0; i < 12; ++i) { f[2 * i] = bflo(xs[i >> 2][i & 3]); f[2 * i + 1] = bfhi(xs[i >> 2][i & 3]); sm += f[2 * i] + f[2 * i + 1]; }
    sm = qsumf(sm) * (1.f / 384.f);
#pragma unroll
    for (int i = 0; i < 24; ++i) { f[i] -= sm; q = fmaf(f[i], f[i], q); }
    const float rs = __builtin_amdgcn_rsqf(qsumf(q) * (1.f / 384.f) + 1e-5f);
#pragma unroll
    for (int i = 0; i < 3; ++i)
      LDS128(lds, L_BN + ((lane >> 2) + 16 * i) * 128 + ((w & 1) * 4 + (lane & 3)) * 16) =
          v4u{cvtpk(f[8 * i] * rs, f[8 * i + 1] * rs), cvtpk(f[8 * i + 2] * rs, f[8 * i + 3] * rs), cvtpk(f[8 * i + 4] * rs, f[8 * i + 5] * rs), cvtpk(f[8 * i + 6] * rs, f[8 * i + 7] * rs)};
  }
  asm volatile("" : "+v"(ep));
  const unsigned tag = __builtin_amdgcn_readfirstlane(ep) + 1u;
  STAMP(2);
  BAR();
  STAMP(3);
  if (w >= 6) {
    v4u nv[3];
#pragma unroll
    for (int i = 0; i < 3; ++i) nv[i] = LDS128(lds, L_BN + (w & 1) * 3072 + i * 1024 + lane * 16);
    const int no = O_NF + (bid >> 1) * 12288 + ((w & 1) * 24 + (lane >> 3)) * 256 + (bid & 1) * 128 + (lane & 7) * 16;
#pragma unroll
    for (int i = 0; i < 3; ++i) ST128(nv[i], no + i * 2048);
    const unsigned ns = wsum(sum4(sum4(sum4(0u, nv[0]), nv[1]), nv[2]));
    VMWAIT(0);
    RST(ns, l63 + O_RN + (bid * 2 + (w & 1)) * 128);
    __builtin_amdgcn_s_setprio(0);
  }
  const int rg = b * 32 + kb * 8 + w;
  const int nro = lane < 8 ? O_RN + (rg * 8 + lane) * 128 : OOB;
  if (w < (role == 2 ? 2 : 6)) {
    const int t3 = w >> 1, t2 = w & 1;
    f32x4 acc = {};
    v4u a[12], bq[12];
    if (role == 0) {
      lp ap = opq(lds + L_P0 + (3 + t2) * 12288 + rslot), bp = opq(lds + L_P0 + t3 * 12288 + rslot);
#pragma unroll
      for (int k = 0; k < 12; ++k) {
        a[k] = LDS128(ap, k * 1024); bq[k] = LDS128(bp, k * 1024);
        if (k >= 5) acc = mfma(a[k - 5], bq[k - 5], acc);
        SB();
      }
    } else {
      lp ap = opq(lds + L_P0 + (role == 2 ? ((n16 & 1) * 4 + g) * 16 : t3 * 12288 + rslot));
      lp tb = opq(lds + L_P0 + 36864 + (8 * g + (n16 >> 2)) * 64 + 16 * ((2 * t2 + ((n16 >> 1) & 1)) ^ ((g & 1) ? 3 : 0)) + 8 * (n16 & 1));
#pragma unroll
      for (int k = 0; k < 12; ++k) {
        a[k] = LDS128(ap, k * 1024);
        bq[k] = __builtin_bit_cast(v4u, __builtin_shufflevector(__builtin_amdgcn_ds_read_tr16_b64_v4i16((LAS s16x4*)(tb + k * 2048)),
                                                                __builtin_amdgcn_ds_read_tr16_b64_v4i16((LAS s16x4*)(tb + k * 2048 + 256)), 0, 1, 2, 3, 4, 5, 6, 7));
        if (k >= 5) acc = mfma(a[k - 5], bq[k - 5], acc);
        SB();
      }
    }
#pragma unroll
    for (int k = 7; k < 12; ++k) acc = mfma(a[k], bq[k], acc);
    if (role != 2) {
      const int blk = role * 576 + (role == 0 ? (3 * u8 + t3) * 24 + 2 * u12 + t2 : (2 * u12 + t2) * 24 + 3 * u8 + t3);
      const unsigned o0 = cvtpk(acc[0], acc[1]), o1 = cvtpk(acc[2], acc[3]);
      auto r0 = __builtin_amdgcn_permlane16_swap(o0, o0, false, false);
      auto r1 = __builtin_amdgcn_permlane16_swap(o1, o1, false, false);
      ST128((v4u{r0[0], r1[0], r0[1], r1[1]}), (((g & 1) ? OOB : 0) | dead0) + O_MT + blk * 512 + ((g >> 1) * 16 + n16) * 16);
      RST(wsum(sum2(0u, o0, o1)), (l63 | dead0) + O_RM + blk * 128);
    } else if (g < 2) {
      float v = bfround(g ? acc[1] : acc[0]);
      if (mu >= 12 && mu < 24) v += 1.f;
      ST32(tagged(v, tag & 0xffffu), O_MOD + (2 * mu + t2) * 128 + lane * 4);
    }
  }
  STAMP(4);
  __builtin_amdgcn_s_barrier();
  const int l16 = opq(lane * 16);
  const int s_n = O_NF + rg * 24576;
  const int rot = ROT ? (ql * 3) >> 1 : 0;
  const int l16b = opq(lane * 16 + 12288);
  v4u xf[2][12];
#define XF(i) xf[(i) & 1][(i) >> 1]
#define XLOAD(i) XF(i) = LD128(((i) & 1) ? l16b : l16, s_n + (((i) >> 1) + rot - (((i) >> 1) + rot >= 12 ? 12 : 0)) * 1024)
  if (DLY4) __builtin_amdgcn_s_sleep(DLY4);
#pragma unroll
  for (int i = 0; i < E1; ++i) XLOAD(i);
  SB();
  lp fl = opq(lds + L_MS + lane * 16);
  v4u qf[3]; unsigned qr, vsum; int rvo;
  const int qro = opq(lane < 3 ? O_RQ + (qt * 24 + 3 * w + lane) * 128 : OOB);
  const int s_ql = sopq(O_QF + (qt * 24 + 3 * w) * 1024);
#define HOP2_LOADS() do { _Pragma("unroll") for (int i = 0; i < 3; ++i) qf[i] = LD128(l16 + i * 1024, s_ql); qr = LD32(qro); } while (0)
  {
    const int s0 = 3 * (w & 3);
    const int mc4 = lane < 24 ? 96 + 8 * s0 + lane : (lane < 48 ? 8 * s0 + lane - 24 : (w < 4 ? 96 : 192) + 4 * cb + (lane & 3));
    const int mo = opq(O_MOD + (mc4 >> 2) * 128 + b * 64 + (mc4 & 3) * 16);
    const int fb = (w >> 2) * 576 + cb * 24 + 2 * s0;
    const int ro = opq(lane < 6 ? O_RM + (fb + lane) * 128 : OOB);
    const int s_f = sopq(O_MT + fb * 512);
    const int blk1 = rg * 24 + cb, dead1 = act1 ? 0 : OOB;
    const int qso = opq(dead1 + O_QF + blk1 * 1024 + ((g & 1) * 32 + (g >> 1) * 16 + n16) * 16);
    const int vso = opq(dead1 + O_VF + blk1 * 1024 + lane * 16);
    const int rso = opq((dead1 | l63) + O_RQ + blk1 * 128);
    rvo = opq((dead1 | l63) + O_RV + (((b * 4 + kb) * 24 + cb) * 8 + w) * 128);
    lp cg = opq(lds + L_CS + g * 16), cn = opq(lds + L_CS + 256 + n16 * 4), csw = opq(lds + L_CS + w * 64 + n16 * 4);
    lp bn = lds + L_BN + w * 768, bg = opq(bn + g * 32), msw[3];
#pragma unroll
    for (int i = 0; i < 3; ++i) { int sl = s0 + i - rot; if (sl < 0) sl += 12; msw[i] = opq(fl + (w >> 2) * 12288 + sl * 1024); }
    SB();
    v4u mf[3], mv4; unsigned mr;
#define HOP1_LOADS() do { _Pragma("unroll") for (int i = 0; i < 3; ++i) mf[i] = LD128(l16 + i * 1024, s_f); mr = LD32(ro); } while (0)
#pragma unroll
    for (int i = E1; i < E2; ++i) XLOAD(i);
    if (DLY1) __builtin_amdgcn_s_sleep(DLY1);
    mv4 = LD128(mo, 0);
    HOP1_LOADS();
    if (DLY0) __builtin_amdgcn_s_sleep(DLY0);
    v2u nf = LD64(nro);
    SB();
    __builtin_amdgcn_s_barrier();
#pragma unroll
    for (int i = E2; i < 12; ++i) XLOAD(i);
    SB();
    v2u hx = {0u, 0u};
#pragma unroll
    for (int i = 0; i < E2; ++i) hx = sum4(hx, XF(i));
    hx = opq(hx);
    f32x4 sa[3], sb[3]; v4u tf[3]; float oc_;
#define MODOK() __all((tchk(0u, mv4, tag) & 0xffffu) == 0u)
    if (UNLIKELY(!MODOK())) {
      unsigned it = 0;
      do { RETRY(1); __builtin_amdgcn_s_sleep(3); CBAR(); mv4 = LD128(mo, 0); } while (!MODOK() && ++it < SPIN_CAP);
    }
    if (lane >= 24 && lane < 48) *(LAS v2u*)(bn + 512 + (lane - 24) * 8) = v2u{pkhi(mv4[0], mv4[1]), pkhi(mv4[2], mv4[3])};
    else LDS128(bn, (lane < 24 ? lane : (lane & 3) + 24) * 16) = mv4 & 0xffff0000u;
    CBAR();
#pragma unroll
    for (int k = 0; k < 3; ++k) { sa[k] = LDSF4(bg, 128 * k); sb[k] = LDSF4(bg, 128 * k + 16); tf[k] = LDS128(bn, 512 + 64 * k + 16 * g); }
    oc_ = *(LAS float*)(bn + 384 + n16 * 4);
    if (UNLIKELY(!RECOK(nf[1], nro))) { RETRY(0); WAITREC(nro); nf = LD64(nro); }
    CBAR();
    STAMP(5);
#pragma unroll
    for (int i = 12; i < XE; ++i) XLOAD(i);
    SB();
#define FBAD() (wsum(sum4(sum4(sum4(-mr, mf[0]), mf[1]), mf[2])) + 6u * tag != 0u)
    if (UNLIKELY(FBAD())) {
      unsigned it = 0;
      do { RETRY(9); __builtin_amdgcn_s_sleep(3); CBAR(); HOP1_LOADS(); } while (FBAD() && ++it < SPIN_CAP);
    }
    if (w < 4) oc_ *= 0.05103103630798288f * 1.4426950408889634f;
    {
      f32x4 au = {};
#pragma unroll
      for (int i = 0; i < 3; ++i) {
#pragma unroll
        for (int x_ = 0; x_ < XS0; ++x_) XLOAD(XE + XS0 * i + x_);
        au = mfma(tf[i], mf[i], au);
        const f32x4 a_ = sa[i] * oc_, b_ = sb[i] * oc_;
        const v4u o = {cvtpk(bflo(mf[i][0]) * a_[0], bfhi(mf[i][0]) * a_[1]), cvtpk(bflo(mf[i][1]) * a_[2], bfhi(mf[i][1]) * a_[3]),
                       cvtpk(bflo(mf[i][2]) * b_[0], bfhi(mf[i][2]) * b_[1]), cvtpk(bflo(mf[i][3]) * b_[2], bfhi(mf[i][3]) * b_[3])};
        LDS128(msw[i], 0) = o;
        SB();
      }
      *(LAS float*)(csw) = au[0] * oc_;
    }
#pragma unroll
    for (int i = XE + 3 * XS0; i < NX1 + 3; ++i) XLOAD(i);
#pragma unroll
    for (int i = E2; i < 12; ++i) hx = sum4(hx, XF(i));
    if (UNLIKELY(wsum(hx, (lane & 1) ? 0u : nf[0]) + 4u * tag != 0u)) {
      RETRY(4); CBAR();
#pragma unroll
      for (int i = 0; i < 12; ++i) XLOAD(i);
    }
    STAMP(9);
    __builtin_amdgcn_s_setprio(2);
    BAR();
    v4u a[24];
    f32x4 uu[4]; float zz[4], zs;
#define AOP(k) do { if ((k) + PD < ((k) < 12 ? 12 : 24)) a[(k) + PD] = LDS128(fl, ((k) + PD) * 1024); \
      if ((k) < 12) { aq[0] = mfma(a[k], xf[0][(k) % 12], aq[0]); aq[1] = mfma(a[k], xf[1][(k) % 12], aq[1]); } \
      else { av[0] = mfma(xf[0][(k) % 12], a[k], av[0]); av[1] = mfma(xf[1][(k) % 12], a[k], av[1]); } } while (0)
#pragma unroll
    for (int k = 0; k < PD; ++k) a[k] = LDS128(fl, k * 1024);
    SB();
    f32x4 aq[2] = {}, av[2] = {};
#pragma unroll
    for (int k = 0; k < 12; ++k) {
#pragma unroll
      for (int x_ = 0; x_ < XL; ++x_) if (NX1 + 3 + XL * k + x_ < 24) XLOAD(NX1 + 3 + XL * k + x_);
      if (k >= 2 && k < 6) uu[k - 2] = LDSF4(cg, (k - 2) * 64);
      AOP(k);
      if (k == 9) { uu[0] = uu[0] + uu[1]; uu[2] = uu[2] + uu[3]; }
      if (k == 10) uu[0] = uu[0] + uu[2];
      SB();
    }
    {
      const f32x4 v0 = aq[0] + uu[0], v1 = aq[1] + uu[0];
      auto r0 = __builtin_amdgcn_permlane16_swap(cvtpk(v0[0], v0[1]), cvtpk(v1[0], v1[1]), false, false);
      auto r1 = __builtin_amdgcn_permlane16_swap(cvtpk(v0[2], v0[3]), cvtpk(v1[2], v1[3]), false, false);
      const v4u ov = {r0[0], r1[0], r0[1], r1[1]};
      ST128(ov, qso);
      RST(wsum(sum4(0u, ov)), rso);
      __builtin_amdgcn_s_setprio(0);
      STAMP(12);
#pragma unroll
      for (int k = 12; k < 12 + PD; ++k) a[k] = LDS128(fl, k * 1024);
      SB();
#pragma unroll
      for (int k = 12; k < 24; ++k) {
        if (k >= 14 && k < 18) zz[k - 14] = *(LAS float*)(cn + (k - 14) * 64);
        AOP(k);
        if (k == 21) zs = (zz[0] + zz[1]) + (zz[2] + zz[3]);
        SB();
      }
      const f32x4 w0 = av[0] + zs, w1 = av[1] + zs;
      const v4u o = {cvtpk(w0[0], w0[1]), cvtpk(w0[2], w0[3]), cvtpk(w1[0], w1[1]), cvtpk(w1[2], w1[3])};
      if (H2E == 1) { HOP2_LOADS(); SB(); }
      ST128(o, vso);
      vsum = wsum(sum4(0u, o));
      if (H2E == 2) { SB(); if (DLY2) __builtin_amdgcn_s_sleep(DLY2); HOP2_LOADS(); }
    }
    STAMP(10);
    SB();
  }
  STAMP(11);
  if (w == 7) ST128((v4u{tag, tag, tag, tag}), l63 + O_CL + bid * 128);
  const int vro = opq(lane < 24 ? O_RV + (((b * 4 + kb) * 8 + w) * 24 + lane) * 128 : OOB);
  const int mso = opq((w == 0 && g < 2) ? O_ML + ((qt * 4 + kb) * 32 + lane) * 16 : OOB);
  const int s_vl = sopq(O_VF + ((b * 32 + 8 * kb) * 24 + 3 * w) * 1024);
  const bool own = (w >> 1) == kb;
  const int oso = opq((own ? OOB : 0) + O_OF + ((qt * 24 + 3 * w) * 4 + kb) * 1024 + ((g & 1) * 32 + (g >> 1) * 16 + n16) * 16);
  const int roo = opq(((own ? OOB : 0) | l63) + O_RO + ((qt * 24 + 3 * w) * 4 + kb) * 128);
  lp mxr = opq(lds + L_MX + n16 * 32), mxw = opq(lds + L_MX + n16 * 32 + w * 4);
  lp plw = opq(lds + lane * 16 + w * 2048);
  lp pl = opq(lds + lane * 16);
  const int ooff = opq(((qt * 32 + 8 * (n16 >> 2) + 4 * (lane >> 5) + (n16 & 3)) * 384 + (6 * kb + w) * 16 + 8 * (g & 1)) * 2);
  const v4u xr = __builtin_amdgcn_raw_buffer_load_b128(rx, w < 6 ? ooff : OOB, 0, 0);
  const int mlo = opq(O_ML + (qt * 128 + (lane >> 5) * 16 + n16) * 16);
  const int s_ol = sopq(O_OF + (qt * 24 + 6 * kb + w) * 4096), rlo = opq((lane < 4 && lane != kb) ? O_RO + ((qt * 24 + 6 * kb + w) * 4 + lane) * 128 : OOB);
  if (PREWR & 1) {
    const unsigned nt_ = ~tag;
    ST128((v4u{nt_, nt_, nt_, nt_}), lane < 24 ? O_RO + ((qt * 24 + 3 * w + (lane >> 3)) * 4 + kb) * 128 + (lane & 7) * 16 : ((w == 0 && lane >= 32) ? O_ML + ((qt * 4 + kb) * 32 + lane - 32) * 16 : OOB));
  }
  v4u vf[8][3];
#define VF(i) vf[(i) & 7][(i) >> 3]
#define VLOAD(i) VF(i) = LD128(l16 + ((i) >> 3) * 1024, s_vl + ((i) & 7) * 24576)
  {
    lp qw = opq(lds + L_QS + (lane >> 5) * 12288 + 3 * w * 512 + (lane & 31) * 16);
    SB();
    if (!H2E) { __builtin_amdgcn_s_barrier(); if (DLY2) __builtin_amdgcn_s_sleep(DLY2); HOP2_LOADS(); }
#pragma unroll
    for (int i = 0; i < NV0; ++i) VLOAD(i);
    SB();
#define QBAD() (wsum(sum4(sum4(sum4(-qr, qf[0]), qf[1]), qf[2])) + 3u * tag != 0u)
#pragma unroll
    for (int i = 0; i < 3; ++i) LDS128(qw, i * 512) = qf[i];
    if (UNLIKELY(QBAD())) {
      unsigned it = 0;
      do { RETRY(2); __builtin_amdgcn_s_sleep(3); CBAR(); HOP2_LOADS(); } while (QBAD() && ++it < SPIN_CAP);
#pragma unroll
      for (int i = 0; i < 3; ++i) LDS128(qw, i * 512) = qf[i];
    }
  }
  VMWAIT(3);
  RST(vsum, rvo);
  STAMP(13);
  BAR();
  float m[2]; v2u vr, hv0 = {0u, 0u};
  {
    v4u q[24];
#define QREAD(i) q[i] = LDS128(fl, 24576 + (((i) & 1) * 12 + ((i) >> 1)) * 1024)
#pragma unroll
    for (int i = 0; i < PD; ++i) QREAD(i);
    SB();
    f32x4 acc[2][2] = {};
#pragma unroll
    for (int i = 0; i < 24; ++i) {
      if (i + PD < 24) QREAD(i + PD);
      acc[i & 1][0] = mfma(xf[0][i >> 1], q[i], acc[i & 1][0]); acc[i & 1][1] = mfma(xf[1][i >> 1], q[i], acc[i & 1][1]);
      if (i >= VS0 && ((i - VS0) * NVS) / (24 - VS0) != ((i - VS0 + 1) * NVS) / (24 - VS0)) VLOAD(((i - VS0) * NVS) / (24 - VS0));
      if (i >= 24 - NH) hv0 = opq(sum4(hv0, VF(i - (24 - NH))));
      SB();
    }
#pragma unroll
    for (int u = 0; u < 2; ++u) {
      float mx = max3(max3(acc[u][0][0], acc[u][0][1], acc[u][0][2]), acc[u][0][3], acc[u][1][0]);
      mx = max_g(max2(max3(mx, acc[u][1][1], acc[u][1][2]), acc[u][1][3]));
      *(LAS float*)(mxw + u * 512) = mx;
    }
    STAMP(14);
    BAR();
    f32x4 ma[2], mb[2];
#pragma unroll
    for (int u = 0; u < 2; ++u) { ma[u] = LDSF4(mxr, u * 512); mb[u] = LDSF4(mxr, u * 512 + 16); }
    vr = LD64(vro);
    SB();
#pragma unroll
    for (int u = 0; u < 2; ++u) {
      m[u] = max2(max3(max3(ma[u][0], ma[u][1], ma[u][2]), ma[u][3], mb[u][0]), max3(mb[u][1], mb[u][2], mb[u][3]));
      float p[8];
#pragma unroll
      for (int i = 0; i < 8; ++i) p[i] = __builtin_amdgcn_exp2f(acc[u][i >> 2][i & 3] - m[u]);
      LDS128(plw, u * 1024) = v4u{cvtpk(p[0], p[1]), cvtpk(p[2], p[3]), cvtpk(p[4], p[5]), cvtpk(p[6], p[7])};
      if (NV0 + NVS + u < NV2) VLOAD(NV0 + NVS + u);
      SB();
    }
#pragma unroll
    for (int i = NV0 + NVS + 2; i < NV2; ++i) VLOAD(i);
  }
#pragma unroll
  for (int i = NH; i < NH2; ++i) hv0 = sum4(hv0, VF(i));
  hv0 = opq(hv0);
  if (UNLIKELY(!RECOK(vr[1], vro))) { RETRY(7); WAITREC(vro); vr = LD64(vro); }
  STAMP(15);
  BAR();
  v4u ml[4];
#define ML_LOADS() do { _Pragma("unroll") for (int k = 0; k < 4; ++k) ml[k] = LD128(mlo, ((kb + k) & 3) * 512); } while (0)
  {
    v4u pf[16];
    f32x4 acc[3][2] = {};
    v4u ov[3];
#define OSTORE(et) do { const f32x4 v0 = acc[et][0], v1 = acc[et][1]; \
      auto r0 = __builtin_amdgcn_permlane16_swap(cvtpk(v0[0], v0[1]), cvtpk(v1[0], v1[1]), false, false); \
      auto r1 = __builtin_amdgcn_permlane16_swap(cvtpk(v0[2], v0[3]), cvtpk(v1[2], v1[3]), false, false); \
      ov[et] = v4u{r0[0], r1[0], r0[1], r1[1]}; ST128S(ov[et], oso, (et) * 4096); \
      if (own) LDS128(lds + 16384 + ((w & 1) * 3 + (et)) * 1024, ((g & 1) * 32 + (g >> 1) * 16 + n16) * 16) = ov[et]; } while (0)
#define ORECORD(et) RST(wsum(sum4(0u, ov[et])), roo + (et) * 512)
#pragma unroll
    for (int i = 0; i < 8; ++i) pf[i] = LDS128(pl, i * 1024);
    SB();
#pragma unroll
    for (int i = 0; i < 16; ++i) {
      if (i + 8 < 16) pf[i + 8] = LDS128(pl, (i + 8) * 1024);
      if ((i & 1) && NV2 + (i >> 1) < 24) VLOAD(NV2 + (i >> 1));
      acc[0][i & 1] = mfma(vf[i >> 1][0], pf[i], acc[0][i & 1]);
      if (i >= NH2) hv0 = opq(sum4(hv0, VF(i)));
      SB();
    }
    if (w == 0) {
      const v4u ones = {0x3f803f80u, 0x3f803f80u, 0x3f803f80u, 0x3f803f80u};
      f32x4 ls[2] = {};
#pragma unroll
      for (int i = 0; i < 16; ++i) ls[i & 1] = mfma(ones, pf[i], ls[i & 1]);
      ST128((v4u{__float_as_uint(g ? m[1] : m[0]), tag, __float_as_uint(g ? ls[1][0] : ls[0][0]), tag}), mso);
    }
    if (UNLIKELY(wsum(hv0, lane < 16 ? vr[0] : 0u) + 16u * tag != 0u)) {
      RETRY(3); CBAR();
#pragma unroll
      for (int i = 0; i < 16; ++i) VLOAD(i);
      acc[0][0] = acc[0][1] = f32x4{0.f, 0.f, 0.f, 0.f};
#pragma unroll
      for (int i = 0; i < 16; ++i) acc[0][i & 1] = mfma(vf[i >> 1][0], pf[i], acc[0][i & 1]);
    }
#pragma unroll
    for (int i = 0; i < 16; ++i) {
      acc[1][i & 1] = mfma(vf[i >> 1][1], pf[i], acc[1][i & 1]);
      if (i == 0) OSTORE(0);
      if (i == 6) ORECORD(0);
      if ((i & 3) == 3) SB();
    }
#pragma unroll
    for (int i = 0; i < 16; ++i) {
      acc[2][i & 1] = mfma(vf[i >> 1][2], pf[i], acc[2][i & 1]);
      if (i == 0) OSTORE(1);
      if (i == 6) ORECORD(1);
      if (i == MLP) ML_LOADS();
      if ((i & 3) == 3) SB();
    }
    STAMP(16);
    OSTORE(2);
    ORECORD(2);
  }
  STAMP(17);
  BAR();
  if (w < 6) {
    v4u pp[4]; unsigned orr;
#define MLOK() __all(((ml[0][1] ^ tag) | (ml[1][1] ^ tag) | (ml[2][1] ^ tag) | (ml[3][1] ^ tag) | (ml[0][3] ^ tag) | (ml[1][3] ^ tag) | (ml[2][3] ^ tag) | (ml[3][3] ^ tag)) == 0u)
#define HOP3_LOADS() do { _Pragma("unroll") for (int k = 1; k < 4; ++k) pp[k] = LD128(l16, s_ol + ((kb + k) & 3) * 1024); orr = LD32(rlo); } while (0)
#define OBAD() (wsum(sum4(sum4(sum4(-orr, pp[1]), pp[2]), pp[3])) + 3u * tag != 0u)
    pp[0] = LDS128(pl, 16384 + w * 1024);
    SB();
    if (DLY3) __builtin_amdgcn_s_sleep(DLY3);
    if (UNLIKELY(!MLOK())) {
      unsigned it = 0;
      RETRY(6); WAITREC(rlo);
      do { CBAR(); ML_LOADS(); } while (!MLOK() && ++it < SPIN_CAP);
    }
    HOP3_LOADS();
    SB();
    STAMP(18);
    const float mm = max2(max2(__uint_as_float(ml[0][0]), __uint_as_float(ml[1][0])), max2(__uint_as_float(ml[2][0]), __uint_as_float(ml[3][0])));
    float wk[4], o[8];
#pragma unroll
    for (int k = 0; k < 4; ++k) wk[k] = __builtin_amdgcn_exp2f(__uint_as_float(ml[k][0]) - mm);
    const float inv = __builtin_amdgcn_rcpf((wk[0] * __uint_as_float(ml[0][2]) + wk[1] * __uint_as_float(ml[1][2])) + (wk[2] * __uint_as_float(ml[2][2]) + wk[3] * __uint_as_float(ml[3][2])));
#pragma unroll
    for (int k = 0; k < 4; ++k) wk[k] *= inv;
#pragma unroll
    for (int e = 0; e < 4; ++e) { o[2 * e] = opq(fmaf(wk[0], bflo(pp[0][e]), bflo(xr[e]))); o[2 * e + 1] = opq(fmaf(wk[0], bfhi(pp[0][e]), bfhi(xr[e]))); }
    SB();
    if (UNLIKELY(OBAD())) {
      unsigned it = 0;
      do { RETRY(5); WAITREC(rlo); HOP3_LOADS(); } while (OBAD() && ++it < SPIN_CAP);
    }
    STAMP(1);
    v4u ov;
#pragma unroll
    for (int e = 0; e < 4; ++e) {
#pragma unroll
      for (int k = 1; k < 4; ++k) { o[2 * e] = fmaf(wk[k], bflo(pp[k][e]), o[2 * e]); o[2 * e + 1] = fmaf(wk[k], bfhi(pp[k][e]), o[2 * e + 1]); }
      ov[e] = cvtpk(o[2 * e], o[2 * e + 1]);
    }
    *(v4u*)((unsigned char*)out + ooff) = ov;
  } else if (UNLIKELY(bid == 255 && w == 7)) {
#pragma unroll 1
    for (unsigned it = 0; it < SPIN_CAP; ++it) {
      CBAR();
      unsigned bad = 0;
#pragma unroll
      for (int k = 0; k < 4; ++k) bad |= LD32(O_CL + (k * 64 + lane) * 128) ^ tag;
      if (__all(bad == 0)) break;
    }
    if (lane == 0) __hip_atomic_store((gu32*)(ws + O_EPOCH), tag, __ATOMIC_RELAXED, __HIP_MEMORY_SCOPE_AGENT);
  }
  STAMP(19);
  STFLUSH();
}

extern "C" void kernel_launch(void* const* d_in, const int* in_sizes, int n_in, void* d_out, int out_size, void* d_ws, size_t ws_size,
                              hipStream_t stream) {
  (void)hipFuncSetAttribute((const void*)dit_kernel, hipFuncAttributeMaxDynamicSharedMemorySize, L_TOTAL);
  dit_kernel<<<256, 512, L_TOTAL, stream>>>((const u16*)d_in[0], (const u16*)d_in[1], (const u16*)d_in[2], (const u16*)d_in[3],
                                            (const u16*)d_in[4], (unsigned char*)d_ws, (u16*)d_out);
}
```

```cpp
#include <hip/hip_runtime.h>
#include <hip/hip_bf16.h>

typedef short bf16x8 __attribute__((ext_vector_type(8)));
typedef short s16x4 __attribute__((ext_vector_type(4)));
typedef float f32x4 __attribute__((ext_vector_type(4)));
typedef float f32x2 __attribute__((ext_vector_type(2)));
typedef unsigned v4u __attribute__((ext_vector_type(4)));
typedef unsigned v2u __attribute__((ext_vector_type(2)));
typedef __bf16 bf2 __attribute__((ext_vector_type(2)));
typedef unsigned short u16;
#define DI __device__ __forceinline__
#define LAS __attribute__((address_space(3)))
#define GAS __attribute__((address_space(1)))
typedef GAS unsigned gu32;
typedef LAS unsigned char* lp;

constexpr int DLY1 = 6, DLY2 = 5, DLY3 = 4;
constexpr int PD = 4;
constexpr int E1 = 5, XE = 14, XS0 = 2, NX1 = 20, XL = 2;
constexpr int NVS = 12, NH2 = 12, MLP = 12;
constexpr int CODEPF = 15360;

constexpr int O_EPOCH = 0;
constexpr int O_MOD = 4096;
constexpr int O_CL = 16384;
constexpr int O_MT = 131072;
constexpr int O_NF = O_MT + 589824;
constexpr int O_QF = O_NF + 1572864;
constexpr int O_VF = O_QF + 1572864;
constexpr int O_OF = O_VF + 1572864;
constexpr int O_ML = O_OF + 6291456;
constexpr int O_RM = O_ML + 131072;
constexpr int O_RN = O_RM + 1152 * 128;
constexpr int O_RQ = O_RN + 512 * 128;
constexpr int O_RV = O_RQ + 1536 * 128;
constexpr int O_RO = O_RV + 1536 * 128;
constexpr int WS_BYTES = O_RO + 6144 * 128;
constexpr int OOB = 0x40000000;

constexpr int L_P0 = 0;
constexpr int L_MS = 61440, L_QS = L_MS + 24576, L_CS = L_QS + 24576, L_MX = L_CS + 512, L_BN = L_MX + 1024;
constexpr int L_SCR = L_BN + 6144;
constexpr int L_TOTAL = L_SCR + 256;

static_assert(NH2 <= NVS && NVS <= 16, "");
static_assert(E1 <= 12 && XE >= 12 && XE + 3 * XS0 <= NX1 && NX1 <= 24 && XL >= 2, "");
constexpr int AUX_SC1 = 16;
constexpr unsigned SPIN_CAP = 1u << 18;

DI float bflo(unsigned v) { return __uint_as_float(v << 16); }
DI float bfhi(unsigned v) { return __uint_as_float(v & 0xffff0000u); }
DI unsigned cvtpk(float lo, float hi) {
  f32x2 x = {lo, hi};
  return __builtin_bit_cast(unsigned, __builtin_convertvector(x, bf2));
}
DI float bfround(float x) { return bflo(cvtpk(x, 0.f)); }
DI unsigned pkhi(unsigned lo, unsigned hi) { return __builtin_amdgcn_perm(hi, lo, 0x07060302u); }
DI unsigned tagged(float v, unsigned tag16) { return (cvtpk(0.f, v) & 0xffff0000u) | tag16; }
DI unsigned tchk(unsigned a, v4u v, unsigned tag) { return a | (v[0] ^ tag) | (v[1] ^ tag) | (v[2] ^ tag) | (v[3] ^ tag); }
typedef unsigned short u16x2 __attribute__((ext_vector_type(2)));
DI unsigned sum2(unsigned a, unsigned x, unsigned y) { return __builtin_amdgcn_udot2(__builtin_bit_cast(u16x2, x), __builtin_bit_cast(u16x2, y), a, false); }
DI v2u sum4(v2u a, v4u v) { return v2u{sum2(a[0], v[0], v[1]), sum2(a[1], v[2], v[3])}; }
DI v2u sum4(unsigned a, v4u v) { return sum4(v2u{a, 0u}, v); }
DI unsigned scan63(unsigned v) {
  v += __builtin_amdgcn_update_dpp(0u, v, 0x111, 0xf, 0xf, true);
  v += __builtin_amdgcn_update_dpp(0u, v, 0x112, 0xf, 0xf, true);
  v += __builtin_amdgcn_update_dpp(0u, v, 0x114, 0xf, 0xf, true);
  v += __builtin_amdgcn_update_dpp(0u, v, 0x118, 0xf, 0xf, true);
  v += __builtin_amdgcn_update_dpp(0u, v, 0x142, 0xa, 0xf, true);
  v += __builtin_amdgcn_update_dpp(0u, v, 0x143, 0xc, 0xf, true);
  return v;
}
DI float sum_g(float v);
DI float qsumf(float v) {
#define FSTEP(ctl) v += __uint_as_float(__builtin_amdgcn_update_dpp(0u, __float_as_uint(v), ctl, 0xf, 0xf, true))
  FSTEP(0x128); FSTEP(0x124);
#undef FSTEP
  return sum_g(v);
}
DI unsigned wsum(unsigned v) { return __builtin_amdgcn_readlane(scan63(v), 63); }
DI unsigned wsum(v2u v, unsigned minus = 0u) { return wsum(v[0] + v[1] - minus); }
DI float max3(float a, float b, float c) { float r; asm("v_max3_f32 %0, %1, %2, %3" : "=v"(r) : "v"(a), "v"(b), "v"(c)); return r; }
DI float max2(float a, float b) { float r; asm("v_max_f32 %0, %1, %2" : "=v"(r) : "v"(a), "v"(b)); return r; }
DI f32x4 mfma(v4u a, v4u b, f32x4 c) {
  return __builtin_amdgcn_mfma_f32_16x16x32_bf16(__builtin_bit_cast(bf16x8, a), __builtin_bit_cast(bf16x8, b), c, 0, 0, 0);
}
DI float sum_g(float v) {
  auto r = __builtin_amdgcn_permlane16_swap(__float_as_uint(v), __float_as_uint(v), false, false);
  v = __uint_as_float(r[0]) + __uint_as_float(r[1]);
  auto q = __builtin_amdgcn_permlane32_swap(__float_as_uint(v), __float_as_uint(v), false, false);
  return __uint_as_float(q[0]) + __uint_as_float(q[1]);
}
DI float max_g(float v) {
  auto r = __builtin_amdgcn_permlane16_swap(__float_as_uint(v), __float_as_uint(v), false, false);
  v = max2(__uint_as_float(r[0]), __uint_as_float(r[1]));
  auto q = __builtin_amdgcn_permlane32_swap(__float_as_uint(v), __float_as_uint(v), false, false);
  return max2(__uint_as_float(q[0]), __uint_as_float(q[1]));
}
DI int sopq(int v) { v = __builtin_amdgcn_readfirstlane(v); asm volatile("" : "+s"(v)); return v; }
template <typename T> DI T opq(T v) { asm volatile("" : "+v"(v)); return v; }
#define BAR() do { asm volatile("s_waitcnt lgkmcnt(0)" ::: "memory"); __builtin_amdgcn_s_barrier(); asm volatile("" ::: "memory"); } while (0)
#define VMWAIT(n) asm volatile("s_waitcnt vmcnt(%0)" :: "i"(n) : "memory")
#define SB() __builtin_amdgcn_sched_barrier(0)
#define CBAR() asm volatile("" ::: "memory")
#define LD128(off, so) __builtin_amdgcn_raw_buffer_load_b128(rws, (off), (so), AUX_SC1)
#define LD64(off) __builtin_amdgcn_raw_buffer_load_b64(rws, (off), 0, AUX_SC1)
#define LD32(off) __builtin_amdgcn_raw_buffer_load_b32(rws, (off), 0, AUX_SC1)
#define ST128(v, off) __builtin_amdgcn_raw_buffer_store_b128((v), rws, (off), 0, AUX_SC1)
#define ST128S(v, off, so) __builtin_amdgcn_raw_buffer_store_b128((v), rws, (off), (so), AUX_SC1)
#define ST32(v, off) __builtin_amdgcn_raw_buffer_store_b32((v), rws, (off), 0, AUX_SC1)
#define RST(sum, off) ST128((v4u{(sum) + tag, tag, tag, tag}), (off))
#define RECOK(v, off) __all((off) >= OOB || (v) == tag)
#define WAITREC(off) do { unsigned w_ = 0; CBAR(); while (!RECOK(LD32((off) + 4), (off)) && ++w_ < SPIN_CAP) { __builtin_amdgcn_s_sleep(2); CBAR(); } CBAR(); } while (0)
#define LDS128(p, off) (*(LAS v4u*)((p) + (off)))
#define LDSF4(p, off) (*(LAS f32x4*)((p) + (off)))
DI void dma(unsigned ldsaddr, int voff, __amdgpu_buffer_rsrc_t rs, int soff) {
  asm volatile("s_mov_b32 m0, %0\n\ts_nop 0\n\tbuffer_load_dwordx4 %1, %2, %3 offen lds" :: "s"(ldsaddr), "v"(voff), "s"(rs), "s"(soff) : "memory");
}

DI void touch(unsigned ldsaddr, int voff, __amdgpu_buffer_rsrc_t rs) {
  asm volatile("s_mov_b32 m0, %0\n\ts_nop 0\n\tbuffer_load_dword %1, %2, 0 offen lds" :: "s"(ldsaddr), "v"(voff), "s"(rs) : "memory");
}
#define UNLIKELY(c) __builtin_expect(!!(c), 0)

__global__ __launch_bounds__(512, 2) void dit_kernel(const u16* __restrict__ x, const u16* __restrict__ c, const u16* __restrict__ wqkv,
                                                     const u16* __restrict__ wo, const u16* __restrict__ wmod, unsigned char* ws,
                                                     u16* __restrict__ out) {
  extern __shared__ __attribute__((aligned(16))) unsigned char lds_[];
  lp lds = (lp)lds_;
  const unsigned ldsb = (unsigned)(size_t)lds;
  const unsigned long long pc0 = __builtin_amdgcn_s_getpc();
  const int tid = threadIdx.x, lane = tid & 63, w = __builtin_amdgcn_readfirstlane(tid >> 6);
  const int n16 = lane & 15, g = lane >> 4;
  const int bid = blockIdx.x, xg = bid & 7, r = bid >> 3;
  const int b = xg >> 2, ql = r & 7, kb = r >> 3;
  const int qt = b * 32 + (xg & 3) * 8 + ql;
  const bool act1 = ql < 6;
  const int cb = (xg & 3) * 6 + (act1 ? ql : ql - 2);
  const __amdgpu_buffer_rsrc_t rws = __builtin_amdgcn_make_buffer_rsrc(ws, 0, WS_BYTES, 0x00020000);
  const __amdgpu_buffer_rsrc_t rx = __builtin_amdgcn_make_buffer_rsrc((void*)x, 0, 2048 * 768, 0x00020000);
  const int dch = 16 * ((lane & 3) ^ ((lane & 32) ? 3 : 0));
  const int rslot = (4 * n16 + (g ^ ((n16 & 8) ? 3 : 0))) * 16;
  const int l63 = lane < 8 ? lane * 16 : OOB;

  const int mu = (((r - 24) >> 1) * 8 + xg) * 2 + (r & 1);
  const int role = r < 12 ? 0 : ((r >= 24 && mu < 36) ? 2 : 1);
  const int dead0 = (r >= 24 && mu >= 36) ? OOB : 0;
  const int ru = r < 12 ? r : (r < 24 ? r - 12 : r - 24);
  const int u12 = 6 * (xg & 1) + ru % 6, u8 = 2 * (xg >> 1) + ru / 6;
  unsigned ep;
  asm volatile("s_nop 4\n\tbuffer_load_dword %0, off, %1, 0 sc1" : "=v"(ep) : "s"(rws) : "memory");
  if (w < 6) {
    unsigned cv = 0;
    if (role == 2) cv = ((const unsigned*)c)[tid];
    if (role < 2) {
      const __amdgpu_buffer_rsrc_t rq = __builtin_amdgcn_make_buffer_rsrc((void*)wqkv, 0, 384 * 2304, 0x00020000);
      const int v = (lane >> 2) * 2304 + dch;
      const int s3 = 48 * u8 * 2304 + (role ? 1536 : 768) + 128 * w;
#pragma unroll
      for (int t = 0; t < 3; ++t) {
        dma(ldsb + L_P0 + (t * 12 + 2 * w) * 1024, v, rq, s3 + 16 * t * 2304);
        dma(ldsb + L_P0 + (t * 12 + 2 * w + 1) * 1024, v, rq, s3 + 16 * t * 2304 + 64);
      }
      if (role == 0) {
        const int s2 = 32 * u12 * 2304 + 128 * w;
#pragma unroll
        for (int t = 0; t < 2; ++t) {
          dma(ldsb + L_P0 + ((3 + t) * 12 + 2 * w) * 1024, v, rq, s2 + 16 * t * 2304);
          dma(ldsb + L_P0 + ((3 + t) * 12 + 2 * w + 1) * 1024, v, rq, s2 + 16 * t * 2304 + 64);
        }
      }
    }
    if (role == 1 || role == 2) {
      const int ld = role == 1 ? 768 : 4608;
      const __amdgpu_buffer_rsrc_t rc = __builtin_amdgcn_make_buffer_rsrc((void*)(role == 1 ? wo : wmod), 0, 384 * ld, 0x00020000);
      const int v = (lane >> 2) * ld + dch;
#pragma unroll
      for (int j = 0; j < 4; ++j) dma(ldsb + L_P0 + (36 + 4 * w + j) * 1024, v, rc, (role == 1 ? u12 : mu) * 64 + (4 * w + j) * 16 * ld);
    }
    if (role == 2) {
      const float f0 = bflo(cv), f1 = bfhi(cv);
      const int bb = tid >= 192, i2 = 2 * (tid - 192 * bb);
      *(LAS unsigned*)(lds + L_P0 + (i2 >> 5) * 1024 + (bb * 4 + ((i2 >> 3) & 3)) * 16 + (i2 & 7) * 2) =
          cvtpk(f0 * __builtin_amdgcn_rcpf(1.f + __expf(-f0)), f1 * __builtin_amdgcn_rcpf(1.f + __expf(-f1)));
    }
    VMWAIT(0);
    __builtin_amdgcn_s_waitcnt(0x0F70);
  } else {
    __builtin_amdgcn_s_setprio(3);
    v4u xs[3];
    asm volatile("buffer_load_dwordx4 %0, %3, %4, 0 offen\n\tbuffer_load_dwordx4 %1, %3, %4, 0 offen offset:256\n\tbuffer_load_dwordx4 %2, %3, %4, 0 offen offset:512"
                 : "=&v"(xs[0]), "=&v"(xs[1]), "=&v"(xs[2]) : "v"((32 * (bid >> 2) + 16 * (bid & 1) + 8 * (w & 1) + 2 * (bid & 2) + (lane & 3)) * 768 + (lane >> 2) * 16), "s"(rx) : "memory");
    {
      const unsigned scr = ldsb + L_SCR;
      const int dead = lane < 36 ? 0 : OOB;
      const __amdgpu_buffer_rsrc_t rq = __builtin_amdgcn_make_buffer_rsrc((void*)wqkv, 0, 384 * 2304, 0x00020000);
      touch(scr, dead + (w == 6 ? (96 * (xg >> 1) + 3 * r + lane / 12) * 2304 + 768 + (lane % 12) * 128 : (192 * (xg & 1) + 6 * r + lane / 6) * 2304 + (lane % 6) * 128), rq);
      const __amdgpu_buffer_rsrc_t r2 = __builtin_amdgcn_make_buffer_rsrc(w == 6 ? (void*)wo : (void*)(pc0 & ~127ull), 0, w == 6 ? 384 * 768 : CODEPF, 0x00020000);
      touch(scr, w == 6 ? dead + (12 * r + lane / 3) * 768 + 384 * (xg & 1) + (lane % 3) * 128 : (lane < 5 ? (r * 5 + lane) * 128 : OOB), r2);
    }
    if (w == 7) {
      const __amdgpu_buffer_rsrc_t r3 = __builtin_amdgcn_make_buffer_rsrc((void*)wmod, 0, 384 * 4608, 0x00020000);
      touch(ldsb + L_SCR, (lane < (xg < 2 ? 36 : 24) ? 0 : OOB) + (12 * r + lane % 12) * 4608 + ((lane / 12) * 8 + xg) * 128, r3);
    }
    else touch(ldsb + L_SCR, OOB, rx);
    asm volatile("s_waitcnt vmcnt(3)" : "+v"(xs[0]), "+v"(xs[1]), "+v"(xs[2]) :: "memory");
    float f[24], sm = 0.f, q = 0.f;
#pragma unroll
    for (int i = 0; i < 12; ++i) { f[2 * i] = bflo(xs[i >> 2][i & 3]); f[2 * i + 1] = bfhi(xs[i >> 2][i & 3]); sm += f[2 * i] + f[2 * i + 1]; }
    sm = qsumf(sm) * (1.f / 384.f);
#pragma unroll
    for (int i = 0; i < 24; ++i) { f[i] -= sm; q = fmaf(f[i], f[i], q); }
    const float rs = __builtin_amdgcn_rsqf(qsumf(q) * (1.f / 384.f) + 1e-5f);
#pragma unroll
    for (int i = 0; i < 3; ++i)
      LDS128(lds, L_BN + ((lane >> 2) + 16 * i) * 128 + ((w & 1) * 4 + (lane & 3)) * 16) =
          v4u{cvtpk(f[8 * i] * rs, f[8 * i + 1] * rs), cvtpk(f[8 * i + 2] * rs, f[8 * i + 3] * rs), cvtpk(f[8 * i + 4] * rs, f[8 * i + 5] * rs), cvtpk(f[8 * i + 6] * rs, f[8 * i + 7] * rs)};
  }
  asm volatile("" : "+v"(ep));
  const unsigned tag = __builtin_amdgcn_readfirstlane(ep) + 1u;
  BAR();
  if (w >= 6) {
    v4u nv[3];
#pragma unroll
    for (int i = 0; i < 3; ++i) nv[i] = LDS128(lds, L_BN + (w & 1) * 3072 + i * 1024 + lane * 16);
    const int no = O_NF + (bid >> 1) * 12288 + ((w & 1) * 24 + (lane >> 3)) * 256 + (bid & 1) * 128 + (lane & 7) * 16;
#pragma unroll
    for (int i = 0; i < 3; ++i) ST128(nv[i], no + i * 2048);
    const unsigned ns = wsum(sum4(sum4(sum4(0u, nv[0]), nv[1]), nv[2]));
    VMWAIT(0);
    RST(ns, l63 + O_RN + (bid * 2 + (w & 1)) * 128);
    __builtin_amdgcn_s_setprio(0);
  }
  const int rg = b * 32 + kb * 8 + w;
  const int nro = lane < 8 ? O_RN + (rg * 8 + lane) * 128 : OOB;
  if (w < (role == 2 ? 2 : 6)) {
    const int t3 = w >> 1, t2 = w & 1;
    f32x4 acc = {};
    v4u a[12], bq[12];
    if (role == 0) {
      lp ap = opq(lds + L_P0 + (3 + t2) * 12288 + rslot), bp = opq(lds + L_P0 + t3 * 12288 + rslot);
#pragma unroll
      for (int k = 0; k < 12; ++k) {
        a[k] = LDS128(ap, k * 1024); bq[k] = LDS128(bp, k * 1024);
        if (k >= 5) acc = mfma(a[k - 5], bq[k - 5], acc);
        SB();
      }
    } else {
      lp ap = opq(lds + L_P0 + (role == 2 ? ((n16 & 1) * 4 + g) * 16 : t3 * 12288 + rslot));
      lp tb = opq(lds + L_P0 + 36864 + (8 * g + (n16 >> 2)) * 64 + 16 * ((2 * t2 + ((n16 >> 1) & 1)) ^ ((g & 1) ? 3 : 0)) + 8 * (n16 & 1));
#pragma unroll
      for (int k = 0; k < 12; ++k) {
        a[k] = LDS128(ap, k * 1024);
        bq[k] = __builtin_bit_cast(v4u, __builtin_shufflevector(__builtin_amdgcn_ds_read_tr16_b64_v4i16((LAS s16x4*)(tb + k * 2048)),
                                                                __builtin_amdgcn_ds_read_tr16_b64_v4i16((LAS s16x4*)(tb + k * 2048 + 256)), 0, 1, 2, 3, 4, 5, 6, 7));
        if (k >= 5) acc = mfma(a[k - 5], bq[k - 5], acc);
        SB();
      }
    }
#pragma unroll
    for (int k = 7; k < 12; ++k) acc = mfma(a[k], bq[k], acc);
    if (role != 2) {
      const int blk = role * 576 + (role == 0 ? (3 * u8 + t3) * 24 + 2 * u12 + t2 : (2 * u12 + t2) * 24 + 3 * u8 + t3);
      const unsigned o0 = cvtpk(acc[0], acc[1]), o1 = cvtpk(acc[2], acc[3]);
      auto r0 = __builtin_amdgcn_permlane16_swap(o0, o0, false, false);
      auto r1 = __builtin_amdgcn_permlane16_swap(o1, o1, false, false);
      ST128((v4u{r0[0], r1[0], r0[1], r1[1]}), (((g & 1) ? OOB : 0) | dead0) + O_MT + blk * 512 + ((g >> 1) * 16 + n16) * 16);
      RST(wsum(sum2(0u, o0, o1)), (l63 | dead0) + O_RM + blk * 128);
    } else if (g < 2) {
      float v = bfround(g ? acc[1] : acc[0]);
      if (mu >= 12 && mu < 24) v += 1.f;
      ST32(tagged(v, tag & 0xffffu), O_MOD + (2 * mu + t2) * 128 + lane * 4);
    }
  }
  __builtin_amdgcn_s_barrier();
  const int l16 = opq(lane * 16);
  const int s_n = O_NF + rg * 24576;
  const int l16b = opq(lane * 16 + 12288);
  v4u xf[2][12];
#define XF(i) xf[(i) & 1][(i) >> 1]
#define XLOAD(i) XF(i) = LD128(((i) & 1) ? l16b : l16, s_n + ((i) >> 1) * 1024)
  __builtin_amdgcn_s_sleep(DLY1);
#pragma unroll
  for (int i = 0; i < E1; ++i) XLOAD(i);
  SB();
  lp fl = opq(lds + L_MS + lane * 16);
  v4u qf[3]; unsigned qr, vsum; int rvo;
  const int qro = opq(lane < 3 ? O_RQ + (qt * 24 + 3 * w + lane) * 128 : OOB);
  const int s_ql = sopq(O_QF + (qt * 24 + 3 * w) * 1024);
#define HOP2_LOADS() do { _Pragma("unroll") for (int i = 0; i < 3; ++i) qf[i] = LD128(l16 + i * 1024, s_ql); qr = LD32(qro); } while (0)
  {
    const int s0 = 3 * (w & 3);
    const int mc4 = lane < 24 ? 96 + 8 * s0 + lane : (lane < 48 ? 8 * s0 + lane - 24 : (w < 4 ? 96 : 192) + 4 * cb + (lane & 3));
    const int mo = opq(O_MOD + (mc4 >> 2) * 128 + b * 64 + (mc4 & 3) * 16);
    const int fb = (w >> 2) * 576 + cb * 24 + 2 * s0;
    const int ro = opq(lane < 6 ? O_RM + (fb + lane) * 128 : OOB);
    const int s_f = sopq(O_MT + fb * 512);
    const int blk1 = rg * 24 + cb, dead1 = act1 ? 0 : OOB;
    const int qso = opq(dead1 + O_QF + blk1 * 1024 + ((g & 1) * 32 + (g >> 1) * 16 + n16) * 16);
    const int vso = opq(dead1 + O_VF + blk1 * 1024 + lane * 16);
    const int rso = opq((dead1 | l63) + O_RQ + blk1 * 128);
    rvo = opq((dead1 | l63) + O_RV + (((b * 4 + kb) * 24 + cb) * 8 + w) * 128);
    lp cg = opq(lds + L_CS + g * 16), cn = opq(lds + L_CS + 256 + n16 * 4), csw = opq(lds + L_CS + w * 64 + n16 * 4);
    lp bn = lds + L_BN + w * 768, bg = opq(bn + g * 32), msw = opq(fl + 3 * w * 1024);
    SB();
    v4u mf[3], mv4; unsigned mr;
#define HOP1_LOADS() do { _Pragma("unroll") for (int i = 0; i < 3; ++i) mf[i] = LD128(l16 + i * 1024, s_f); mr = LD32(ro); } while (0)
    mv4 = LD128(mo, 0);
    HOP1_LOADS();
    v2u nf = LD64(nro);
    SB();
    __builtin_amdgcn_s_barrier();
#pragma unroll
    for (int i = E1; i < 12; ++i) XLOAD(i);
    SB();
    v2u hx = {0u, 0u};
#pragma unroll
    for (int i = 0; i < E1; ++i) hx = sum4(hx, XF(i));
    hx = opq(hx);
    f32x4 sa[3], sb[3]; v4u tf[3]; float oc_;
#define MODOK() __all((tchk(0u, mv4, tag) & 0xffffu) == 0u)
    if (UNLIKELY(!MODOK())) {
      unsigned it = 0;
      do { __builtin_amdgcn_s_sleep(3); CBAR(); mv4 = LD128(mo, 0); } while (!MODOK() && ++it < SPIN_CAP);
    }
    if (lane >= 24 && lane < 48) *(LAS v2u*)(bn + 512 + (lane - 24) * 8) = v2u{pkhi(mv4[0], mv4[1]), pkhi(mv4[2], mv4[3])};
    else LDS128(bn, (lane < 24 ? lane : (lane & 3) + 24) * 16) = mv4 & 0xffff0000u;
    CBAR();
#pragma unroll
    for (int k = 0; k < 3; ++k) { sa[k] = LDSF4(bg, 128 * k); sb[k] = LDSF4(bg, 128 * k + 16); tf[k] = LDS128(bn, 512 + 64 * k + 16 * g); }
    oc_ = *(LAS float*)(bn + 384 + n16 * 4);
    if (UNLIKELY(!RECOK(nf[1], nro))) { WAITREC(nro); nf = LD64(nro); }
    CBAR();
#pragma unroll
    for (int i = 12; i < XE; ++i) XLOAD(i);
    SB();
#define FBAD() (wsum(sum4(sum4(sum4(-mr, mf[0]), mf[1]), mf[2])) + 6u * tag != 0u)
    if (UNLIKELY(FBAD())) {
      unsigned it = 0;
      do { __builtin_amdgcn_s_sleep(3); CBAR(); HOP1_LOADS(); } while (FBAD() && ++it < SPIN_CAP);
    }
    if (w < 4) oc_ *= 0.05103103630798288f * 1.4426950408889634f;
    {
      f32x4 au = {};
#pragma unroll
      for (int i = 0; i < 3; ++i) {
#pragma unroll
        for (int x_ = 0; x_ < XS0; ++x_) XLOAD(XE + XS0 * i + x_);
        au = mfma(tf[i], mf[i], au);
        const f32x4 a_ = sa[i] * oc_, b_ = sb[i] * oc_;
        const v4u o = {cvtpk(bflo(mf[i][0]) * a_[0], bfhi(mf[i][0]) * a_[1]), cvtpk(bflo(mf[i][1]) * a_[2], bfhi(mf[i][1]) * a_[3]),
                       cvtpk(bflo(mf[i][2]) * b_[0], bfhi(mf[i][2]) * b_[1]), cvtpk(bflo(mf[i][3]) * b_[2], bfhi(mf[i][3]) * b_[3])};
        LDS128(msw, i * 1024) = o;
        SB();
      }
      *(LAS float*)(csw) = au[0] * oc_;
    }
#pragma unroll
    for (int i = XE + 3 * XS0; i < NX1; ++i) XLOAD(i);
#pragma unroll
    for (int i = E1; i < 12; ++i) hx = sum4(hx, XF(i));
    if (UNLIKELY(wsum(hx, (lane & 1) ? 0u : nf[0]) + 4u * tag != 0u)) {
      CBAR();
#pragma unroll
      for (int i = 0; i < 12; ++i) XLOAD(i);
    }
    __builtin_amdgcn_s_setprio(2);
    BAR();
    v4u a[24];
    f32x4 uu[4]; float zz[4], zs;
#define AOP(k) do { if ((k) + PD < ((k) < 12 ? 12 : 24)) a[(k) + PD] = LDS128(fl, ((k) + PD) * 1024); \
      if ((k) < 12) { aq[0] = mfma(a[k], xf[0][(k) % 12], aq[0]); aq[1] = mfma(a[k], xf[1][(k) % 12], aq[1]); } \
      else { av[0] = mfma(xf[0][(k) % 12], a[k], av[0]); av[1] = mfma(xf[1][(k) % 12], a[k], av[1]); } } while (0)
#pragma unroll
    for (int k = 0; k < PD; ++k) a[k] = LDS128(fl, k * 1024);
    SB();
    f32x4 aq[2] = {}, av[2] = {};
#pragma unroll
    for (int k = 0; k < 12; ++k) {
#pragma unroll
      for (int x_ = 0; x_ < XL; ++x_) if (NX1 + XL * k + x_ < 24) XLOAD(NX1 + XL * k + x_);
      if (k >= 2 && k < 6) uu[k - 2] = LDSF4(cg, (k - 2) * 64);
      AOP(k);
      if (k == 9) { uu[0] = uu[0] + uu[1]; uu[2] = uu[2] + uu[3]; }
      if (k == 10) uu[0] = uu[0] + uu[2];
      SB();
    }
    {
      const f32x4 v0 = aq[0] + uu[0], v1 = aq[1] + uu[0];
      auto r0 = __builtin_amdgcn_permlane16_swap(cvtpk(v0[0], v0[1]), cvtpk(v1[0], v1[1]), false, false);
      auto r1 = __builtin_amdgcn_permlane16_swap(cvtpk(v0[2], v0[3]), cvtpk(v1[2], v1[3]), false, false);
      const v4u ov = {r0[0], r1[0], r0[1], r1[1]};
      ST128(ov, qso);
      RST(wsum(sum4(0u, ov)), rso);
      __builtin_amdgcn_s_setprio(0);
#pragma unroll
      for (int k = 12; k < 12 + PD; ++k) a[k] = LDS128(fl, k * 1024);
      SB();
#pragma unroll
      for (int k = 12; k < 24; ++k) {
        if (k >= 14 && k < 18) zz[k - 14] = *(LAS float*)(cn + (k - 14) * 64);
        AOP(k);
        if (k == 21) zs = (zz[0] + zz[1]) + (zz[2] + zz[3]);
        SB();
      }
      const f32x4 w0 = av[0] + zs, w1 = av[1] + zs;
      const v4u o = {cvtpk(w0[0], w0[1]), cvtpk(w0[2], w0[3]), cvtpk(w1[0], w1[1]), cvtpk(w1[2], w1[3])};
      ST128(o, vso);
      vsum = wsum(sum4(0u, o));
      SB();
      __builtin_amdgcn_s_sleep(DLY2);
      HOP2_LOADS();
    }
    SB();
  }
  if (w == 7) ST128((v4u{tag, tag, tag, tag}), l63 + O_CL + bid * 128);
  const int vro = opq(lane < 24 ? O_RV + (((b * 4 + kb) * 8 + w) * 24 + lane) * 128 : OOB);
  const int mso = opq((w == 0 && g < 2) ? O_ML + ((qt * 4 + kb) * 32 + lane) * 16 : OOB);
  const int s_vl = sopq(O_VF + ((b * 32 + 8 * kb) * 24 + 3 * w) * 1024);
  const bool own = (w >> 1) == kb;
  const int oso = opq((own ? OOB : 0) + O_OF + ((qt * 24 + 3 * w) * 4 + kb) * 1024 + ((g & 1) * 32 + (g >> 1) * 16 + n16) * 16);
  const int roo = opq(((own ? OOB : 0) | l63) + O_RO + ((qt * 24 + 3 * w) * 4 + kb) * 128);
  lp mxr = opq(lds + L_MX + n16 * 32), mxw = opq(lds + L_MX + n16 * 32 + w * 4);
  lp plw = opq(lds + lane * 16 + w * 2048);
  lp pl = opq(lds + lane * 16);
  const int ooff = opq(((qt * 32 + 8 * (n16 >> 2) + 4 * (lane >> 5) + (n16 & 3)) * 384 + (6 * kb + w) * 16 + 8 * (g & 1)) * 2);
  const v4u xr = __builtin_amdgcn_raw_buffer_load_b128(rx, w < 6 ? ooff : OOB, 0, 0);
  const int mlo = opq(O_ML + (qt * 128 + (lane >> 5) * 16 + n16) * 16);
  const int s_ol = sopq(O_OF + (qt * 24 + 6 * kb + w) * 4096), rlo = opq((lane < 4 && lane != kb) ? O_RO + ((qt * 24 + 6 * kb + w) * 4 + lane) * 128 : OOB);
  {
    const unsigned nt_ = ~tag;
    ST128((v4u{nt_, nt_, nt_, nt_}), lane < 24 ? O_RO + ((qt * 24 + 3 * w + (lane >> 3)) * 4 + kb) * 128 + (lane & 7) * 16 : ((w == 0 && lane >= 32) ? O_ML + ((qt * 4 + kb) * 32 + lane - 32) * 16 : OOB));
  }
  v4u vf[8][3];
#define VF(i) vf[(i) & 7][(i) >> 3]
#define VLOAD(i) VF(i) = LD128(l16 + ((i) >> 3) * 1024, s_vl + ((i) & 7) * 24576)
  {
    lp qw = opq(lds + L_QS + (lane >> 5) * 12288 + 3 * w * 512 + (lane & 31) * 16);
    SB();
    SB();
#define QBAD() (wsum(sum4(sum4(sum4(-qr, qf[0]), qf[1]), qf[2])) + 3u * tag != 0u)
#pragma unroll
    for (int i = 0; i < 3; ++i) LDS128(qw, i * 512) = qf[i];
    if (UNLIKELY(QBAD())) {
      unsigned it = 0;
      do { __builtin_amdgcn_s_sleep(3); CBAR(); HOP2_LOADS(); } while (QBAD() && ++it < SPIN_CAP);
#pragma unroll
      for (int i = 0; i < 3; ++i) LDS128(qw, i * 512) = qf[i];
    }
  }
  VMWAIT(3);
  RST(vsum, rvo);
  BAR();
  float m[2]; v2u vr, hv0 = {0u, 0u};
  {
    v4u q[24];
#define QREAD(i) q[i] = LDS128(fl, 24576 + (((i) & 1) * 12 + ((i) >> 1)) * 1024)
#pragma unroll
    for (int i = 0; i < PD; ++i) QREAD(i);
    SB();
    f32x4 acc[2][2] = {};
#pragma unroll
    for (int i = 0; i < 24; ++i) {
      if (i + PD < 24) QREAD(i + PD);
      acc[i & 1][0] = mfma(xf[0][i >> 1], q[i], acc[i & 1][0]); acc[i & 1][1] = mfma(xf[1][i >> 1], q[i], acc[i & 1][1]);
      if ((i * NVS) / 24 != ((i + 1) * NVS) / 24) VLOAD((i * NVS) / 24);
      SB();
    }
    asm volatile("s_nop 7" : "+v"(acc[0][0]), "+v"(acc[0][1]), "+v"(acc[1][0]), "+v"(acc[1][1]));
#pragma unroll
    for (int u = 0; u < 2; ++u) {
      float mx = max3(max3(acc[u][0][0], acc[u][0][1], acc[u][0][2]), acc[u][0][3], acc[u][1][0]);
      mx = max_g(max2(max3(mx, acc[u][1][1], acc[u][1][2]), acc[u][1][3]));
      *(LAS float*)(mxw + u * 512) = mx;
    }
    BAR();
    f32x4 ma[2], mb[2];
#pragma unroll
    for (int u = 0; u < 2; ++u) { ma[u] = LDSF4(mxr, u * 512); mb[u] = LDSF4(mxr, u * 512 + 16); }
    vr = LD64(vro);
    SB();
#pragma unroll
    for (int u = 0; u < 2; ++u) {
      m[u] = max2(max3(max3(ma[u][0], ma[u][1], ma[u][2]), ma[u][3], mb[u][0]), max3(mb[u][1], mb[u][2], mb[u][3]));
      float p[8];
#pragma unroll
      for (int i = 0; i < 8; ++i) p[i] = __builtin_amdgcn_exp2f(acc[u][i >> 2][i & 3] - m[u]);
      LDS128(plw, u * 1024) = v4u{cvtpk(p[0], p[1]), cvtpk(p[2], p[3]), cvtpk(p[4], p[5]), cvtpk(p[6], p[7])};
      if (NVS + u < 16) VLOAD(NVS + u);
      SB();
    }
#pragma unroll
    for (int i = NVS + 2; i < 16; ++i) VLOAD(i);
  }
#pragma unroll
  for (int i = 0; i < NH2; ++i) hv0 = sum4(hv0, VF(i));
  hv0 = opq(hv0);
  if (UNLIKELY(!RECOK(vr[1], vro))) { WAITREC(vro); vr = LD64(vro); }
  CBAR();
  BAR();
  v4u ml[4];
#define ML_LOADS() do { _Pragma("unroll") for (int k = 0; k < 4; ++k) ml[k] = LD128(mlo, ((kb + k) & 3) * 512); } while (0)
  {
    v4u pf[16];
    f32x4 acc[3][2] = {};
    v4u ov[3];
#define OSTORE(et) do { const f32x4 v0 = acc[et][0], v1 = acc[et][1]; \
      auto r0 = __builtin_amdgcn_permlane16_swap(cvtpk(v0[0], v0[1]), cvtpk(v1[0], v1[1]), false, false); \
      auto r1 = __builtin_amdgcn_permlane16_swap(cvtpk(v0[2], v0[3]), cvtpk(v1[2], v1[3]), false, false); \
      ov[et] = v4u{r0[0], r1[0], r0[1], r1[1]}; ST128S(ov[et], oso, (et) * 4096); \
      if (own) LDS128(lds + 16384 + ((w & 1) * 3 + (et)) * 1024, ((g & 1) * 32 + (g >> 1) * 16 + n16) * 16) = ov[et]; } while (0)
#define ORECORD(et) RST(wsum(sum4(0u, ov[et])), roo + (et) * 512)
#pragma unroll
    for (int i = 0; i < 8; ++i) pf[i] = LDS128(pl, i * 1024);
    SB();
#pragma unroll
    for (int i = 0; i < 16; ++i) {
      if (i + 8 < 16) pf[i + 8] = LDS128(pl, (i + 8) * 1024);
      if ((i & 1) && 16 + (i >> 1) < 24) VLOAD(16 + (i >> 1));
      acc[0][i & 1] = mfma(vf[i >> 1][0], pf[i], acc[0][i & 1]);
      if (i >= NH2) hv0 = opq(sum4(hv0, VF(i)));
      SB();
    }
    if (w == 0) {
      const v4u ones = {0x3f803f80u, 0x3f803f80u, 0x3f803f80u, 0x3f803f80u};
      f32x4 ls[2] = {};
#pragma unroll
      for (int i = 0; i < 16; ++i) ls[i & 1] = mfma(ones, pf[i], ls[i & 1]);
      ST128((v4u{__float_as_uint(g ? m[1] : m[0]), tag, __float_as_uint(g ? ls[1][0] : ls[0][0]), tag}), mso);
    }
    if (UNLIKELY(wsum(hv0, lane < 16 ? vr[0] : 0u) + 16u * tag != 0u)) {
      CBAR();
#pragma unroll
      for (int i = 0; i < 16; ++i) VLOAD(i);
      acc[0][0] = acc[0][1] = f32x4{0.f, 0.f, 0.f, 0.f};
#pragma unroll
      for (int i = 0; i < 16; ++i) acc[0][i & 1] = mfma(vf[i >> 1][0], pf[i], acc[0][i & 1]);
    }
#pragma unroll
    for (int i = 0; i < 16; ++i) {
      acc[1][i & 1] = mfma(vf[i >> 1][1], pf[i], acc[1][i & 1]);
      if (i == 0) OSTORE(0);
      if (i == 6) ORECORD(0);
      if ((i & 3) == 3) SB();
    }
#pragma unroll
    for (int i = 0; i < 16; ++i) {
      acc[2][i & 1] = mfma(vf[i >> 1][2], pf[i], acc[2][i & 1]);
      if (i == 0) OSTORE(1);
      if (i == 6) ORECORD(1);
      if (i == MLP) ML_LOADS();
      if ((i & 3) == 3) SB();
    }
    OSTORE(2);
    ORECORD(2);
  }
  BAR();
  if (w < 6) {
    v4u pp[4]; unsigned orr;
#define MLOK() __all(((ml[0][1] ^ tag) | (ml[1][1] ^ tag) | (ml[2][1] ^ tag) | (ml[3][1] ^ tag) | (ml[0][3] ^ tag) | (ml[1][3] ^ tag) | (ml[2][3] ^ tag) | (ml[3][3] ^ tag)) == 0u)
#define HOP3_LOADS() do { _Pragma("unroll") for (int k = 1; k < 4; ++k) pp[k] = LD128(l16, s_ol + ((kb + k) & 3) * 1024); orr = LD32(rlo); } while (0)
#define OBAD() (wsum(sum4(sum4(sum4(-orr, pp[1]), pp[2]), pp[3])) + 3u * tag != 0u)
    pp[0] = LDS128(pl, 16384 + w * 1024);
    SB();
    __builtin_amdgcn_s_sleep(DLY3);
    if (UNLIKELY(!MLOK())) {
      unsigned it = 0;
      WAITREC(rlo);
      do { CBAR(); ML_LOADS(); } while (!MLOK() && ++it < SPIN_CAP);
    }
    HOP3_LOADS();
    SB();
    const float mm = max2(max2(__uint_as_float(ml[0][0]), __uint_as_float(ml[1][0])), max2(__uint_as_float(ml[2][0]), __uint_as_float(ml[3][0])));
    float wk[4], o[8];
#pragma unroll
    for (int k = 0; k < 4; ++k) wk[k] = __builtin_amdgcn_exp2f(__uint_as_float(ml[k][0]) - mm);
    const float inv = __builtin_amdgcn_rcpf((wk[0] * __uint_as_float(ml[0][2]) + wk[1] * __uint_as_float(ml[1][2])) + (wk[2] * __uint_as_float(ml[2][2]) + wk[3] * __uint_as_float(ml[3][2])));
#pragma unroll
    for (int k = 0; k < 4; ++k) wk[k] *= inv;
#pragma unroll
    for (int e = 0; e < 4; ++e) { o[2 * e] = opq(fmaf(wk[0], bflo(pp[0][e]), bflo(xr[e]))); o[2 * e + 1] = opq(fmaf(wk[0], bfhi(pp[0][e]), bfhi(xr[e]))); }
    SB();
    if (UNLIKELY(OBAD())) {
      unsigned it = 0;
      do { WAITREC(rlo); HOP3_LOADS(); } while (OBAD() && ++it < SPIN_CAP);
    }
    v4u ov;
#pragma unroll
    for (int e = 0; e < 4; ++e) {
#pragma unroll
      for (int k = 1; k < 4; ++k) { o[2 * e] = fmaf(wk[k], bflo(pp[k][e]), o[2 * e]); o[2 * e + 1] = fmaf(wk[k], bfhi(pp[k][e]), o[2 * e + 1]); }
      ov[e] = cvtpk(o[2 * e], o[2 * e + 1]);
    }
    *(v4u*)((unsigned char*)out + ooff) = ov;
  } else if (UNLIKELY(bid == 255 && w == 7)) {
#pragma unroll 1
    for (unsigned it = 0; it < SPIN_CAP; ++it) {
      CBAR();
      unsigned bad = 0;
#pragma unroll
      for (int k = 0; k < 4; ++k) bad |= LD32(O_CL + (k * 64 + lane) * 128) ^ tag;
      if (__all(bad == 0)) break;
    }
    if (lane == 0) __hip_atomic_store((gu32*)(ws + O_EPOCH), tag, __ATOMIC_RELAXED, __HIP_MEMORY_SCOPE_AGENT);
  }
}

extern "C" void kernel_launch(void* const* d_in, const int* in_sizes, int n_in, void* d_out, int out_size, void* d_ws, size_t ws_size,
                              hipStream_t stream) {
  (void)hipFuncSetAttribute((const void*)dit_kernel, hipFuncAttributeMaxDynamicSharedMemorySize, L_TOTAL);
  dit_kernel<<<256, 512, L_TOTAL, stream>>>((const u16*)d_in[0], (const u16*)d_in[1], (const u16*)d_in[2], (const u16*)d_in[3],
                                            (const u16*)d_in[4], (unsigned char*)d_ws, (u16*)d_out);
}
```
